```python
import torch

def _softmax(x, axis=-1):
    return torch.softmax(x, dim=axis)
def _gelu(x):
    return torch.nn.functional.gelu(x, approximate="tanh")
def _silu(x):
    return torch.nn.functional.silu(x)
def _sigmoid(x):
    return torch.sigmoid(x)
def _layernorm(x, axis=-1, eps=1e-5):
    m = x.mean(dim=axis, keepdim=True)
    v = x.var(dim=axis, keepdim=True, unbiased=False)
    return (x - m) / torch.sqrt(v + eps)
def _scatter_add(tgt, idx, src, axis=0):
    return tgt.clone().index_add_(axis, idx, src)
def _segment_sum(src, seg, n):
    out = torch.zeros((n,) + src.shape[1:], dtype=src.dtype)
    return out.index_add_(0, seg, src)
def _pad(x, pw):
    flat = tuple(v for pair in reversed(pw) for v in pair)
    return torch.nn.functional.pad(x, flat)

kernel_name = 'sequential_preLN__softmax_full_mha__geglu_ffn__prompt'

def setup_inputs():

    _w = (lambda a: (lambda t: t.to(torch.bfloat16) if t.is_floating_point() else t)(torch.as_tensor(a)))
    def _A_setup():
        B, S, D = 1, 2048, 512
        return {
            "x": _w((lambda *s: torch.randn(*s, dtype=torch.bfloat16) * 0.05)(B, S, D)),
            "wqkv": _w((lambda *s: torch.randn(*s, dtype=torch.bfloat16) * 0.05)(D, 3 * D)),
            "wo": _w((lambda *s: torch.randn(*s, dtype=torch.bfloat16) * 0.05)(D, D)),
        }
    _A_in = _A_setup()

    def _B_setup():
        B, S, D, F = 1, 2048, 512, 1376
        return {
            "x": _w((lambda *s: torch.randn(*s, dtype=torch.bfloat16) * 0.05)(B, S, D)),
            "w_gate": _w((lambda *s: torch.randn(*s, dtype=torch.bfloat16) * 0.05)(D, F)),
            "w_up": _w((lambda *s: torch.randn(*s, dtype=torch.bfloat16) * 0.05)(D, F)),
            "w_down": _w((lambda *s: torch.randn(*s, dtype=torch.bfloat16) * 0.05)(F, D)),
        }
    _B_in = _B_setup()

    return {
        "A_x": _A_in["x"],
        "A_wqkv": _A_in["wqkv"],
        "A_wo": _A_in["wo"],
        "B_x": _B_in["x"],
        "B_w_gate": _B_in["w_gate"],
        "B_w_up": _B_in["w_up"],
        "B_w_down": _B_in["w_down"],
    }

def reference(A_x, A_wqkv, A_wo, B_x, B_w_gate, B_w_up, B_w_down):
    def _A_ref(x, wqkv, wo):
        B, S, D = x.shape
        H = 32; Dh = D // H
        qkv = (lambda x, *d: x.permute(*d))((x @ wqkv).reshape(B, S, 3, H, Dh), 2, 0, 3, 1, 4)
        q, k, v = qkv[0], qkv[1], qkv[2]
        scale = Dh ** -0.5
        scores = torch.einsum("bhqd,bhkd->bhqk", q, k) * scale
        p = _softmax(scores, axis=-1)
        o = torch.einsum("bhqk,bhkd->bhqd", p, v)
        return (lambda x, *d: x.permute(*d))(o, 0, 2, 1, 3).reshape(B, S, D) @ wo

    def _B_ref(x, w_gate, w_up, w_down):
        return (_gelu(x @ w_gate) * (x @ w_up)) @ w_down

    x = A_x
    h = x + _A_ref(_layernorm(x, axis=-1), A_wqkv, A_wo)
    h2 = h + _B_ref(_layernorm(h, axis=-1), B_w_gate, B_w_up, B_w_down)
    return h2
```

```cpp
#include <hip/hip_runtime.h>
#include <hip/hip_bf16.h>
#include <stdint.h>

typedef __bf16 bf16x8 __attribute__((ext_vector_type(8)));
typedef __bf16 bf16x2 __attribute__((ext_vector_type(2)));
typedef short s16x4 __attribute__((ext_vector_type(4)));
typedef short s16x8 __attribute__((ext_vector_type(8)));
typedef float f32x16 __attribute__((ext_vector_type(16)));
typedef float f32x4 __attribute__((ext_vector_type(4)));
typedef float f32x2 __attribute__((ext_vector_type(2)));
typedef unsigned u32x4 __attribute__((ext_vector_type(4)));
typedef unsigned u32x2 __attribute__((ext_vector_type(2)));
#define LAS __attribute__((address_space(3)))
#define DI __device__ __forceinline__
typedef LAS const char* lds_cptr;
typedef LAS char* lds_ptr;

constexpr int T = 2048, DM = 512, NH = 32, FF = 1376, NQKV = 1536;
constexpr float LOG2E = 1.4426950408889634f;
constexpr float QSCALE = 0.25f * LOG2E;
constexpr float LN_EPS = 1e-5f;

constexpr size_t WS_QF = 0, WS_KF = 2u << 20, WS_VF = 4u << 20, WS_OF = 6u << 20, WS_HF = 8u << 20, WS_AF = 10u << 20,
                 WS_ST = 16u << 20, WS_CS = 17u << 20, WS_KM = (17u << 20) + 65536;

#define MFMA(a, b, c) __builtin_amdgcn_mfma_f32_32x32x16_bf16(a, b, c, 0, 0, 0)
#define WAIT_BAR(N) asm volatile("s_waitcnt vmcnt(" #N ") lgkmcnt(0)\n\ts_barrier" ::: "memory")

template <int N> DI void wait_bar() { asm volatile("s_waitcnt vmcnt(%0) lgkmcnt(0)\n\ts_barrier" ::"i"(N) : "memory"); }

DI void glds16(const void* sbase, unsigned voff, unsigned lds_dst) {
    unsigned keep;
    asm volatile("s_mov_b32 %0, m0\n\ts_mov_b32 m0, %3\n\ts_nop 0\n\tglobal_load_lds_dwordx4 %1, %2\n\ts_mov_b32 m0, %0"
                 : "=&s"(keep) : "v"(voff), "s"(sbase), "s"(lds_dst) : "memory");
}
DI u32x4 gld128(const void* sbase, unsigned voff) { u32x4 r; asm volatile("global_load_dwordx4 %0, %1, %2" : "=v"(r) : "v"(voff), "s"(sbase) : "memory"); return r; }
DI u32x2 gld64(const void* sbase, unsigned voff) { u32x2 r; asm volatile("global_load_dwordx2 %0, %1, %2" : "=v"(r) : "v"(voff), "s"(sbase) : "memory"); return r; }
DI unsigned gld32(const void* sbase, unsigned voff) { unsigned r; asm volatile("global_load_dword %0, %1, %2" : "=v"(r) : "v"(voff), "s"(sbase) : "memory"); return r; }
#define VWAIT(N) asm volatile("s_waitcnt vmcnt(" #N ")" ::: "memory")
#define PIN(x) asm volatile("" : "+v"(x))
DI unsigned rfl(unsigned x) { return __builtin_amdgcn_readfirstlane(x); }
DI unsigned cvtpk(float lo, float hi) {
    f32x2 v = {lo, hi};
    bf16x2 b = __builtin_convertvector(v, bf16x2);
    return __builtin_bit_cast(unsigned, b);
}
DI float bflo(unsigned u) { return __uint_as_float(u << 16); }
DI float bfhi(unsigned u) { return __uint_as_float(u & 0xffff0000u); }
DI bf16x8 lds_b128(lds_cptr p) { return *(LAS const bf16x8*)p; }
typedef short v4i16_t __attribute__((ext_vector_type(4)));
DI s16x4 lds_tr(lds_cptr p) { return __builtin_bit_cast(s16x4, __builtin_amdgcn_ds_read_tr16_b64_v4i16((LAS v4i16_t*)p)); }
DI bf16x8 lds_trfrag(lds_cptr p, int off2) {
    s16x4 a = lds_tr(p), b = lds_tr(p + off2);
    s16x8 r = __builtin_shufflevector(a, b, 0, 1, 2, 3, 4, 5, 6, 7);
    return __builtin_bit_cast(bf16x8, r);
}
DI float dot2(unsigned a, unsigned b, float c) {
    return __builtin_amdgcn_fdot2_f32_bf16(__builtin_bit_cast(bf16x2, a), __builtin_bit_cast(bf16x2, b), c, false);
}
DI float other_half(float v) {
    auto r = __builtin_amdgcn_permlane32_swap(__float_as_uint(v), __float_as_uint(v), false, false);
    return __uint_as_float((threadIdx.x & 32) ? r[0] : r[1]);
}
DI float sum_halves(float v) {
    auto r = __builtin_amdgcn_permlane32_swap(__float_as_uint(v), __float_as_uint(v), false, false);
    return __uint_as_float(r[0]) + __uint_as_float(r[1]);
}
DI u32x4 pack_swap(float a0, float a1, float a2, float a3, float a4, float a5, float a6, float a7) {
    unsigned p0 = cvtpk(a0, a1), p1 = cvtpk(a2, a3), p2 = cvtpk(a4, a5), p3 = cvtpk(a6, a7);
    auto s0 = __builtin_amdgcn_permlane32_swap(p0, p2, false, false);
    auto s1 = __builtin_amdgcn_permlane32_swap(p1, p3, false, false);
    u32x4 r = {s0[0], s1[0], s0[1], s1[1]};
    return r;
}

constexpr int P1_BUF = 32768, P1_NBUF = 4, P1_MISC = P1_BUF * P1_NBUF, P1_LDS = P1_MISC + 2048;

template <int TYPE>
DI void p1_body(const __hip_bfloat16* x, const __hip_bfloat16* wqkv, char* ws, int tb, int cb, char* smem) {
    const int tid = threadIdx.x, lane = tid & 63, w = rfl(tid >> 6), r = lane & 31, hh = lane >> 5;
    const unsigned lds0 = (unsigned)(uintptr_t)smem;
    const int tp = w & 1, cp = w >> 1;
    const char* xb = (const char*)x + (size_t)tb * 128 * 1024;
    const char* wb = (const char*)wqkv + cb * 256;
    unsigned xvo[4], wvo;
    {
        const int rl = lane >> 3, cs = lane & 7;
#pragma unroll
        for (int i = 0; i < 4; ++i) { const int row = (4 * w + i) * 8 + rl; xvo[i] = row * 1024 + ((cs ^ ((row >> 1) & 7)) << 4); }
        const int nb = lane >> 4, q = (lane >> 2) & 3, c = lane & 3;
        wvo = (16 * w + q) * 3072 + nb * 64 + c * 16;
    }
    auto issue = [&](int ch) {
        const unsigned dst = lds0 + (ch % P1_NBUF) * P1_BUF + w * 4096;
#pragma unroll
        for (int i = 0; i < 4; ++i) glds16(xb + ch * 128, xvo[i], dst + i * 1024);
#pragma unroll
        for (int i = 0; i < 4; ++i) glds16(wb + (size_t)(ch * 64 + 4 * i) * 3072, wvo, dst + 16384 + i * 1024);
    };
#pragma unroll
    for (int ch = 0; ch < 4; ++ch) issue(ch);

    unsigned xro[4];
    {
        const int f = (r >> 1) & 7;
#pragma unroll
        for (int ks = 0; ks < 4; ++ks) xro[ks] = (64 * tp + 32 * cp + r) * 128 + (((2 * ks + hh) ^ f) << 4);
    }
    const int xd = cp ? -4096 : 4096, wd = tp ? -256 : 256;
    const unsigned wro = 16384 + hh * 2048 + ((lane & 15) >> 2) * 64 + ((lane >> 4) & 1) * 32 + (lane & 3) * 8 + cp * 512 + tp * 256;
    f32x16 acc[2][2] = {};
    float s1 = 0.f, s2 = 0.f, cs = 0.f;
    lds_cptr L = (lds_cptr)smem;
#pragma unroll
    for (int ch = 0; ch < 8; ++ch) {
        if (ch == 0) wait_bar<24>(); else if (ch < 6) wait_bar<16>(); else if (ch == 6) wait_bar<8>(); else wait_bar<0>();
        if (ch >= 1 && ch + 3 < 8) issue(ch + 3);
        lds_cptr B = L + (ch % P1_NBUF) * P1_BUF;
#pragma unroll
        for (int ks = 0; ks < 4; ++ks) {
            bf16x8 xf[2], wf[2];
            xf[0] = lds_b128(B + xro[ks]); xf[1] = lds_b128(B + xro[ks] + xd);
            wf[0] = lds_trfrag(B + wro + ks * 4096, 1024); wf[1] = lds_trfrag(B + wro + wd + ks * 4096, 1024);
            {
                u32x4 xs = __builtin_bit_cast(u32x4, xf[0]);
                u32x4 wsel = __builtin_bit_cast(u32x4, wf[0]);
#pragma unroll
                for (int j = 0; j < 4; ++j) { s1 = dot2(xs[j], 0x3f803f80u, s1); s2 = dot2(xs[j], xs[j], s2); cs = dot2(wsel[j], 0x3f803f80u, cs); }
            }
#pragma unroll
            for (int a = 0; a < 2; ++a)
#pragma unroll
                for (int b = 0; b < 2; ++b) acc[a][b] = (TYPE == 2) ? MFMA(xf[a], wf[b], acc[a][b]) : MFMA(wf[b], xf[a], acc[a][b]);
        }
    }
    s1 = sum_halves(s1); s2 = sum_halves(s2); cs = sum_halves(cs);
    LAS f32x2* st = (LAS f32x2*)(smem + P1_MISC);
    LAS float* csl = (LAS float*)(smem + P1_MISC + 1024);
    if (lane < 32) {
        const float mean = s1 * (1.f / DM), var = fmaxf(s2 * (1.f / DM) - mean * mean, 0.f);
        f32x2 v = {mean, rsqrtf(var + LN_EPS)};
        st[(2 * tp + cp) * 32 + r] = v;
        csl[(2 * cp + tp) * 32 + r] = cs;
    }
    asm volatile("s_waitcnt lgkmcnt(0)\n\ts_barrier" ::: "memory");
#pragma unroll
    for (int a = 0; a < 2; ++a) {
        const int tl = 2 * tp + (a ^ cp), tile = tb * 4 + tl;
#pragma unroll
        for (int b = 0; b < 2; ++b) {
            const int ct = 2 * cp + (b ^ tp);
            const int head0 = (cb & 3) * 8 + ct * 2;
            if constexpr (TYPE != 2) {
                const f32x2 ms = st[tl * 32 + r];
                const float sc = ms[1] * (TYPE == 0 ? QSCALE : 1.f);
                float v[16];
#pragma unroll
                for (int g = 0; g < 4; ++g) {
                    const f32x4 c4 = *(LAS const f32x4*)(csl + ct * 32 + 8 * g + 4 * hh);
#pragma unroll
                    for (int e = 0; e < 4; ++e) v[4 * g + e] = (acc[a][b][4 * g + e] - ms[0] * c4[e]) * sc;
                }
                char* dst = ws + (TYPE == 0 ? WS_QF : WS_KF);
#pragma unroll
                for (int hsel = 0; hsel < 2; ++hsel) {
                    const u32x4 o = pack_swap(v[8 * hsel], v[8 * hsel + 1], v[8 * hsel + 2], v[8 * hsel + 3], v[8 * hsel + 4], v[8 * hsel + 5], v[8 * hsel + 6], v[8 * hsel + 7]);
                    *(u32x4*)(dst + ((size_t)(head0 + hsel) * 64 + tile) * 1024 + lane * 16) = o;
                    if constexpr (TYPE == 1) {
                        float n2 = 0.f;
#pragma unroll
                        for (int e = 0; e < 4; ++e) n2 = dot2(o[e], o[e], n2);
                        n2 = sum_halves(n2);
#pragma unroll
                        for (int d = 1; d < 32; d <<= 1) n2 = fmaxf(n2, __shfl_xor(n2, d, 64));
                        if (lane == 0) ((float*)(ws + WS_KM))[(head0 + hsel) * 64 + tile] = n2;
                    }
                }
            } else {
                const float csn = csl[ct * 32 + r];
                float v[16];
#pragma unroll
                for (int g = 0; g < 4; ++g) {
#pragma unroll
                    for (int e = 0; e < 4; ++e) {
                        const f32x2 ms = st[tl * 32 + 8 * g + 4 * hh + e];
                        v[4 * g + e] = (acc[a][b][4 * g + e] - ms[0] * csn) * ms[1];
                    }
                }
                char* dst = ws + WS_VF + ((size_t)(head0 + (r >> 4)) * 64 + tile) * 1024 + hh * 256 + (r & 15) * 16;
#pragma unroll
                for (int s = 0; s < 2; ++s) {
                    u32x4 o = {cvtpk(v[8 * s], v[8 * s + 1]), cvtpk(v[8 * s + 2], v[8 * s + 3]), cvtpk(v[8 * s + 4], v[8 * s + 5]), cvtpk(v[8 * s + 6], v[8 * s + 7])};
                    *(u32x4*)(dst + s * 512) = o;
                }
            }
        }
    }
}

DI void p1_helper(const __hip_bfloat16* wg, const __hip_bfloat16* wu, char* ws, int hid, char* smem) {
    const int tid = threadIdx.x;
    LAS float* red = (LAS float*)smem;
    for (int job = hid; job < 86; job += 64) {
        const __hip_bfloat16* W = job < 43 ? wg : wu;
        const int blk = job < 43 ? job : job - 43;
        const int c4 = tid & 3, rg = tid >> 2;
        float s[8] = {};
#pragma unroll
        for (int i = 0; i < 8; ++i) {
            const u32x4 v = *(const u32x4*)((const char*)W + (size_t)(rg * 8 + i) * (FF * 2) + blk * 64 + c4 * 16);
#pragma unroll
            for (int e = 0; e < 4; ++e) { s[2 * e] += bflo(v[e]); s[2 * e + 1] += bfhi(v[e]); }
        }
        __syncthreads();
#pragma unroll
        for (int e = 0; e < 8; ++e) red[rg * 32 + c4 * 8 + e] = s[e];
        __syncthreads();
        if (tid < 32) {
            float t = 0.f;
            for (int i = 0; i < 64; ++i) t += red[i * 32 + tid];
            ((float*)(ws + WS_CS))[(job < 43 ? 0 : FF) + blk * 32 + tid] = t;
        }
    }
}

__global__ __launch_bounds__(256, 1) void k_qkv(const __hip_bfloat16* __restrict__ x, const __hip_bfloat16* __restrict__ wqkv, const __hip_bfloat16* __restrict__ wg,
                                                 const __hip_bfloat16* __restrict__ wu, char* __restrict__ ws) {
    extern __shared__ __attribute__((aligned(16))) char smem[];
    const int b = blockIdx.x, xcd = b & 7, idx = b >> 3;
    if (idx >= 24) { p1_helper(wg, wu, ws, xcd * 8 + idx - 24, smem); return; }
    const int tb = (xcd >> 1) * 4 + (idx & 3), cb = (xcd & 1) * 6 + (idx >> 2);
    if (cb < 4) p1_body<0>(x, wqkv, ws, tb, cb, smem);
    else if (cb < 8) p1_body<1>(x, wqkv, ws, tb, cb, smem);
    else p1_body<2>(x, wqkv, ws, tb, cb, smem);
}

constexpr int P2_LDS = 131072 + 2048;
constexpr float SAFE_RANGE = 80.f;

DI void attn_reads(bf16x8& kf, bf16x8& v0, bf16x8& v1, unsigned ka, unsigned va, const int koff, const int voff) {
    asm volatile("ds_read_b128 %0, %3 offset:%5\n\t"
                 "s_mov_b64 exec, %7\n\t"
                 "ds_read_b128 %1, %4 offset:%6\n\t"
                 "ds_read_b128 %2, %4 offset:%6+512\n\t"
                 "s_mov_b64 exec, -1"
                 : "=&v"(kf), "+v"(v0), "+v"(v1) : "v"(ka), "v"(va), "i"(koff), "i"(voff), "s"(0x0000ffff0000ffffull) : "memory");
}

__global__ __launch_bounds__(512, 2) void k_attn(char* __restrict__ ws) {
    extern __shared__ __attribute__((aligned(16))) char smem[];
    const int b = blockIdx.x, xcd = b & 7, idx = b >> 3, head = xcd * 4 + (idx >> 3), qb = idx & 7;
    const int tid = threadIdx.x, lane = tid & 63, w = rfl(tid >> 6), r = lane & 31, hh = lane >> 5;
    const unsigned lds0 = (unsigned)(uintptr_t)smem;
    const int qtile = qb * 8 + w;
    u32x4 qu = gld128(ws + WS_QF + ((size_t)head * 64 + qtile) * 1024, lane * 16);
    unsigned kmu = gld32(ws + WS_KM + head * 256, lane * 4);
    const char* kg = ws + WS_KF + (size_t)head * 65536;
    const char* vg = ws + WS_VF + (size_t)head * 65536;
#pragma unroll
    for (int c = 0; c < 8; ++c) {
        glds16(kg + (c * 8) * 1024, w * 1024 + lane * 16, lds0 + (c * 8 + w) * 1024);
        glds16(vg + (c * 8) * 1024, w * 1024 + lane * 16, lds0 + 65536 + (c * 8 + w) * 1024);
    }
    asm volatile("s_waitcnt vmcnt(16)" : "+v"(qu), "+v"(kmu)::"memory");
    const bf16x8 qf = __builtin_bit_cast(bf16x8, qu);
    float kmax2 = __uint_as_float(kmu);
#pragma unroll
    for (int d = 1; d < 64; d <<= 1) kmax2 = fmaxf(kmax2, __shfl_xor(kmax2, d, 64));
    float q2 = 0.f;
    {
#pragma unroll
        for (int e = 0; e < 4; ++e) q2 = dot2(qu[e], qu[e], q2);
        q2 = sum_halves(q2);
    }
    const float bound = sqrtf(q2 * kmax2) * 1.01f;
    const bool fast = __all(2.f * bound <= SAFE_RANGE);

    f32x16 o = {};
    const unsigned ka = lds0 + lane * 16, va = lds0 + 65536 + hh * 256 + (r & 15) * 16;
    if (fast) {
        f32x16 negm;
#pragma unroll
        for (int i = 0; i < 16; ++i) negm[i] = -bound;
        const u32x4 ones = {0x3f803f80u, 0x3f803f80u, 0x3f803f80u, 0x3f803f80u};
        bf16x8 kA, kB, vA0 = __builtin_bit_cast(bf16x8, ones), vA1 = vA0, vB0 = vA0, vB1 = vA0;
        f32x16 sA, sB;
        wait_bar<14>();
        {
            bf16x8 k0;
            attn_reads(k0, vA0, vA1, ka, va, 0, 0);
            asm volatile("ds_read_b128 %0, %1 offset:1024" : "=v"(kB) : "v"(ka) : "memory");
            asm volatile("s_waitcnt lgkmcnt(0)" : "+v"(k0), "+v"(vA0), "+v"(vA1), "+v"(kB)::"memory");
            sA = MFMA(k0, qf, negm);
        }
#define ATT_STEP(sC, sN, kN, kNN, vC0, vC1, vN0, vN1, KOFF, VOFF)                                                          \
    {                                                                                                                     \
        attn_reads(kNN, vN0, vN1, kp, vp, KOFF, VOFF);                                                                     \
        sN = MFMA(kN, qf, negm);                                                                                          \
        u32x4 p0, p1;                                                                                                     \
        _Pragma("unroll") for (int i = 0; i < 4; ++i)                                                                      \
            p0[i] = cvtpk(__builtin_amdgcn_exp2f(sC[2 * i]), __builtin_amdgcn_exp2f(sC[2 * i + 1]));                       \
        o = MFMA(vC0, __builtin_bit_cast(bf16x8, p0), o);                                                                 \
        _Pragma("unroll") for (int i = 0; i < 4; ++i)                                                                      \
            p1[i] = cvtpk(__builtin_amdgcn_exp2f(sC[8 + 2 * i]), __builtin_amdgcn_exp2f(sC[9 + 2 * i]));                   \
        o = MFMA(vC1, __builtin_bit_cast(bf16x8, p1), o);                                                                 \
        asm volatile("s_waitcnt lgkmcnt(0)" : "+v"(kNN), "+v"(vN0), "+v"(vN1)::"memory");                                  \
    }
        for (int c = 0; c < 8; ++c) {
            unsigned kp = ka + c * 8192, vp = va + c * 8192;
            ATT_STEP(sA, sB, kB, kA, vA0, vA1, vB0, vB1, 2048, 1024)
            ATT_STEP(sB, sA, kA, kB, vB0, vB1, vA0, vA1, 3072, 2048)
            ATT_STEP(sA, sB, kB, kA, vA0, vA1, vB0, vB1, 4096, 3072)
            ATT_STEP(sB, sA, kA, kB, vB0, vB1, vA0, vA1, 5120, 4096)
            ATT_STEP(sA, sB, kB, kA, vA0, vA1, vB0, vB1, 6144, 5120)
            ATT_STEP(sB, sA, kA, kB, vB0, vB1, vA0, vA1, 7168, 6144)
            switch (c) {
                case 0: wait_bar<12>(); break; case 1: wait_bar<10>(); break; case 2: wait_bar<8>(); break; case 3: wait_bar<6>(); break;
                case 4: wait_bar<4>(); break; case 5: wait_bar<2>(); break; case 6: wait_bar<0>(); break; default: break;
            }
            ATT_STEP(sA, sB, kB, kA, vA0, vA1, vB0, vB1, 8192, 7168)
            ATT_STEP(sB, sA, kA, kB, vB0, vB1, vA0, vA1, 9216, 8192)
        }
    } else {
        wait_bar<0>();
        float m = -INFINITY;
        const u32x4 ones = {0x3f803f80u, 0x3f803f80u, 0x3f803f80u, 0x3f803f80u};
        bf16x8 v0 = __builtin_bit_cast(bf16x8, ones), v1 = v0, kf;
        const f32x16 zero = {};
        for (int t = 0; t < 64; ++t) {
            attn_reads(kf, v0, v1, ka + t * 1024, va + t * 1024, 0, 0);
            asm volatile("s_waitcnt lgkmcnt(0)" : "+v"(kf), "+v"(v0), "+v"(v1)::"memory");
            f32x16 s = MFMA(kf, qf, zero);
            float tm = s[0];
#pragma unroll
            for (int i = 1; i < 16; ++i) tm = fmaxf(tm, s[i]);
            tm = fmaxf(tm, other_half(tm));
            const float mn = fmaxf(m, tm);
            const float alpha = __builtin_amdgcn_exp2f(m - mn);
            m = mn;
#pragma unroll
            for (int i = 0; i < 16; ++i) o[i] *= alpha;
            u32x4 p0, p1;
#pragma unroll
            for (int i = 0; i < 4; ++i) p0[i] = cvtpk(__builtin_amdgcn_exp2f(s[2 * i] - m), __builtin_amdgcn_exp2f(s[2 * i + 1] - m));
#pragma unroll
            for (int i = 0; i < 4; ++i) p1[i] = cvtpk(__builtin_amdgcn_exp2f(s[8 + 2 * i] - m), __builtin_amdgcn_exp2f(s[9 + 2 * i] - m));
            o = MFMA(v0, __builtin_bit_cast(bf16x8, p0), o);
            o = MFMA(v1, __builtin_bit_cast(bf16x8, p1), o);
        }
    }
    const float inv = 1.f / o[8];
    const u32x4 ov = pack_swap(o[0] * inv, o[1] * inv, o[2] * inv, o[3] * inv, o[4] * inv, o[5] * inv, o[6] * inv, o[7] * inv);
    *(u32x4*)(ws + WS_OF + ((size_t)qtile * 32 + head) * 1024 + lane * 16) = ov;
}

constexpr int P35_BUF = 32768, P35_NBUF = 4, P35_LDS = P35_BUF * P35_NBUF;

template <int MODE>
__global__ __launch_bounds__(256, 1) void k_proj(const __hip_bfloat16* __restrict__ x, const __hip_bfloat16* __restrict__ W, char* __restrict__ ws, __hip_bfloat16* __restrict__ out) {
    extern __shared__ __attribute__((aligned(16))) char smem[];
    constexpr int KS = MODE == 0 ? 32 : 86;
    constexpr int NCH = (KS + 7) / 8;
    const int b = blockIdx.x, xcd = b & 7, idx = b >> 3, tb = xcd * 4 + (idx & 3), cb = idx >> 2;
    const int tid = threadIdx.x, lane = tid & 63, w = rfl(tid >> 6), r = lane & 31, hh = lane >> 5;
    const unsigned lds0 = (unsigned)(uintptr_t)smem;
    const char* ab = ws + (MODE == 0 ? WS_OF : WS_AF) + (size_t)tb * 2 * KS * 1024;
    const char* wb = (const char*)W + cb * 128;
    const unsigned wvo = ((lane >> 5) * 4 + ((lane >> 2) & 3)) * 1024 + ((lane >> 4) & 1) * 64 + (lane & 3) * 16;
    auto issue = [&](int ch) {
        const int nks = (KS - ch * 8) < 8 ? (KS - ch * 8) : 8;
        const unsigned dst = lds0 + (ch % P35_NBUF) * P35_BUF;
        if (nks == 8) {
#pragma unroll
            for (int i = 0; i < 4; ++i) { const int a = w & 1, j = (w >> 1) * 4 + i; glds16(ab + ((size_t)a * KS + ch * 8 + j) * 1024, lane * 16, dst + (a * 8 + j) * 1024); }
#pragma unroll
            for (int i = 0; i < 4; ++i) { const int p = 4 * w + i; glds16(wb + (size_t)(ch * 128 + p * 8) * 1024, wvo, dst + 16384 + p * 1024); }
        } else {
#pragma unroll
            for (int i = 0; i < 3; ++i) { const int a = w & 1, j = (w >> 1) * 3 + i; glds16(ab + ((size_t)a * KS + ch * 8 + j) * 1024, lane * 16, dst + (a * 8 + j) * 1024); }
#pragma unroll
            for (int i = 0; i < 3; ++i) { const int p = 3 * w + i; glds16(wb + (size_t)(ch * 128 + p * 8) * 1024, wvo, dst + 16384 + p * 1024); }
        }
    };
    const int ta = w & 1, cj = w >> 1;
    const int tile = tb * 2 + ta, token = tile * 32 + r, n0 = cb * 64 + cj * 32;
    u32x2 res[4];
#pragma unroll
    for (int g = 0; g < 4; ++g) {
        if constexpr (MODE == 0) res[g] = gld64(x, token * 1024 + (n0 + 8 * g + 4 * hh) * 2);
        else res[g] = gld64(ws + WS_HF, (tile * 32 + (n0 >> 4) + (g >> 1)) * 1024 + ((g & 1) * 32 + r) * 16 + hh * 8);
    }
#pragma unroll
    for (int ch = 0; ch < 4; ++ch) issue(ch);
    const unsigned aro = ta * 8192 + lane * 16;
    const unsigned wro = 16384 + hh * 1024 + ((lane & 15) >> 2) * 64 + ((lane >> 4) & 1) * 32 + (lane & 3) * 8 + cj * 256;
    f32x16 acc = {};
    lds_cptr L = (lds_cptr)smem;
#pragma unroll
    for (int ch = 0; ch < NCH; ++ch) {
        if constexpr (MODE == 0) {
            if (ch == 0) wait_bar<24>(); else if (ch == 1) wait_bar<16>(); else if (ch == 2) wait_bar<8>(); else wait_bar<0>();
        } else {
            if (ch == 0) wait_bar<24>(); else if (ch <= 7) wait_bar<16>(); else if (ch == 8) wait_bar<14>(); else if (ch == 9) wait_bar<6>(); else wait_bar<0>();
            if (ch >= 1 && ch + 3 < NCH) issue(ch + 3);
        }
        lds_cptr B = L + (ch % P35_NBUF) * P35_BUF;
        const int nks = (KS - ch * 8) < 8 ? (KS - ch * 8) : 8;
#pragma unroll
        for (int ks = 0; ks < 8; ++ks) {
            if (ks < nks) {
                const bf16x8 af = lds_b128(B + aro + ks * 1024);
                const bf16x8 wf = lds_trfrag(B + wro + ks * 2048, 512);
                acc = MFMA(wf, af, acc);
            }
        }
    }
    float v[16];
#pragma unroll
    for (int g = 0; g < 4; ++g) {
        PIN(res[g]);
        v[4 * g] = acc[4 * g] + bflo(res[g][0]); v[4 * g + 1] = acc[4 * g + 1] + bfhi(res[g][0]);
        v[4 * g + 2] = acc[4 * g + 2] + bflo(res[g][1]); v[4 * g + 3] = acc[4 * g + 3] + bfhi(res[g][1]);
    }
    if constexpr (MODE == 0) {
        float s1 = 0.f, s2 = 0.f;
#pragma unroll
        for (int g = 0; g < 2; ++g) {
            const u32x4 o = pack_swap(v[8 * g], v[8 * g + 1], v[8 * g + 2], v[8 * g + 3], v[8 * g + 4], v[8 * g + 5], v[8 * g + 6], v[8 * g + 7]);
            *(u32x4*)(ws + WS_HF + ((size_t)tile * 32 + (n0 >> 4) + g) * 1024 + lane * 16) = o;
#pragma unroll
            for (int e = 0; e < 4; ++e) { s1 = dot2(o[e], 0x3f803f80u, s1); s2 = dot2(o[e], o[e], s2); }
        }
        s1 = sum_halves(s1); s2 = sum_halves(s2);
        if (lane < 32) { f32x2 sv = {s1, s2}; *(f32x2*)(ws + WS_ST + ((size_t)token * 16 + (n0 >> 5)) * 8) = sv; }
    } else {
#pragma unroll
        for (int g = 0; g < 2; ++g) {
            const u32x4 o = pack_swap(v[8 * g], v[8 * g + 1], v[8 * g + 2], v[8 * g + 3], v[8 * g + 4], v[8 * g + 5], v[8 * g + 6], v[8 * g + 7]);
            *(u32x4*)((char*)out + (size_t)token * 1024 + (n0 + 16 * g + 8 * hh) * 2) = o;
        }
    }
}

constexpr int P4_BUF = 20480, P4_NBUF = 7, P4_LDS = P4_BUF * P4_NBUF;

__global__ __launch_bounds__(256, 1) void k_gateup(const __hip_bfloat16* __restrict__ wg, const __hip_bfloat16* __restrict__ wu, char* __restrict__ ws) {
    extern __shared__ __attribute__((aligned(16))) char smem[];
    const int b = blockIdx.x, xcd = b & 7, idx = b >> 3;
    const int u = xcd * 30 + idx;
    const int tb = u / 15, cg = u % 15;
    const int tid = threadIdx.x, lane = tid & 63, w = rfl(tid >> 6), r = lane & 31, hh = lane >> 5;
    const unsigned lds0 = (unsigned)(uintptr_t)smem;
    const char* hb = ws + WS_HF + (size_t)tb * 4 * 32 * 1024;
    const char* wsrc = (const char*)((w >> 1) ? wu : wg);
    unsigned wvo[3];
#pragma unroll
    for (int j = 0; j < 3; ++j) {
        const int s = (3 * (w & 1) + j) * 64 + lane, st = s >> 4, kb = st / 3, nb = st % 3, q = (s >> 2) & 3, c = s & 3;
        int blk = cg * 3 + nb; blk = blk > 42 ? 42 : blk;
        wvo[j] = (4 * kb + q) * (FF * 2) + blk * 64 + c * 16;
    }
    auto issue = [&](int ch) {
        const unsigned dst = lds0 + (ch % P4_NBUF) * P4_BUF;
#pragma unroll
        for (int j = 0; j < 2; ++j) glds16(hb + ((size_t)w * 32 + ch * 2 + j) * 1024, lane * 16, dst + (w * 2 + j) * 1024);
#pragma unroll
        for (int j = 0; j < 3; ++j) glds16(wsrc + (size_t)ch * 32 * (FF * 2), wvo[j], dst + 8192 + (w >> 1) * 6144 + (3 * (w & 1) + j) * 1024);
    };
    const int tile = tb * 4 + w, token = tile * 32 + r;
    u32x4 stv[8], cgv[3][4], cuv[3][4];
#pragma unroll
    for (int i = 0; i < 8; ++i) stv[i] = gld128(ws + WS_ST, token * 128 + i * 16);
#pragma unroll
    for (int j = 0; j < 3; ++j) {
        int blk = cg * 3 + j; blk = blk > 42 ? 42 : blk;
#pragma unroll
        for (int g = 0; g < 4; ++g) {
            cgv[j][g] = gld128(ws + WS_CS + blk * 128, (4 * hh + 8 * g) * 4);
            cuv[j][g] = gld128(ws + WS_CS + FF * 4 + blk * 128, (4 * hh + 8 * g) * 4);
        }
    }
#pragma unroll
    for (int ch = 0; ch < 7; ++ch) issue(ch);
    VWAIT(35);
#pragma unroll
    for (int i = 0; i < 8; ++i) PIN(stv[i]);
#pragma unroll
    for (int j = 0; j < 3; ++j)
#pragma unroll
        for (int g = 0; g < 4; ++g) { PIN(cgv[j][g]); PIN(cuv[j][g]); }
    float mean, rstd;
    {
        float s1 = 0.f, s2 = 0.f;
#pragma unroll
        for (int i = 0; i < 8; ++i) {
            const f32x4 v = __builtin_bit_cast(f32x4, stv[i]);
            s1 += v[0] + v[2]; s2 += v[1] + v[3];
        }
        mean = s1 * (1.f / DM);
        rstd = rsqrtf(fmaxf(s2 * (1.f / DM) - mean * mean, 0.f) + LN_EPS);
    }
    f32x16 ag[3], au[3];
#pragma unroll
    for (int j = 0; j < 3; ++j) {
#pragma unroll
        for (int g = 0; g < 4; ++g) {
            const f32x4 cgf = __builtin_bit_cast(f32x4, cgv[j][g]), cuf = __builtin_bit_cast(f32x4, cuv[j][g]);
#pragma unroll
            for (int e = 0; e < 4; ++e) { ag[j][4 * g + e] = -mean * cgf[e]; au[j][4 * g + e] = -mean * cuf[e]; }
        }
    }
    const unsigned hro = w * 2048 + lane * 16;
    const unsigned wro = 8192 + hh * 1536 + ((lane & 15) >> 2) * 64 + ((lane >> 4) & 1) * 32 + (lane & 3) * 8;
    lds_cptr L = (lds_cptr)smem;
#pragma unroll
    for (int ch = 0; ch < 16; ++ch) {
        if (ch == 0) wait_bar<30>(); else if (ch <= 10) wait_bar<25>();
        else if (ch == 11) wait_bar<20>(); else if (ch == 12) wait_bar<15>(); else if (ch == 13) wait_bar<10>(); else if (ch == 14) wait_bar<5>(); else wait_bar<0>();
        if (ch >= 1 && ch + 6 < 16) issue(ch + 6);
        lds_cptr B = L + (ch % P4_NBUF) * P4_BUF;
#pragma unroll
        for (int ks = 0; ks < 2; ++ks) {
            const bf16x8 hf = lds_b128(B + hro + ks * 1024);
#pragma unroll
            for (int j = 0; j < 3; ++j) {
                const bf16x8 gf = lds_trfrag(B + wro + ks * 3072 + j * 256, 768);
                const bf16x8 uf = lds_trfrag(B + wro + 6144 + ks * 3072 + j * 256, 768);
                ag[j] = MFMA(gf, hf, ag[j]);
                au[j] = MFMA(uf, hf, au[j]);
            }
        }
    }
    constexpr float C1 = -2.f * 0.7978845608028654f * LOG2E, C2 = C1 * 0.044715f;
#pragma unroll
    for (int j = 0; j < 3; ++j) {
        const int blk = cg * 3 + j;
        if (blk < 43) {
            float v[16];
#pragma unroll
            for (int i = 0; i < 16; ++i) {
                const float g = ag[j][i] * rstd, up = au[j][i] * rstd;
                const float e = __builtin_amdgcn_exp2f(g * (C1 + C2 * g * g));
                v[i] = g * __builtin_amdgcn_rcpf(1.f + e) * up;
            }
#pragma unroll
            for (int g = 0; g < 2; ++g) {
                const u32x4 o = pack_swap(v[8 * g], v[8 * g + 1], v[8 * g + 2], v[8 * g + 3], v[8 * g + 4], v[8 * g + 5], v[8 * g + 6], v[8 * g + 7]);
                *(u32x4*)(ws + WS_AF + ((size_t)tile * 86 + blk * 2 + g) * 1024 + lane * 16) = o;
            }
        }
    }
}

extern "C" void kernel_launch(void* const* d_in, const int* in_sizes, int n_in, void* d_out, int out_size, void* d_ws, size_t ws_size, hipStream_t stream) {
    const __hip_bfloat16* x = (const __hip_bfloat16*)d_in[0];
    const __hip_bfloat16* wqkv = (const __hip_bfloat16*)d_in[1];
    const __hip_bfloat16* wo = (const __hip_bfloat16*)d_in[2];
    const __hip_bfloat16* wg = (const __hip_bfloat16*)d_in[4];
    const __hip_bfloat16* wu = (const __hip_bfloat16*)d_in[5];
    const __hip_bfloat16* wd = (const __hip_bfloat16*)d_in[6];
    char* ws = (char*)d_ws;
    __hip_bfloat16* out = (__hip_bfloat16*)d_out;
    (void)hipFuncSetAttribute((const void*)k_qkv, hipFuncAttributeMaxDynamicSharedMemorySize, P1_LDS);
    (void)hipFuncSetAttribute((const void*)k_attn, hipFuncAttributeMaxDynamicSharedMemorySize, P2_LDS);
    (void)hipFuncSetAttribute((const void*)k_proj<0>, hipFuncAttributeMaxDynamicSharedMemorySize, P35_LDS);
    (void)hipFuncSetAttribute((const void*)k_proj<1>, hipFuncAttributeMaxDynamicSharedMemorySize, P35_LDS);
    (void)hipFuncSetAttribute((const void*)k_gateup, hipFuncAttributeMaxDynamicSharedMemorySize, P4_LDS);
    k_qkv<<<256, 256, P1_LDS, stream>>>(x, wqkv, wg, wu, ws);
    k_attn<<<256, 512, P2_LDS, stream>>>(ws);
    k_proj<0><<<256, 256, P35_LDS, stream>>>(x, wo, ws, out);
    k_gateup<<<240, 256, P4_LDS, stream>>>(wg, wu, ws);
    k_proj<1><<<256, 256, P35_LDS, stream>>>(x, wd, ws, out);
}
```

```cpp
#include <hip/hip_runtime.h>
#include <hip/hip_bf16.h>
#include <stdint.h>

typedef __bf16 bf16x8 __attribute__((ext_vector_type(8)));
typedef __bf16 bf16x2 __attribute__((ext_vector_type(2)));
typedef short s16x4 __attribute__((ext_vector_type(4)));
typedef short s16x8 __attribute__((ext_vector_type(8)));
typedef float f32x16 __attribute__((ext_vector_type(16)));
typedef float f32x4 __attribute__((ext_vector_type(4)));
typedef float f32x2 __attribute__((ext_vector_type(2)));
typedef unsigned u32x4 __attribute__((ext_vector_type(4)));
typedef unsigned u32x2 __attribute__((ext_vector_type(2)));
#define LAS __attribute__((address_space(3)))
#define DI __device__ __forceinline__

constexpr int DM = 512, FF = 1376;
constexpr float LOG2E = 1.4426950408889634f;
constexpr float QSCALE = 0.25f * LOG2E;
constexpr float LN_EPS = 1e-5f;

constexpr size_t WS_QF = 0, WS_KF = 2u << 20, WS_VF = 4u << 20, WS_OF = 6u << 20, WS_HF = 8u << 20, WS_AF = 10u << 20,
                 WS_ST = 16u << 20, WS_CS = 17u << 20, WS_KM = (17u << 20) + 65536, WS_FLAG = 18u << 20, WS_EPOCH = WS_FLAG + 4 * 256 * 128,
                 WS_G1 = 19u << 20, WS_G2 = WS_G1 + 65536;
constexpr int LDS_BYTES = 163840;
constexpr unsigned SPIN_CAP = 1u << 17;

#define MFMA(a, b, c) __builtin_amdgcn_mfma_f32_32x32x16_bf16(a, b, c, 0, 0, 0)
#define SB() __builtin_amdgcn_sched_barrier(0)
DI void cbar() { asm volatile("s_waitcnt lgkmcnt(0)\n\ts_barrier" ::: "memory"); }
DI void pbar() { asm volatile("s_waitcnt vmcnt(0) lgkmcnt(0)\n\ts_barrier" ::: "memory"); }
template <int N> DI void lbar() { asm volatile("s_waitcnt vmcnt(%0)\n\ts_barrier" ::"i"(N > 63 ? 63 : N) : "memory"); }
DI void bar() { asm volatile("s_barrier" ::: "memory"); }

DI void dma4(const void* sbase, unsigned v0, unsigned v1, unsigned v2, unsigned v3, unsigned dst) {
    asm volatile("s_mov_b32 m0, %5\n\ts_nop 0\n\tglobal_load_lds_dwordx4 %0, %4 sc1\n\tglobal_load_lds_dwordx4 %1, %4 offset:1024 sc1\n\t"
                 "global_load_lds_dwordx4 %2, %4 offset:2048 sc1\n\tglobal_load_lds_dwordx4 %3, %4 offset:3072 sc1"
                 :: "v"(v0), "v"(v1), "v"(v2), "v"(v3), "s"(sbase), "s"(dst) : "memory");
}
DI void dma4lin(const void* sbase, unsigned v, unsigned dst) { dma4(sbase, v, v, v, v, dst); }
DI void dma2lin(const void* sbase, unsigned v, unsigned dst) {
    asm volatile("s_mov_b32 m0, %2\n\ts_nop 0\n\tglobal_load_lds_dwordx4 %0, %1 sc1\n\tglobal_load_lds_dwordx4 %0, %1 offset:1024 sc1" :: "v"(v), "s"(sbase), "s"(dst) : "memory");
}
DI void dma1dw(const void* sbase, unsigned v, unsigned dst) {
    asm volatile("s_mov_b32 m0, %2\n\ts_nop 0\n\tglobal_load_lds_dword %0, %1 sc1" :: "v"(v), "s"(sbase), "s"(dst) : "memory");
}
DI void gtouch(const void* sbase, unsigned voff) {
    asm volatile("global_load_lds_dword %0, %1" :: "v"(voff), "s"(sbase) : "memory");
}
DI unsigned gld32_wait(const void* sbase, unsigned voff) {
    unsigned r; asm volatile("global_load_dword %0, %1, %2 sc1\n\ts_waitcnt vmcnt(0)" : "=&v"(r) : "v"(voff), "s"(sbase) : "memory"); return r;
}
DI void gst128(void* sbase, unsigned voff, u32x4 v) { asm volatile("global_store_dwordx4 %0, %1, %2 sc1\n\ts_nop 1" ::"v"(voff), "v"(v), "s"(sbase) : "memory"); }
DI void gst64(void* sbase, unsigned voff, u32x2 v) { asm volatile("global_store_dwordx2 %0, %1, %2 sc1\n\ts_nop 1" ::"v"(voff), "v"(v), "s"(sbase) : "memory"); }
DI void gst32(void* sbase, unsigned voff, unsigned v) { asm volatile("global_store_dword %0, %1, %2 sc1\n\ts_nop 1" ::"v"(voff), "v"(v), "s"(sbase) : "memory"); }
#define VWAIT_(N) asm volatile("s_waitcnt vmcnt(" #N ")" ::: "memory")
#define VWAIT(N) VWAIT_(N)
#define PIN(x) asm volatile("" : "+v"(x))
DI unsigned rfl(unsigned x) { return __builtin_amdgcn_readfirstlane(x); }
DI unsigned cvtpk(float lo, float hi) {
    f32x2 v = {lo, hi};
    bf16x2 b = __builtin_convertvector(v, bf16x2);
    return __builtin_bit_cast(unsigned, b);
}
DI float bflo(unsigned u) { return __uint_as_float(u << 16); }
DI float bfhi(unsigned u) { return __uint_as_float(u & 0xffff0000u); }
DI bf16x8 lds_b128(unsigned a) { return *(LAS const bf16x8*)(uintptr_t)a; }
typedef short v4i16_t __attribute__((ext_vector_type(4)));
DI s16x4 lds_tr(unsigned a) { return __builtin_bit_cast(s16x4, __builtin_amdgcn_ds_read_tr16_b64_v4i16((LAS v4i16_t*)(uintptr_t)a)); }
DI bf16x8 lds_trfrag(unsigned a, int off2) {
    s16x4 x = lds_tr(a), y = lds_tr(a + off2);
    s16x8 r = __builtin_shufflevector(x, y, 0, 1, 2, 3, 4, 5, 6, 7);
    return __builtin_bit_cast(bf16x8, r);
}
DI float dot2(unsigned a, unsigned b, float c) {
    return __builtin_amdgcn_fdot2_f32_bf16(__builtin_bit_cast(bf16x2, a), __builtin_bit_cast(bf16x2, b), c, false);
}
DI float other_half(float v) {
    auto r = __builtin_amdgcn_permlane32_swap(__float_as_uint(v), __float_as_uint(v), false, false);
    return __uint_as_float((threadIdx.x & 32) ? r[0] : r[1]);
}
DI float sum_halves(float v) {
    auto r = __builtin_amdgcn_permlane32_swap(__float_as_uint(v), __float_as_uint(v), false, false);
    return __uint_as_float(r[0]) + __uint_as_float(r[1]);
}
template <bool ALL> DI float dpp_max(float v) {
#define DPPMAX(ctrl, rm) v = fmaxf(v, __int_as_float(__builtin_amdgcn_update_dpp(0, __float_as_int(v), ctrl, rm, 0xf, true)))
    DPPMAX(0x111, 0xf); DPPMAX(0x112, 0xf); DPPMAX(0x114, 0xf); DPPMAX(0x118, 0xf); DPPMAX(0x142, 0xa);
    if (ALL) DPPMAX(0x143, 0xc);
    return v;
}
DI u32x4 swap4(unsigned p0, unsigned p1, unsigned p2, unsigned p3) {
    auto s0 = __builtin_amdgcn_permlane32_swap(p0, p2, false, false);
    auto s1 = __builtin_amdgcn_permlane32_swap(p1, p3, false, false);
    u32x4 r = {s0[0], s1[0], s0[1], s1[1]};
    return r;
}
DI u32x4 pack_swap(float a0, float a1, float a2, float a3, float a4, float a5, float a6, float a7) {
    return swap4(cvtpk(a0, a1), cvtpk(a2, a3), cvtpk(a4, a5), cvtpk(a6, a7));
}
constexpr unsigned ONES = 0x3f803f80u;

#define ST(k)
#define ST2(k)

DI void poll(const char* fl, unsigned voff, bool active, unsigned tag) {
    for (unsigned spins = 0; spins < SPIN_CAP; ++spins) {
        unsigned f;
        asm volatile("global_load_dword %0, %1, %2 sc1\n\ts_waitcnt vmcnt(0)" : "=v"(f) : "v"(voff), "s"(fl) : "memory");
        if (__all(!active || f == tag)) break;
        __builtin_amdgcn_s_sleep(1);
    }
}

struct Ptrs { const char *x, *wqkv, *wo, *wg, *wu, *wd; char* ws; char* out; };

#define P1ROT 0
#define T1W 19
constexpr int P1_BUF = 32768, P1_MISC = 3 * P1_BUF;

template <bool ISV>
DI void p1_compute(char* ws, int tb, int cb, char* smem, int w, int lane) {
    const int r = lane & 31, hh = lane >> 5;
    const unsigned lds0 = (unsigned)(uintptr_t)smem;
    const int th = w & 1, ct = w >> 1;
    unsigned x0[4], x1[4];
    {
        const int f = (r >> 1) & 7, t0 = 2 * th, t1 = 2 * th + 1;
#pragma unroll
        for (int ks = 0; ks < 4; ++ks) {
            const unsigned sw = ((2 * ks + hh) ^ f) << 4;
            x0[ks] = lds0 + (32 * t0 + r) * 128 + sw; x1[ks] = lds0 + (32 * t1 + r) * 128 + sw;
        }
    }
    const unsigned wro = lds0 + 16384 + hh * 2048 + ((lane & 15) >> 2) * 64 + ((lane >> 4) & 1) * 32 + (lane & 3) * 8 + ct * 256;
    f32x16 acc0 = {}, acc1 = {};
    bf16x8 X0[4], X1[4], W[4];
#define P1_LOAD(S, B, ks) { X0[S] = lds_b128(x0[ks] + (B)); X1[S] = lds_b128(x1[ks] + (B)); W[S] = lds_trfrag(wro + (B) + (ks) * 4096, 1024); }
#define P1_MMA(S)                                                                                                                 \
    {                                                                                                                             \
        acc0 = ISV ? MFMA(X0[S], W[S], acc0) : MFMA(W[S], X0[S], acc0);                                                            \
        acc1 = ISV ? MFMA(X1[S], W[S], acc1) : MFMA(W[S], X1[S], acc1);                                                            \
    }
    cbar();
    ST(11);
    P1_LOAD(0, 0, 0); P1_LOAD(1, 0, 1);
    unsigned B = 0;
#pragma unroll 1
    for (int ch = 0; ch < 8; ++ch) {
        P1_LOAD(2, B, 2); SB(); P1_MMA(0); SB();
        P1_LOAD(3, B, 3); SB(); P1_MMA(1); SB();
        if (ch < 7) {
            cbar();
            B = B == 2 * P1_BUF ? 0 : B + P1_BUF;
            P1_LOAD(0, B, 0);
        }
        SB(); P1_MMA(2); SB();
        if (ch < 7) { P1_LOAD(1, B, 1); }
        SB(); P1_MMA(3); SB();
    }
    ST(12);
    cbar();
    const LAS f32x2* st = (const LAS f32x2*)(smem + P1_MISC);
    const LAS float* csl = (const LAS float*)(smem + P1_MISC + 1024);
    const int sec = cb >> 2, head0 = (cb & 3) * 8 + ct * 2;
    if constexpr (!ISV) {
        f32x4 c4[4];
#pragma unroll
        for (int g = 0; g < 4; ++g) c4[g] = *(LAS const f32x4*)(csl + ct * 32 + 8 * g + 4 * hh);
#pragma unroll
        for (int a = 0; a < 2; ++a) {
            const int tl = 2 * th + a, tile = tb * 4 + tl;
            const f32x16& acc = a ? acc1 : acc0;
            const f32x2 ms = st[tl * 32 + r];
            const float sc = ms[1] * (sec == 0 ? QSCALE : 1.f), msc = -ms[0] * sc;
            float v[16];
#pragma unroll
            for (int i = 0; i < 16; ++i) v[i] = acc[i] * sc + msc * c4[i >> 2][i & 3];
            char* dst = ws + (sec == 0 ? WS_QF : WS_KF) + ((size_t)head0 * 64 + tile) * 1024;
#pragma unroll
            for (int hs = 0; hs < 2; ++hs) {
                const u32x4 o = pack_swap(v[8 * hs], v[8 * hs + 1], v[8 * hs + 2], v[8 * hs + 3], v[8 * hs + 4], v[8 * hs + 5], v[8 * hs + 6], v[8 * hs + 7]);
                gst128(dst + hs * 65536, lane * 16, o);
                if (sec == 1) {
                    float n2 = 0.f;
#pragma unroll
                    for (int e = 0; e < 8; ++e) n2 = fmaf(v[8 * hs + e], v[8 * hs + e], n2);
                    n2 = dpp_max<false>(sum_halves(n2));
                    if (lane == 31) gst32(ws + WS_KM + (head0 + hs) * 256 + tile * 4, 0, __float_as_uint(n2));
                }
            }
        }
    } else {
        const float csn = csl[ct * 32 + r];
#pragma unroll
        for (int a = 0; a < 2; ++a) {
            const int tl = 2 * th + a, tile = tb * 4 + tl;
            const f32x16& acc = a ? acc1 : acc0;
            float v[16];
#pragma unroll
            for (int g = 0; g < 4; ++g) {
                const LAS f32x4* sp = (const LAS f32x4*)(st + tl * 32 + 8 * g + 4 * hh);
                const f32x4 pa = sp[0], pb = sp[1];
                v[4 * g] = (acc[4 * g] - pa[0] * csn) * pa[1]; v[4 * g + 1] = (acc[4 * g + 1] - pa[2] * csn) * pa[3];
                v[4 * g + 2] = (acc[4 * g + 2] - pb[0] * csn) * pb[1]; v[4 * g + 3] = (acc[4 * g + 3] - pb[2] * csn) * pb[3];
            }
            char* dst = ws + WS_VF + ((size_t)head0 * 64 + tile) * 1024;
            const unsigned vo = (r >> 4) * 65536 + hh * 256 + (r & 15) * 16;
#pragma unroll
            for (int s = 0; s < 2; ++s) {
                u32x4 o = {cvtpk(v[8 * s], v[8 * s + 1]), cvtpk(v[8 * s + 2], v[8 * s + 3]), cvtpk(v[8 * s + 4], v[8 * s + 5]), cvtpk(v[8 * s + 6], v[8 * s + 7])};
                gst128(dst + s * 512, vo, o);
            }
        }
    }
}

DI void gst_granule(char* sbase, unsigned voff, float v, unsigned tag) { u32x2 g = {__float_as_uint(v), tag}; gst64(sbase, voff, g); }
DI void p1_helper(const char* px, const char* pqkv, const char* pg, const char* pu, char* ws, int hw, int lane) {
    if (hw >= 390) return;
    const unsigned tag = __hip_atomic_load((const __attribute__((address_space(1))) unsigned*)(ws + WS_EPOCH), __ATOMIC_RELAXED, __HIP_MEMORY_SCOPE_AGENT) + 1;
    if (hw < 256) {
        const int rl = lane >> 3, seg = lane & 7;
        const char* src = px + (size_t)(hw * 8 + rl) * 1024 + seg * 16;
        u32x4 v[8];
#pragma unroll
        for (int i = 0; i < 8; ++i) v[i] = *(const u32x4*)(src + i * 128);
        float s1 = 0.f, s2 = 0.f;
#pragma unroll
        for (int i = 0; i < 8; ++i)
#pragma unroll
            for (int e = 0; e < 4; ++e) { s1 = dot2(v[i][e], ONES, s1); s2 = dot2(v[i][e], v[i][e], s2); }
#pragma unroll
        for (int d = 1; d < 8; d <<= 1) { s1 += __shfl_xor(s1, d, 64); s2 += __shfl_xor(s2, d, 64); }
        const float mean = s1 * (1.f / DM);
        const float rstd = __builtin_amdgcn_rsqf(fmaxf(s2 * (1.f / DM) - mean * mean, 0.f) + LN_EPS);
        if (seg < 2) gst_granule(ws + WS_G1, (hw * 8 + rl) * 16 + seg * 8, seg ? rstd : mean, tag);
        return;
    }
    const int kind = hw < 304 ? 0 : hw < 347 ? 1 : 2, blk = hw - (kind == 0 ? 256 : kind == 1 ? 304 : 347);
    const uintptr_t wa = (uintptr_t)pqkv + (kind >= 1 ? (uintptr_t)pg - (uintptr_t)pqkv : 0) + (kind == 2 ? (uintptr_t)pu - (uintptr_t)pg : 0);
    const __attribute__((address_space(1))) char* W = (const __attribute__((address_space(1))) char*)wa;
    const unsigned rs = kind == 0 ? 3072 : FF * 2;
    const unsigned vo = (lane >> 2) * rs + blk * 64 + (lane & 3) * 16;
    float s[8] = {};
    {
        u32x4 v[32];
#pragma unroll
        for (int i = 0; i < 32; ++i) v[i] = *(const __attribute__((address_space(1))) u32x4*)(W + (size_t)i * 16 * rs + vo);
        __builtin_amdgcn_sched_barrier(0);
#pragma unroll
        for (int i = 0; i < 32; ++i)
#pragma unroll
            for (int e = 0; e < 4; ++e) { s[2 * e] += bflo(v[i][e]); s[2 * e + 1] += bfhi(v[i][e]); }
    }
#pragma unroll
    for (int e = 0; e < 8; ++e)
#pragma unroll
        for (int d = 4; d < 64; d <<= 1) s[e] += __shfl_xor(s[e], d, 64);
    if (kind == 0) {
        const int rg = (lane >> 2) & 7;
        float val = s[0];
#pragma unroll
        for (int e = 1; e < 8; ++e) val = rg == e ? s[e] : val;
        if (lane < 32) gst_granule(ws + WS_G2, (blk * 32 + (lane & 3) * 8 + rg) * 8, val, tag);
    } else if (lane < 4) {
        char* dst = ws + WS_CS + ((kind == 1 ? 0 : FF) + blk * 32) * 4;
        u32x4 a = {__float_as_uint(s[0]), __float_as_uint(s[1]), __float_as_uint(s[2]), __float_as_uint(s[3])};
        u32x4 c = {__float_as_uint(s[4]), __float_as_uint(s[5]), __float_as_uint(s[6]), __float_as_uint(s[7])};
        gst128(dst, lane * 32, a); gst128(dst, lane * 32 + 16, c);
    }
}

DI unsigned p1_load(const Ptrs& P, int L, int tb, int cb, int rot, int sidx, unsigned lds0, int lane) {
    unsigned vo[4];
    const char* base;
    unsigned cstep, gstep;
    if (L == 0) {
        const int rl = lane >> 3, cs = lane & 7;
#pragma unroll
        for (int i = 0; i < 4; ++i) vo[i] = (8 * i + rl) * 1024 + ((cs ^ ((4 * (i & 1) + (rl >> 1)) & 7)) << 4) - i * 1024;
        base = P.x + (size_t)tb * 128 * 1024; cstep = 128; gstep = 4 * 8192;
    } else {
        const int nb = lane >> 4, q = (lane >> 2) & 3, c = lane & 3;
#pragma unroll
        for (int i = 0; i < 4; ++i) vo[i] = (4 * i + q) * 3072 + nb * 64 + c * 16 - i * 1024;
        base = P.wqkv + cb * 256; cstep = 64 * 3072; gstep = 16 * 3072;
    }
    auto issue = [&](int c, unsigned B) {
        const char* src = base + (size_t)((c + rot) & 7) * cstep;
        const unsigned dst = lds0 + B + L * 16384;
#pragma unroll
        for (int g = 0; g < 4; ++g) dma4(src + (size_t)g * gstep, vo[0], vo[1], vo[2], vo[3], dst + g * 4096);
    };
    issue(0, 0);
    {
        asm volatile("s_mov_b32 m0, %0\n\ts_nop 0" ::"s"(lds0 + LDS_BYTES - 256) : "memory");
        if (L == 0) {
#pragma unroll
            for (int i = 0; i < 3; ++i) { int ln = sidx * 171 + i * 64 + lane; ln = ln > 4095 ? 4095 : ln; gtouch(P.x + (size_t)(tb & ~3) * 128 * 1024, ln * 128); }
        } else {
#pragma unroll
            for (int i = 0; i < 4; ++i) { const int ln = sidx * 256 + i * 64 + lane; gtouch(P.wqkv + (cb / 6) * 1536, (ln / 12) * 3072 + (ln % 12) * 128); }
        }
    }
    issue(1, P1_BUF);
    unsigned B = 2 * P1_BUF;
#pragma unroll 1
    for (int c = 0; c < 8; ++c) {
        if (c == 0) lbar<T1W>(); else if (c < 7) lbar<16>(); else lbar<0>();
        if (c + 2 < 8) { issue(c + 2, B); B = B == 2 * P1_BUF ? 0 : B + P1_BUF; }
    }
    const unsigned tag = rfl(gld32_wait(P.ws + WS_EPOCH, 0)) + 1;
    if (L == 1) {
        u32x4 a, b, c;
        for (unsigned spins = 0; spins < SPIN_CAP; ++spins) {
            asm volatile("global_load_dwordx4 %0, %3, %4 sc1\n\tglobal_load_dwordx4 %1, %3, %4 offset:16 sc1\n\tglobal_load_dwordx4 %2, %5, %6 sc1\n\ts_waitcnt vmcnt(0)"
                         : "=&v"(a), "=&v"(b), "=&v"(c) : "v"(lane * 32), "s"(P.ws + WS_G1 + tb * 128 * 16), "v"(lane * 16), "s"(P.ws + WS_G2 + cb * 128 * 8) : "memory");
            if (__all(a[1] == tag && a[3] == tag && b[1] == tag && b[3] == tag && c[1] == tag && c[3] == tag)) break;
            __builtin_amdgcn_s_sleep(1);
        }
        u32x4 ms = {a[0], a[2], b[0], b[2]};
        u32x2 cs = {c[0], c[2]};
        *(LAS u32x4*)(uintptr_t)(lds0 + P1_MISC + lane * 16) = ms;
        *(LAS u32x2*)(uintptr_t)(lds0 + P1_MISC + 1024 + lane * 8) = cs;
        asm volatile("s_waitcnt lgkmcnt(0)" ::: "memory");
    }
    bar();
    return tag;
}

constexpr int P2_X = 131072 + 2048;
constexpr int P2_KM = P2_X + 8 * 2560;
constexpr float SAFE_RANGE = 80.f;

DI void p2_compute(char* ws, char* smem, int head, int qb, int w, int lane) {
    const int r = lane & 31, hh = lane >> 5;
    const unsigned lds0 = (unsigned)(uintptr_t)smem;
    const int qg = w & 3, kh = w >> 2;
    const int la = qg * 2 + kh, lb = qg * 2 + (1 - kh);
    cbar();
    ST(13);
    const u32x4 qa = *(LAS const u32x4*)(uintptr_t)(lds0 + P2_X + la * 1024 + lane * 16), qbv = *(LAS const u32x4*)(uintptr_t)(lds0 + P2_X + lb * 1024 + lane * 16);
    const float kmax2 = __int_as_float(__builtin_amdgcn_readlane(__float_as_int(dpp_max<true>(*(LAS const float*)(uintptr_t)(lds0 + P2_KM + lane * 4))), 63));
    float qa2 = 0.f, qb2 = 0.f;
#pragma unroll
    for (int e = 0; e < 4; ++e) { qa2 = dot2(qa[e], qa[e], qa2); qb2 = dot2(qbv[e], qbv[e], qb2); }
    const float bnda = __builtin_amdgcn_sqrtf(sum_halves(qa2) * kmax2) * 1.01f, bndb = __builtin_amdgcn_sqrtf(sum_halves(qb2) * kmax2) * 1.01f;
    const bool fast = __all(2.f * bnda <= SAFE_RANGE && 2.f * bndb <= SAFE_RANGE);
    const unsigned ka = lds0 + kh * 32768 + lane * 16, va = lds0 + 65536 + kh * 32768 + hh * 256 + (r & 15) * 16;
    const unsigned xw = lds0 + P2_X + w * 2560 + lane * 16, xr = lds0 + P2_X + (w ^ 4) * 2560 + lane * 16;
    float o[9], ma;
    ST(5);
    if (fast) {
        ma = bnda;
        unsigned kp = ka, vp = va, cnt;
#define EXP(r) "v_exp_f32 v" #r ", v" #r "\n\t"
#define CVT(d, a, b) "v_cvt_pk_bf16_f32 v" #d ", v" #a ", v" #b "\n\t"
#define MSQ(SN, KN, Q, C) "v_mfma_f32_32x32x16_bf16 " SN ", " KN ", " Q ", " C "\n\t"
#define MPO(O, V, P) "v_mfma_f32_32x32x16_bf16 " O ", " V ", " P ", " O "\n\t"
#define RDV(VN0, VN1, VOFF) "s_mov_b64 exec, %[msk]\n\t" "ds_read_b128 " VN0 ", %[vp] offset:" #VOFF "\n\t" "ds_read_b128 " VN1 ", %[vp] offset:" #VOFF "+512\n\t" "s_mov_b64 exec, -1\n\t"
#define RDK(KNN, KOFF) "ds_read_b128 " KNN ", %[kp] offset:" #KOFF "\n\t"
#define STEP(PRE, c0, c1, c2, c3, c4, c5, c6, c7, c8, c9, c10, c11, c12, c13, c14, c15, p14, p15, SN, KN, Q, CC, O, OP, VC0, VP1)  \
        EXP(c0) EXP(c1) CVT(127, p14, p15) EXP(c2) EXP(c3) MPO(OP, VP1, "v[124:127]") PRE                                    \
        CVT(120, c0, c1) EXP(c4) EXP(c5) CVT(121, c2, c3) EXP(c6)                                                            \
        "s_waitcnt lgkmcnt(3)\n\t" MSQ(SN, KN, Q, CC)                                                                        \
        EXP(c7) CVT(122, c4, c5) EXP(c8) EXP(c9) CVT(123, c6, c7) EXP(c10) EXP(c11)                                          \
        MPO(O, VC0, "v[120:123]")                                                                                            \
        CVT(124, c8, c9) EXP(c12) EXP(c13) CVT(125, c10, c11) EXP(c14) EXP(c15) CVT(126, c12, c13)
#define SA_ 32, 33, 34, 35, 36, 37, 38, 39, 40, 41, 42, 43, 44, 45, 46, 47
#define SB_ 48, 49, 50, 51, 52, 53, 54, 55, 56, 57, 58, 59, 60, 61, 62, 63
#define CALL(M, ...) M(__VA_ARGS__)
#define TE(KOFF, VOFF) CALL(STEP, RDV("v[112:115]", "v[116:119]", VOFF), SA_, 62, 63, "v[48:63]", "v[96:99]", "%[qb]", "v[144:159]", "v[64:79]", "v[80:95]", "v[104:107]", "v[116:119]") \
                       CALL(STEP, RDK("v[96:99]", KOFF), SB_, 46, 47, "v[32:47]", "v[100:103]", "%[qa]", "v[128:143]", "v[80:95]", "v[64:79]", "v[104:107]", "v[108:111]")
#define TO(KOFF, VOFF) CALL(STEP, RDV("v[104:107]", "v[108:111]", VOFF), SA_, 62, 63, "v[48:63]", "v[100:103]", "%[qb]", "v[144:159]", "v[64:79]", "v[80:95]", "v[112:115]", "v[108:111]") \
                       CALL(STEP, RDK("v[100:103]", KOFF), SB_, 46, 47, "v[32:47]", "v[96:99]", "%[qa]", "v[128:143]", "v[80:95]", "v[64:79]", "v[112:115]", "v[116:119]")
#define M64(a, b, c, d) "v_mov_b64 v[" #a ":" #b "], v[" #c ":" #d "]\n\t"
#define MOV4(a, b, c, d, src) "v_mov_b32 v" #a ", " src "\n\tv_mov_b32 v" #b ", " src "\n\tv_mov_b32 v" #c ", " src "\n\tv_mov_b32 v" #d ", " src "\n\t"
        asm volatile(
            "s_setprio 3\n\t"
            "ds_read_b128 v[96:99], %[kp]\n\t"
            "v_mov_b32 v128, %[na]\n\tv_mov_b32 v129, %[na]\n\tv_mov_b32 v144, %[nb]\n\tv_mov_b32 v145, %[nb]\n\t"
            "v_mov_b32 v104, %[one]\n\tv_mov_b32 v105, %[one]\n\tv_mov_b64 v[64:65], 0\n\t"
            M64(106, 107, 104, 105) M64(108, 109, 104, 105) M64(110, 111, 104, 105) M64(112, 113, 104, 105) M64(114, 115, 104, 105) M64(116, 117, 104, 105) M64(118, 119, 104, 105)
            "s_mov_b64 exec, %[msk]\n\t"
            "ds_read_b128 v[104:107], %[vp]\n\t"
            "ds_read_b128 v[108:111], %[vp] offset:512\n\t"
            "s_mov_b64 exec, -1\n\t"
            "ds_read_b128 v[100:103], %[kp] offset:1024\n\t"
            M64(130, 131, 128, 129) M64(132, 133, 128, 129) M64(134, 135, 128, 129) M64(136, 137, 128, 129) M64(138, 139, 128, 129) M64(140, 141, 128, 129) M64(142, 143, 128, 129)
            "s_waitcnt lgkmcnt(3)\n\t"
            "s_nop 1\n\t"
            "v_mfma_f32_32x32x16_bf16 v[32:47], v[96:99], %[qa], v[128:143]\n\t"
            M64(146, 147, 144, 145) M64(148, 149, 144, 145) M64(150, 151, 144, 145) M64(152, 153, 144, 145) M64(154, 155, 144, 145) M64(156, 157, 144, 145) M64(158, 159, 144, 145)
            M64(66, 67, 64, 65) M64(68, 69, 64, 65) M64(70, 71, 64, 65) M64(72, 73, 64, 65) M64(74, 75, 64, 65) M64(76, 77, 64, 65) M64(78, 79, 64, 65)
            M64(80, 81, 64, 65) M64(82, 83, 64, 65) M64(84, 85, 64, 65) M64(86, 87, 64, 65) M64(88, 89, 64, 65) M64(90, 91, 64, 65) M64(92, 93, 64, 65) M64(94, 95, 64, 65)
            M64(62, 63, 64, 65) M64(124, 125, 64, 65) M64(126, 127, 64, 65)
            "s_mov_b32 %[cnt], 0\n\t"
            "L_att_%=:\n\t"
            TE(2048, 1024) TO(3072, 2048) TE(4096, 3072) TO(5120, 4096)
            "v_add_u32 %[kp], 0x1000, %[kp]\n\t"
            "v_add_u32 %[vp], 0x1000, %[vp]\n\t"
            "s_cmp_lg_u32 %[cnt], 0\n\t"
            "s_cbranch_scc1 L_att_nb_%=\n\t"
            "s_barrier\n\t"
            "L_att_nb_%=:\n\t"
            "s_add_u32 %[cnt], %[cnt], 1\n\t"
            "s_cmp_eq_u32 %[cnt], 3\n\ts_cbranch_scc0 L_p1_%=\n\ts_setprio 2\n\tL_p1_%=:\n\t"
            "s_cmp_eq_u32 %[cnt], 6\n\ts_cbranch_scc0 L_p2_%=\n\ts_setprio 1\n\tL_p2_%=:\n\t"
            "s_cmp_eq_u32 %[cnt], 7\n\ts_cbranch_scc0 L_p3_%=\n\ts_setprio 0\n\tL_p3_%=:\n\t"
            "s_cmp_lt_u32 %[cnt], 8\n\t"
            "s_cbranch_scc1 L_att_%=\n\t"
            CVT(127, 62, 63)
            "s_nop 1\n\t"
            MPO("v[80:95]", "v[116:119]", "v[124:127]")
            "s_waitcnt lgkmcnt(0)\n\t"
            "s_nop 15\n\t"
            "v_mov_b32 v89, %[bb]\n\t"
            "v_mov_b32 %[o0], v64\n\tv_mov_b32 %[o1], v65\n\tv_mov_b32 %[o2], v66\n\tv_mov_b32 %[o3], v67\n\t"
            "v_mov_b32 %[o4], v68\n\tv_mov_b32 %[o5], v69\n\tv_mov_b32 %[o6], v70\n\tv_mov_b32 %[o7], v71\n\tv_mov_b32 %[o8], v72\n\t"
            "ds_write_b128 %[xw], v[80:83]\n\t"
            "ds_write_b128 %[xw], v[84:87] offset:1024\n\t"
            "ds_write_b64 %[xw2], v[88:89]\n\t"
            : [o0] "=&v"(o[0]), [o1] "=&v"(o[1]), [o2] "=&v"(o[2]), [o3] "=&v"(o[3]), [o4] "=&v"(o[4]), [o5] "=&v"(o[5]), [o6] "=&v"(o[6]), [o7] "=&v"(o[7]), [o8] "=&v"(o[8]),
              [kp] "+v"(kp), [vp] "+v"(vp), [cnt] "=&s"(cnt)
            : [qa] "v"(qa), [qb] "v"(qbv), [na] "v"(-bnda), [nb] "v"(-bndb), [bb] "v"(bndb), [msk] "s"(0x0000ffff0000ffffull), [xw] "v"(xw), [xw2] "v"(xw + 2048 - lane * 8), [one] "v"((r == 16 || r == 20) ? ONES : 0u)
            : "memory", "scc",
              "v32", "v33", "v34", "v35", "v36", "v37", "v38", "v39", "v40", "v41", "v42", "v43", "v44", "v45", "v46", "v47",
              "v48", "v49", "v50", "v51", "v52", "v53", "v54", "v55", "v56", "v57", "v58", "v59", "v60", "v61", "v62", "v63",
              "v64", "v65", "v66", "v67", "v68", "v69", "v70", "v71", "v72", "v73", "v74", "v75", "v76", "v77", "v78", "v79",
              "v80", "v81", "v82", "v83", "v84", "v85", "v86", "v87", "v88", "v89", "v90", "v91", "v92", "v93", "v94", "v95",
              "v96", "v97", "v98", "v99", "v100", "v101", "v102", "v103", "v104", "v105", "v106", "v107", "v108", "v109", "v110", "v111",
              "v112", "v113", "v114", "v115", "v116", "v117", "v118", "v119", "v120", "v121", "v122", "v123", "v124", "v125", "v126", "v127",
              "v128", "v129", "v130", "v131", "v132", "v133", "v134", "v135", "v136", "v137", "v138", "v139", "v140", "v141", "v142", "v143",
              "v144", "v145", "v146", "v147", "v148", "v149", "v150", "v151", "v152", "v153", "v154", "v155", "v156", "v157", "v158", "v159");
    } else {
        bar();
        const u32x4 ones = {ONES, ONES, ONES, ONES};
        const f32x16 zero = {};
#pragma unroll 1
        for (int sl = 1; sl >= 0; --sl) {
            const bf16x8 qf = __builtin_bit_cast(bf16x8, sl ? qbv : qa);
            bf16x8 v0 = __builtin_bit_cast(bf16x8, ones), v1 = v0, kf;
            f32x16 oo = {};
            float m = -INFINITY;
#pragma unroll 1
            for (int t = 0; t < 32; ++t) {
                asm volatile("ds_read_b128 %0, %3\n\ts_mov_b64 exec, %5\n\tds_read_b128 %1, %4\n\tds_read_b128 %2, %4 offset:512\n\ts_mov_b64 exec, -1\n\ts_waitcnt lgkmcnt(0)"
                             : "=&v"(kf), "+v"(v0), "+v"(v1) : "v"(ka + t * 1024), "v"(va + t * 1024), "s"(0x0000ffff0000ffffull) : "memory");
                const f32x16 s = MFMA(kf, qf, zero);
                float tm = s[0];
#pragma unroll
                for (int i = 1; i < 16; ++i) tm = fmaxf(tm, s[i]);
                tm = fmaxf(tm, other_half(tm));
                const float mn = fmaxf(m, tm);
                const float alpha = __builtin_amdgcn_exp2f(m - mn);
                m = mn;
#pragma unroll
                for (int i = 0; i < 16; ++i) oo[i] *= alpha;
                u32x4 p0, p1;
#pragma unroll
                for (int i = 0; i < 4; ++i) p0[i] = cvtpk(__builtin_amdgcn_exp2f(s[2 * i] - m), __builtin_amdgcn_exp2f(s[2 * i + 1] - m));
#pragma unroll
                for (int i = 0; i < 4; ++i) p1[i] = cvtpk(__builtin_amdgcn_exp2f(s[8 + 2 * i] - m), __builtin_amdgcn_exp2f(s[9 + 2 * i] - m));
                oo = MFMA(v0, __builtin_bit_cast(bf16x8, p0), oo);
                oo = MFMA(v1, __builtin_bit_cast(bf16x8, p1), oo);
            }
            if (sl == 1) {
                f32x4 t0 = {oo[0], oo[1], oo[2], oo[3]}, t1 = {oo[4], oo[5], oo[6], oo[7]};
                f32x2 t2 = {oo[8], m};
                *(LAS f32x4*)(uintptr_t)xw = t0; *(LAS f32x4*)(uintptr_t)(xw + 1024) = t1; *(LAS f32x2*)(uintptr_t)(xw + 2048 - lane * 8) = t2;
            } else {
#pragma unroll
                for (int i = 0; i < 9; ++i) o[i] = oo[i];
                ma = m;
            }
        }
    }
    ST(14);
    cbar();
    {
        const f32x4 t0 = *(LAS const f32x4*)(uintptr_t)xr, t1 = *(LAS const f32x4*)(uintptr_t)(xr + 1024);
        const f32x2 t2 = *(LAS const f32x2*)(uintptr_t)(xr + 2048 - lane * 8);
        const float mo = t2[1], mn = fmaxf(ma, mo);
        const float fa = __builtin_amdgcn_exp2f(ma - mn), fo = __builtin_amdgcn_exp2f(mo - mn);
        const float inv = __builtin_amdgcn_rcpf(o[8] * fa + t2[0] * fo);
        const float ia = fa * inv, io = fo * inv;
        const u32x4 ov = pack_swap(o[0] * ia + t0[0] * io, o[1] * ia + t0[1] * io, o[2] * ia + t0[2] * io, o[3] * ia + t0[3] * io,
                                   o[4] * ia + t1[0] * io, o[5] * ia + t1[1] * io, o[6] * ia + t1[2] * io, o[7] * ia + t1[3] * io);
        gst128(ws + WS_OF + ((size_t)(qb * 8 + la) * 32 + head) * 1024, lane * 16, ov);
    }
}

constexpr int P3_X = 153600;
template <int MODE>
DI void p35_compute(const Ptrs& P, char* smem, int tb, int cb, int w, int lane, u32x4& hkeep) {
    constexpr int NFULL = MODE == 0 ? 4 : 10;
    constexpr int A0 = MODE == 0 ? 65536 : 98304, NA = MODE == 0 ? 4 : 3, NW = MODE == 0 ? 4 : 6, XA = MODE == 0 ? 131072 : 147456;
    char* ws = P.ws;
    const int r = lane & 31, hh = lane >> 5;
    const unsigned lds0 = (unsigned)(uintptr_t)smem;
    const int ta = w & 1, cj = (w >> 1) & 1, kh = w >> 2;
    const int tile = tb * 2 + ta, token = tile * 32 + r, n0 = cb * 64 + cj * 32 + kh * 16;
    u32x2 res[2];
    const unsigned aro = lds0 + A0 + ta * 8192 + lane * 16 + kh * 4096;
    const unsigned wro = lds0 + hh * 1024 + ((lane & 15) >> 2) * 64 + ((lane >> 4) & 1) * 32 + (lane & 3) * 8 + cj * 256 + kh * 8192;
    f32x16 acc = {};
    bf16x8 A[4], Wf[4];
#define P35_LD(S, BA, BW, ks) { A[S] = lds_b128(aro + (BA) + (ks) * 1024); Wf[S] = lds_trfrag(wro + (BW) + (ks) * 2048, 512); }
    cbar();
    ST(MODE == 0 ? 7 : 8);
    P35_LD(0, 0, 0, 0); P35_LD(1, 0, 0, 1);
    unsigned BA = 0, BW = 0;
#pragma unroll 1
    for (int ch = 0; ch < NFULL; ++ch) {
        P35_LD(2, BA, BW, 2); SB(); acc = MFMA(Wf[0], A[0], acc); SB();
        P35_LD(3, BA, BW, 3); SB(); acc = MFMA(Wf[1], A[1], acc); SB();
        if (MODE == 1 || ch < NFULL - 1) {
            cbar();
            BA = BA == (NA - 1) * 16384 ? 0 : BA + 16384; BW = BW == (NW - 1) * 16384 ? 0 : BW + 16384;
            if (ch < NFULL - 1) { P35_LD(0, BA, BW, 0); }
        }
        SB(); acc = MFMA(Wf[2], A[2], acc); SB();
        if (ch < NFULL - 1) { P35_LD(1, BA, BW, 1); }
        SB(); acc = MFMA(Wf[3], A[3], acc); SB();
    }
    if constexpr (MODE == 1) {
#pragma unroll
        for (int j = 0; j < 3; ++j) {
            const bf16x8 af = lds_b128(aro + BA - kh * 4096 + (kh * 3 + j) * 1024);
            const bf16x8 wf = lds_trfrag(wro + BW - kh * 8192 + (kh * 3 + j) * 2048, 512);
            acc = MFMA(wf, af, acc);
        }
    }
    ST(MODE == 0 ? 9 : 0);
    {
        LAS f32x4* xw = (LAS f32x4*)(smem + XA + w * 2048 + lane * 16);
        f32x4 s0, s1;
#pragma unroll
        for (int e = 0; e < 4; ++e) { s0[e] = kh ? acc[e] : acc[8 + e]; s1[e] = kh ? acc[4 + e] : acc[12 + e]; }
        xw[0] = s0; xw[64] = s1;
    }
    cbar();
    float v[8];
    {
        LAS const f32x4* xr = (LAS const f32x4*)(smem + XA + (w ^ 4) * 2048 + lane * 16);
        const f32x4 s0 = xr[0], s1 = xr[64];
        if constexpr (MODE == 0) {
            const int row = ta * 32 + r;
#pragma unroll
            for (int g = 0; g < 2; ++g)
                res[g] = *(LAS const u32x2*)(smem + P3_X + row * 128 + (((cj * 4 + kh * 2 + g) ^ ((row >> 1) & 7)) << 4) + hh * 8);
        } else { res[0][0] = hkeep[0]; res[0][1] = hkeep[1]; res[1][0] = hkeep[2]; res[1][1] = hkeep[3]; }
#pragma unroll
        for (int e = 0; e < 4; ++e) { v[e] = (kh ? acc[8 + e] : acc[e]) + s0[e]; v[4 + e] = (kh ? acc[12 + e] : acc[4 + e]) + s1[e]; }
#pragma unroll
        for (int g = 0; g < 2; ++g) {
            v[4 * g] += bflo(res[g][0]); v[4 * g + 1] += bfhi(res[g][0]); v[4 * g + 2] += bflo(res[g][1]); v[4 * g + 3] += bfhi(res[g][1]);
        }
    }
    const unsigned p0 = cvtpk(v[0], v[1]), p1 = cvtpk(v[2], v[3]), p2 = cvtpk(v[4], v[5]), p3 = cvtpk(v[6], v[7]);
    const u32x4 o = swap4(p0, p1, p2, p3);
    if constexpr (MODE == 0) {
        hkeep[0] = p0; hkeep[1] = p1; hkeep[2] = p2; hkeep[3] = p3;
        gst128(ws + WS_HF + ((size_t)tile * 32 + (n0 >> 4)) * 1024, lane * 16, o);
        float s1 = 0.f, s2 = 0.f;
#pragma unroll
        for (int e = 0; e < 4; ++e) { s1 = dot2(o[e], ONES, s1); s2 = dot2(o[e], o[e], s2); }
        s1 = sum_halves(s1); s2 = sum_halves(s2);
        LAS f32x2* sl = (LAS f32x2*)(smem + XA + 8 * 2048);
        if (lane < 32) { f32x2 sv = {s1, s2}; sl[(ta * 32 + r) * 4 + cj * 2 + kh] = sv; }
        cbar();
        if (w == 0) {
            const f32x4 p = *(LAS const f32x4*)(sl + lane * 4), q = *(LAS const f32x4*)(sl + lane * 4 + 2);
            u32x2 sv = {__float_as_uint(p[0] + p[2] + q[0] + q[2]), __float_as_uint(p[1] + p[3] + q[1] + q[3])};
            gst64(ws + WS_ST + (size_t)tb * 64 * 64 + cb * 8, lane * 64, sv);
        }
    } else {
        *(u32x4*)(P.out + (size_t)token * 1024 + (n0 + 8 * hh) * 2) = o;
    }
}

constexpr int P4_ST = 147456, P4_CS = P4_ST + 8192;
template <int NB>
DI void p4_compute(char* ws, char* smem, int tb, int cg, int w, int lane) {
    const int r = lane & 31, hh = lane >> 5, i = w & 3, j0 = NB == 2 ? 0 : 2;
    const unsigned lds0 = (unsigned)(uintptr_t)smem;
    const int tile = tb * 4 + i;
    const unsigned hro = lds0 + 98304 + i * 4096 + lane * 16;
    const unsigned wro = lds0 + hh * 1536 + ((lane & 15) >> 2) * 64 + ((lane >> 4) & 1) * 32 + (lane & 3) * 8 + j0 * 256;
    f32x16 ag[NB], au[NB];
#pragma unroll
    for (int j = 0; j < NB; ++j) { ag[j] = f32x16{}; au[j] = f32x16{}; }
    bf16x8 H[4], G[4][NB], U[4][NB];
#define P4_LOAD(S, BH, BW, ks)                                                                                                    \
    {                                                                                                                             \
        H[S] = lds_b128(hro + (BH) + (ks) * 1024);                                                                                 \
        _Pragma("unroll") for (int j = 0; j < NB; ++j) { G[S][j] = lds_trfrag(wro + (BW) + (ks) * 3072 + j * 256, 768); U[S][j] = lds_trfrag(wro + (BW) + 12288 + (ks) * 3072 + j * 256, 768); } \
    }
#define P4_MMA(S) { _Pragma("unroll") for (int j = 0; j < NB; ++j) { ag[j] = MFMA(G[S][j], H[S], ag[j]); au[j] = MFMA(U[S][j], H[S], au[j]); } }
    cbar();
    ST(6);
    P4_LOAD(0, 0, 0, 0); P4_LOAD(1, 0, 0, 1);
    unsigned BH = 0, BW = 0;
#pragma unroll 1
    for (int ch = 0; ch < 8; ++ch) {
        P4_LOAD(2, BH, BW, 2); SB(); P4_MMA(0); SB();
        P4_LOAD(3, BH, BW, 3); SB(); P4_MMA(1); SB();
        cbar();
        if (ch < 7) {
            BH = BH == 2 * 16384 ? 0 : BH + 16384; BW = BW == 3 * 24576 ? 0 : BW + 24576;
            P4_LOAD(0, BH, BW, 0);
        }
        SB(); P4_MMA(2); SB();
        if (ch < 7) { P4_LOAD(1, BH, BW, 1); }
        SB(); P4_MMA(3); SB();
    }
    ST(15);
    float mean, rstd;
    {
        float s1 = 0.f, s2 = 0.f;
        const LAS f32x4* sp = (const LAS f32x4*)(smem + P4_ST + (i * 32 + r) * 64);
#pragma unroll
        for (int k = 0; k < 4; ++k) { const f32x4 v = sp[k]; s1 += v[0] + v[2]; s2 += v[1] + v[3]; }
        mean = s1 * (1.f / DM);
        rstd = __builtin_amdgcn_rsqf(fmaxf(s2 * (1.f / DM) - mean * mean, 0.f) + LN_EPS);
    }
    constexpr float C1 = -2.f * 0.7978845608028654f * LOG2E, C2 = C1 * 0.044715f;
    const float k1 = C1 * rstd, k2 = C2 * rstd * rstd * rstd, k3 = __builtin_amdgcn_rcpf(rstd * rstd);
#pragma unroll
    for (int j = 0; j < NB; ++j) {
        const int blk = cg * 3 + j0 + j;
        float v[16];
#pragma unroll
        for (int g = 0; g < 4; ++g) {
            const LAS f32x4* cp = (const LAS f32x4*)(smem + P4_CS + ((j0 + j) * 32 + 8 * g + 4 * hh) * 4);
            const f32x4 cgf = cp[0], cuf = cp[32];
#pragma unroll
            for (int e = 0; e < 4; ++e) {
                const float a = ag[j][4 * g + e] - mean * cgf[e], bq = au[j][4 * g + e] - mean * cuf[e];
                const float ex = __builtin_amdgcn_exp2f(a * (k1 + k2 * a * a));
                v[4 * g + e] = a * __builtin_amdgcn_rcpf(k3 + k3 * ex) * bq;
            }
        }
        if (blk < 43) {
#pragma unroll
            for (int g = 0; g < 2; ++g) {
                const u32x4 o = pack_swap(v[8 * g], v[8 * g + 1], v[8 * g + 2], v[8 * g + 3], v[8 * g + 4], v[8 * g + 5], v[8 * g + 6], v[8 * g + 7]);
                gst128(ws + WS_AF + ((size_t)tile * 86 + blk * 2 + g) * 1024, lane * 16, o);
            }
        }
    }
}

__global__ __launch_bounds__(640, 3) void k_block(const char* __restrict__ x, const char* __restrict__ wqkv, const char* __restrict__ wo, const char* __restrict__ wg,
                                                  const char* __restrict__ wu, const char* __restrict__ wd, char* __restrict__ ws, char* __restrict__ out) {
    extern __shared__ __attribute__((aligned(16))) char smem[];
    const Ptrs P = {x, wqkv, wo, wg, wu, wd, ws, out};
    const int b = blockIdx.x, xcd = b & 7, idx = b >> 3;
    const int lane = threadIdx.x & 63, w = rfl(threadIdx.x >> 6);
    const unsigned lds0 = (unsigned)(uintptr_t)smem;
    const bool p1_gemm = idx >= 8;
    const int gi = idx - 8;
    const int tb1 = (xcd >> 1) * 4 + (gi & 3), cb1 = (xcd & 1) * 6 + (gi >> 2);
    const int head = xcd * 4 + (idx >> 3), qb = idx & 7;
    const int tb3 = xcd * 4 + (idx & 3), cb3 = idx >> 2;
    const bool p4_on = !(xcd & 1) || idx < 28;
    const int tb4 = (xcd >> 1) * 4 + (idx & 3), cg4 = (xcd & 1) * 8 + (idx >> 2);
    ST(0);

    if (w < 8) {
        if (p1_gemm) {
            if (cb1 < 8) p1_compute<false>(ws, tb1, cb1, smem, w, lane); else p1_compute<true>(ws, tb1, cb1, smem, w, lane);
        } else p1_helper(x, wqkv, wg, wu, ws, w * 64 + idx * 8 + xcd, lane);
        ST(1);
        pbar();
        p2_compute(ws, smem, head, qb, w, lane);
        ST(2);
        pbar();
        u32x4 hkeep;
        p35_compute<0>(P, smem, tb3, cb3, w, lane, hkeep);
        ST(3);
        pbar();
        if (p4_on) {
            if (w < 4) p4_compute<2>(ws, smem, tb4, cg4, w, lane); else p4_compute<1>(ws, smem, tb4, cg4, w, lane);
        } else bar();
        ST(4);
        pbar();
        p35_compute<1>(P, smem, tb3, cb3, w, lane, hkeep);
        ST(10);
        return;
    }

    const int L = w - 8;
    const char* flags = ws + WS_FLAG;
    const unsigned lin = lane * 16;
    const unsigned tag = p1_gemm ? p1_load(P, L, tb1, cb1, P1ROT, gi, lds0, lane) : rfl(gld32_wait(ws + WS_EPOCH, 0)) + 1;
    bar();
    if (L == 0 && lane == 0) gst32(ws + WS_FLAG, b * 128, tag);
    ST(1);
    {
        const int ptb = lane < 32 ? (lane & 15) : 2 * qb + (lane & 1), pcb = (lane < 16 ? 4 : lane < 32 ? 8 : 0) + (head >> 3);
        const int pb = ((pcb % 6) * 4 + (ptb & 3) + 8) * 8 + (ptb >> 2) * 2 + pcb / 6;
        poll(flags, pb * 128, lane < 34, tag);
        ST(6);
        const int rot = qb * 4;
        const char* src = ws + (L == 0 ? WS_KF : WS_VF) + (size_t)head * 65536;
        const unsigned dst = lds0 + L * 65536;
        if (L == 0) {
            const char* q = ws + WS_QF + ((size_t)head * 64 + qb * 8) * 1024;
            dma4lin(q, lin, lds0 + P2_X); dma4lin(q + 4096, lin, lds0 + P2_X + 4096);
            dma1dw(ws + WS_KM + head * 256, lane * 4, lds0 + P2_KM);
        }
#pragma unroll 1
        for (int g = 0; g < 8; ++g) {
            const unsigned so = ((g * 4 + rot) & 31) * 1024;
            dma4lin(src + so, lin, dst + g * 4096); dma4lin(src + 32768 + so, lin, dst + 32768 + g * 4096);
            if (g == 2) lbar<8>();
        }
        ST(7);
        if (L == 1) {
            asm volatile("s_waitcnt vmcnt(0)" ::: "memory");
        }
        lbar<0>();
        if (L == 1) {
#pragma unroll 1
            for (int g = 0; g < 4; ++g) poll(flags, (g * 64 + lane) * 128, true, tag);
            if (b == 0 && lane == 0) gst32(ws + WS_EPOCH, 0, tag);
            asm volatile("s_mov_b32 m0, %0\n\ts_nop 0" ::"s"(lds0 + LDS_BYTES - 256) : "memory");
            gtouch(wo, (idx * 128 + lane) * 128); gtouch(wo, (idx * 128 + 64 + lane) * 128);
            {
                const int nl = (xcd & 1) ? 10 : 12, ln = lane & 15;
                const unsigned co = (xcd & 1) * 1536 + (ln < nl ? ln : 0) * 128;
#pragma unroll
                for (int i = 0; i < 4; ++i) {
                    unsigned o_ = (idx * 16 + i * 4 + (lane >> 4)) * (FF * 2) + co;
                    o_ = o_ > 512u * FF * 2 - 4 ? 512u * FF * 2 - 4 : o_;
                    gtouch(wg, o_); gtouch(wu, o_);
                }
            }
#pragma unroll
            for (int i = 0; i < 6; ++i) { int ln = i * 64 + lane; ln = ln > 343 ? 343 : ln; gtouch(wd, (idx * 344 + ln) * 128); }
        }
        bar();
    }
    unsigned wvo[4];
    {
        const unsigned wv = ((lane >> 5) * 4 + ((lane >> 2) & 3)) * 1024 + ((lane >> 4) & 1) * 64 + (lane & 3) * 16;
#pragma unroll
        for (int i = 0; i < 4; ++i) wvo[i] = wv + i * 8192 - i * 1024;
    }
    {
        const int rot = cb3 & 3;
        bar();
        if (L == 1) {
            {
                const int rl = lane >> 3, cs = lane & 7;
                unsigned xv[4];
#pragma unroll
                for (int i = 0; i < 4; ++i) xv[i] = (8 * i + rl) * 1024 + ((cs ^ ((4 * (i & 1) + (rl >> 1)) & 7)) << 4) - i * 1024;
                const char* xs = x + (size_t)tb3 * 64 * 1024 + cb3 * 128;
                dma4(xs, xv[0], xv[1], xv[2], xv[3], lds0 + P3_X); dma4(xs + 32768, xv[0], xv[1], xv[2], xv[3], lds0 + P3_X + 4096);
            }
#pragma unroll 1
            for (int c = 0; c < 4; ++c) {
                const char* src = wo + cb3 * 128 + (size_t)((c + rot) & 3) * 128 * 1024;
#pragma unroll
                for (int g = 0; g < 4; ++g) dma4(src + g * 32768, wvo[0], wvo[1], wvo[2], wvo[3], lds0 + c * 16384 + g * 4096);
            }
        }
        if (L == 0) {
            if (lane == 0) gst32(ws + WS_FLAG + 256 * 128, b * 128, tag);
            ST(2);
            const int ph = lane & 31;
            poll(flags + 256 * 128, (((ph & 3) * 8 + (tb3 >> 2)) * 8 + (ph >> 2)) * 128, true, tag);
            ST(8);
            const char* ab = ws + WS_OF + (size_t)tb3 * 2 * 32 * 1024;
#pragma unroll 1
            for (int c = 0; c < 4; ++c) {
                const char* src = ab + ((c + rot) & 3) * 8192;
                const unsigned dst = lds0 + 65536 + c * 16384;
                dma4lin(src, lin, dst); dma4lin(src + 4096, lin, dst + 4096); dma4lin(src + 32768, lin, dst + 8192); dma4lin(src + 32768 + 4096, lin, dst + 12288);
                if (c == 1) lbar<16>();
            }
            lbar<32>(); lbar<16>(); lbar<0>();
        } else {
            lbar<0>(); bar(); bar(); bar();
        }
        bar();
    }
    {
        const int rot = cg4 & 7;
        unsigned gvo[12];
        if (L == 1 && p4_on) {
#pragma unroll
            for (int j = 0; j < 12; ++j) {
                const int s = j * 64 + lane, st = s >> 4, kb = st / 3, nb = st % 3, q = (s >> 2) & 3, c = s & 3;
                int blk = cg4 * 3 + nb; blk = blk > 42 ? 42 : blk;
                gvo[j] = (4 * kb + q) * (FF * 2) + blk * 64 + c * 16 - (j & 3) * 1024;
            }
        }
        auto issue_w = [&](int c, unsigned B) {
            const size_t so = (size_t)((c + rot) & 7) * 64 * (FF * 2);
#pragma unroll
            for (int g = 0; g < 3; ++g) dma4(wg + so, gvo[4 * g], gvo[4 * g + 1], gvo[4 * g + 2], gvo[4 * g + 3], lds0 + B + g * 4096);
#pragma unroll
            for (int g = 0; g < 3; ++g) dma4(wu + so, gvo[4 * g], gvo[4 * g + 1], gvo[4 * g + 2], gvo[4 * g + 3], lds0 + B + 12288 + g * 4096);
        };
        bar();
        bar();
        if (L == 1 && p4_on) { issue_w(0, 0); issue_w(1, 24576); issue_w(2, 2 * 24576); issue_w(3, 3 * 24576); }
        if (L == 0) { if (lane == 0) gst32(ws + WS_FLAG + 2 * 256 * 128, b * 128, tag); ST(3); }
        if (p4_on) {
            if (L == 0) {
                const int ptb = tb4 * 2 + (lane & 1), pcb = (lane >> 1) & 7;
                poll(flags + 2 * 256 * 128, ((pcb * 4 + (ptb & 3)) * 8 + (ptb >> 2)) * 128, true, tag);
                ST(9);
                {
                    const char* sb = ws + WS_ST + (size_t)tb4 * 128 * 64;
                    dma4lin(sb, lin, lds0 + P4_ST); dma4lin(sb + 4096, lin, lds0 + P4_ST + 4096);
                    const char* cb_ = ws + WS_CS + cg4 * 384;
                    dma1dw(cb_, lane * 4, lds0 + P4_CS); dma1dw(cb_ + 256, lane * 4, lds0 + P4_CS + 256);
                    dma1dw(cb_ + FF * 4, lane * 4, lds0 + P4_CS + 512); dma1dw(cb_ + FF * 4 + 256, lane * 4, lds0 + P4_CS + 768);
                }
                const char* hb = ws + WS_HF + (size_t)tb4 * 4 * 32 * 1024;
                auto issue_h = [&](int c, unsigned B) {
                    const char* src = hb + ((c + rot) & 7) * 4096;
#pragma unroll
                    for (int i = 0; i < 4; ++i) dma4lin(src + i * 32768, lin, lds0 + 98304 + B + i * 4096);
                };
                issue_h(0, 0); issue_h(1, 16384);
                unsigned B = 2 * 16384;
#pragma unroll 1
                for (int c = 0; c < 8; ++c) {
                    if (c < 7) lbar<16>(); else lbar<0>();
                    if (c + 2 < 8) { issue_h(c + 2, B); B = B == 2 * 16384 ? 0 : B + 16384; }
                }
            } else {
                unsigned B = 0;
#pragma unroll 1
                for (int c = 0; c < 8; ++c) {
                    if (c == 0) lbar<63>(); else if (c < 6) lbar<48>(); else if (c == 6) lbar<24>(); else lbar<0>();
                    if (c >= 1 && c + 3 < 8) { issue_w(c + 3, B); B = B == 3 * 24576 ? 0 : B + 24576; }
                }
            }
            bar();
        } else bar();
    }
    {
        const int rot = cb3;
        auto issue_w = [&](int c, unsigned B) {
            const int cc = c < 10 ? (c + rot) % 10 : 10;
            const char* src = wd + cb3 * 128 + (size_t)cc * 128 * 1024;
#pragma unroll
            for (int g = 0; g < 3; ++g) dma4(src + g * 32768, wvo[0], wvo[1], wvo[2], wvo[3], lds0 + B + g * 4096);
            dma4(src + (c < 10 ? 3 : 2) * 32768, wvo[0], wvo[1], wvo[2], wvo[3], lds0 + B + 3 * 4096);
        };
        bar();
        if (L == 1) {
#pragma unroll 1
            for (int c = 0; c < 6; ++c) issue_w(c, c * 16384);
        }
        if (L == 0) {
            if (p4_on && lane == 0) gst32(ws + WS_FLAG + 3 * 256 * 128, b * 128, tag);
            ST(4);
            const int ptb = tb3 >> 1, pcg = lane < 15 ? lane : 0;
            poll(flags + 3 * 256 * 128, (((pcg & 7) * 4 + (ptb & 3)) * 8 + (ptb >> 2) * 2 + (pcg >> 3)) * 128, true, tag);
            ST(5);
            const char* ab = ws + WS_AF + (size_t)tb3 * 2 * 86 * 1024;
            auto issue_a = [&](int c, unsigned B) {
                const int cc = c < 10 ? (c + rot) % 10 : 10;
                const char* src = ab + cc * 8192;
                const unsigned dst = lds0 + 98304 + B;
                const int o2 = c < 10 ? 4096 : 2048;
                dma4lin(src, lin, dst); dma4lin(src + o2, lin, dst + o2);
                dma4lin(src + 86 * 1024, lin, dst + 8192); dma4lin(src + 86 * 1024 + o2, lin, dst + 8192 + o2);
            };
            issue_a(0, 0); issue_a(1, 16384);
            unsigned B = 2 * 16384;
#pragma unroll 1
            for (int c = 0; c < 11; ++c) {
                if (c < 10) lbar<16>(); else lbar<0>();
                if (c + 2 < 11) { issue_a(c + 2, B); B = B == 2 * 16384 ? 0 : B + 16384; }
            }
        } else {
            unsigned B = 0;
#pragma unroll 1
            for (int c = 0; c < 11; ++c) {
                if (c < 7) lbar<63>(); else if (c == 7) lbar<48>(); else if (c == 8) lbar<32>(); else if (c == 9) lbar<16>(); else lbar<0>();
                if (c >= 1 && c + 5 < 11) { issue_w(c + 5, B); B = B == 5 * 16384 ? 0 : B + 16384; }
            }
        }
        bar();
    }
}

extern "C" void kernel_launch(void* const* d_in, const int* in_sizes, int n_in, void* d_out, int out_size, void* d_ws, size_t ws_size, hipStream_t stream) {
    (void)hipFuncSetAttribute((const void*)k_block, hipFuncAttributeMaxDynamicSharedMemorySize, LDS_BYTES);
    k_block<<<256, 640, LDS_BYTES, stream>>>((const char*)d_in[0], (const char*)d_in[1], (const char*)d_in[2], (const char*)d_in[4], (const char*)d_in[5], (const char*)d_in[6],
                                             (char*)d_ws, (char*)d_out);
}
```

```cpp
#include <hip/hip_runtime.h>
#include <hip/hip_bf16.h>
#include <stdint.h>

typedef __bf16 bf16x8 __attribute__((ext_vector_type(8)));
typedef __bf16 bf16x2 __attribute__((ext_vector_type(2)));
typedef short s16x4 __attribute__((ext_vector_type(4)));
typedef short s16x8 __attribute__((ext_vector_type(8)));
typedef float f32x16 __attribute__((ext_vector_type(16)));
typedef float f32x4 __attribute__((ext_vector_type(4)));
typedef float f32x2 __attribute__((ext_vector_type(2)));
typedef unsigned u32x4 __attribute__((ext_vector_type(4)));
typedef unsigned u32x2 __attribute__((ext_vector_type(2)));
#define LAS __attribute__((address_space(3)))
#define DI __device__ __forceinline__

constexpr int DM = 512, FF = 1376;
constexpr float LOG2E = 1.4426950408889634f;
constexpr float QSCALE = 0.25f * LOG2E;
constexpr float LN_EPS = 1e-5f;

constexpr size_t WS_QF = 0, WS_KF = 2u << 20, WS_VF = 4u << 20, WS_OF = 6u << 20, WS_HF = 8u << 20, WS_AF = 10u << 20,
                 WS_ST = 16u << 20, WS_CS = 17u << 20, WS_KM = (17u << 20) + 65536, WS_FLAG = 18u << 20, WS_EPOCH = WS_FLAG + 4 * 256 * 128,
                 WS_G1 = 19u << 20, WS_G2 = WS_G1 + 65536;
constexpr int LDS_BYTES = 163840;
constexpr unsigned SPIN_CAP = 1u << 17;
#define CODE_LINES_ 268
constexpr int CODE_LINES = CODE_LINES_;

#define MFMA(a, b, c) __builtin_amdgcn_mfma_f32_32x32x16_bf16(a, b, c, 0, 0, 0)
#define SB() __builtin_amdgcn_sched_barrier(0)
DI void cbar() { asm volatile("s_waitcnt lgkmcnt(0)\n\ts_barrier" ::: "memory"); }
DI void pbar() { asm volatile("s_waitcnt vmcnt(0) lgkmcnt(0)\n\ts_barrier" ::: "memory"); }
template <int N> DI void lbar() { asm volatile("s_waitcnt vmcnt(%0)\n\ts_barrier" ::"i"(N > 63 ? 63 : N) : "memory"); }
DI void bar() { asm volatile("s_barrier" ::: "memory"); }

DI void dma4(const void* sbase, unsigned v0, unsigned v1, unsigned v2, unsigned v3, unsigned dst) {
    asm volatile("s_mov_b32 m0, %5\n\ts_nop 0\n\tglobal_load_lds_dwordx4 %0, %4 sc1\n\tglobal_load_lds_dwordx4 %1, %4 offset:1024 sc1\n\t"
                 "global_load_lds_dwordx4 %2, %4 offset:2048 sc1\n\tglobal_load_lds_dwordx4 %3, %4 offset:3072 sc1"
                 :: "v"(v0), "v"(v1), "v"(v2), "v"(v3), "s"(sbase), "s"(dst) : "memory");
}
DI void dma4lin(const void* sbase, unsigned v, unsigned dst) { dma4(sbase, v, v, v, v, dst); }
DI void dma2lin(const void* sbase, unsigned v, unsigned dst) {
    asm volatile("s_mov_b32 m0, %2\n\ts_nop 0\n\tglobal_load_lds_dwordx4 %0, %1 sc1\n\tglobal_load_lds_dwordx4 %0, %1 offset:1024 sc1" :: "v"(v), "s"(sbase), "s"(dst) : "memory");
}
DI void dma1dw(const void* sbase, unsigned v, unsigned dst) {
    asm volatile("s_mov_b32 m0, %2\n\ts_nop 0\n\tglobal_load_lds_dword %0, %1 sc1" :: "v"(v), "s"(sbase), "s"(dst) : "memory");
}
DI void gtouch(const void* sbase, unsigned voff) {
    asm volatile("global_load_lds_dword %0, %1" :: "v"(voff), "s"(sbase) : "memory");
}
DI unsigned gld32_wait(const void* sbase, unsigned voff) {
    unsigned r; asm volatile("global_load_dword %0, %1, %2 sc1\n\ts_waitcnt vmcnt(0)" : "=&v"(r) : "v"(voff), "s"(sbase) : "memory"); return r;
}
DI void gst128(void* sbase, unsigned voff, u32x4 v) { asm volatile("global_store_dwordx4 %0, %1, %2 sc1\n\ts_nop 1" ::"v"(voff), "v"(v), "s"(sbase) : "memory"); }
DI void gst64(void* sbase, unsigned voff, u32x2 v) { asm volatile("global_store_dwordx2 %0, %1, %2 sc1\n\ts_nop 1" ::"v"(voff), "v"(v), "s"(sbase) : "memory"); }
DI void gst32(void* sbase, unsigned voff, unsigned v) { asm volatile("global_store_dword %0, %1, %2 sc1\n\ts_nop 1" ::"v"(voff), "v"(v), "s"(sbase) : "memory"); }
#define VWAIT_(N) asm volatile("s_waitcnt vmcnt(" #N ")" ::: "memory")
#define VWAIT(N) VWAIT_(N)
#define PIN(x) asm volatile("" : "+v"(x))
DI unsigned rfl(unsigned x) { return __builtin_amdgcn_readfirstlane(x); }
DI unsigned cvtpk(float lo, float hi) {
    f32x2 v = {lo, hi};
    bf16x2 b = __builtin_convertvector(v, bf16x2);
    return __builtin_bit_cast(unsigned, b);
}
DI float bflo(unsigned u) { return __uint_as_float(u << 16); }
DI float bfhi(unsigned u) { return __uint_as_float(u & 0xffff0000u); }
DI bf16x8 lds_b128(unsigned a) { return *(LAS const bf16x8*)(uintptr_t)a; }
typedef short v4i16_t __attribute__((ext_vector_type(4)));
DI s16x4 lds_tr(unsigned a) { return __builtin_bit_cast(s16x4, __builtin_amdgcn_ds_read_tr16_b64_v4i16((LAS v4i16_t*)(uintptr_t)a)); }
DI bf16x8 lds_trfrag(unsigned a, int off2) {
    s16x4 x = lds_tr(a), y = lds_tr(a + off2);
    s16x8 r = __builtin_shufflevector(x, y, 0, 1, 2, 3, 4, 5, 6, 7);
    return __builtin_bit_cast(bf16x8, r);
}
DI float dot2(unsigned a, unsigned b, float c) {
    return __builtin_amdgcn_fdot2_f32_bf16(__builtin_bit_cast(bf16x2, a), __builtin_bit_cast(bf16x2, b), c, false);
}
DI float other_half(float v) {
    auto r = __builtin_amdgcn_permlane32_swap(__float_as_uint(v), __float_as_uint(v), false, false);
    return __uint_as_float((threadIdx.x & 32) ? r[0] : r[1]);
}
DI float sum_halves(float v) {
    auto r = __builtin_amdgcn_permlane32_swap(__float_as_uint(v), __float_as_uint(v), false, false);
    return __uint_as_float(r[0]) + __uint_as_float(r[1]);
}
template <bool ALL> DI float dpp_max(float v) {
#define DPPMAX(ctrl, rm) v = fmaxf(v, __int_as_float(__builtin_amdgcn_update_dpp(0, __float_as_int(v), ctrl, rm, 0xf, true)))
    DPPMAX(0x111, 0xf); DPPMAX(0x112, 0xf); DPPMAX(0x114, 0xf); DPPMAX(0x118, 0xf); DPPMAX(0x142, 0xa);
    if (ALL) DPPMAX(0x143, 0xc);
    return v;
}
template <int CTRL> DI float dpp_add(float v) { return v + __int_as_float(__builtin_amdgcn_update_dpp(0, __float_as_int(v), CTRL, 0xf, 0xf, true)); }
DI u32x4 swap4(unsigned p0, unsigned p1, unsigned p2, unsigned p3) {
    auto s0 = __builtin_amdgcn_permlane32_swap(p0, p2, false, false);
    auto s1 = __builtin_amdgcn_permlane32_swap(p1, p3, false, false);
    u32x4 r = {s0[0], s1[0], s0[1], s1[1]};
    return r;
}
DI u32x4 pack_swap(float a0, float a1, float a2, float a3, float a4, float a5, float a6, float a7) {
    return swap4(cvtpk(a0, a1), cvtpk(a2, a3), cvtpk(a4, a5), cvtpk(a6, a7));
}
constexpr unsigned ONES = 0x3f803f80u;

#define ST(k)
#define ST2(k)

DI void poll(const char* fl, unsigned voff, bool active, unsigned tag) {
    for (unsigned spins = 0; spins < SPIN_CAP; ++spins) {
        unsigned f;
        asm volatile("global_load_dword %0, %1, %2 sc1\n\ts_waitcnt vmcnt(0)" : "=&v"(f) : "v"(voff), "s"(fl) : "memory");
        if (__all(!active || f == tag)) break;
        __builtin_amdgcn_s_sleep(1);
    }
}

struct Ptrs { const char *x, *wqkv, *wo, *wg, *wu, *wd; char* ws; char* out; };

#define P1ROT 0
#define T1W 19
#define P1NB 3
constexpr int P1_BUF = 32768, P1_MISC = P1NB * P1_BUF;

template <bool ISV>
DI void p1_compute(char* ws, int tb, int cb, char* smem, int w, int lane) {
    const int r = lane & 31, hh = lane >> 5;
    const unsigned lds0 = (unsigned)(uintptr_t)smem;
    const int th = w & 1, ct = w >> 1;
    unsigned x0[4], x1[4];
    {
        const int f = (r >> 1) & 7, t0 = 2 * th, t1 = 2 * th + 1;
#pragma unroll
        for (int ks = 0; ks < 4; ++ks) {
            const unsigned sw = ((2 * ks + hh) ^ f) << 4;
            x0[ks] = lds0 + (32 * t0 + r) * 128 + sw; x1[ks] = lds0 + (32 * t1 + r) * 128 + sw;
        }
    }
    const unsigned wro = lds0 + 16384 + hh * 2048 + ((lane & 15) >> 2) * 64 + ((lane >> 4) & 1) * 32 + (lane & 3) * 8 + ct * 256;
    f32x16 acc0 = {}, acc1 = {};
    bf16x8 X0[4], X1[4], W[4];
#define P1_LOAD(S, B, ks) { X0[S] = lds_b128(x0[ks] + (B)); X1[S] = lds_b128(x1[ks] + (B)); W[S] = lds_trfrag(wro + (B) + (ks) * 4096, 1024); }
#define P1_MMA(S)                                                                                                                 \
    {                                                                                                                             \
        acc0 = ISV ? MFMA(X0[S], W[S], acc0) : MFMA(W[S], X0[S], acc0);                                                            \
        acc1 = ISV ? MFMA(X1[S], W[S], acc1) : MFMA(W[S], X1[S], acc1);                                                            \
    }
    cbar();
    ST(11);
    P1_LOAD(0, 0, 0); P1_LOAD(1, 0, 1);
    unsigned B = 0;
#pragma unroll 1
    for (int ch = 0; ch < 8; ++ch) {
        P1_LOAD(2, B, 2); SB(); P1_MMA(0); SB();
        P1_LOAD(3, B, 3); SB(); P1_MMA(1); SB();
        if (ch < 7) {
            cbar();
            B = B == (P1NB - 1) * P1_BUF ? 0 : B + P1_BUF;
            P1_LOAD(0, B, 0);
        }
        SB(); P1_MMA(2); SB();
        if (ch < 7) { P1_LOAD(1, B, 1); }
        SB(); P1_MMA(3); SB();
    }
    ST(12);
    cbar();
    const LAS f32x2* st = (const LAS f32x2*)(smem + P1_MISC);
    const LAS float* csl = (const LAS float*)(smem + P1_MISC + 1024);
    const int sec = cb >> 2, head0 = (cb & 3) * 8 + ct * 2;
    if constexpr (!ISV) {
        f32x4 c4[4];
#pragma unroll
        for (int g = 0; g < 4; ++g) c4[g] = *(LAS const f32x4*)(csl + ct * 32 + 8 * g + 4 * hh);
#pragma unroll
        for (int a = 0; a < 2; ++a) {
            const int tl = 2 * th + a, tile = tb * 4 + tl;
            const f32x16& acc = a ? acc1 : acc0;
            const f32x2 ms = st[tl * 32 + r];
            const float sc = ms[1] * (sec == 0 ? QSCALE : 1.f), msc = -ms[0] * sc;
            float v[16];
#pragma unroll
            for (int i = 0; i < 16; ++i) v[i] = acc[i] * sc + msc * c4[i >> 2][i & 3];
            char* dst = ws + (sec == 0 ? WS_QF : WS_KF) + ((size_t)head0 * 64 + tile) * 1024;
#pragma unroll
            for (int hs = 0; hs < 2; ++hs) {
                const u32x4 o = pack_swap(v[8 * hs], v[8 * hs + 1], v[8 * hs + 2], v[8 * hs + 3], v[8 * hs + 4], v[8 * hs + 5], v[8 * hs + 6], v[8 * hs + 7]);
                gst128(dst + hs * 65536, lane * 16, o);
                if (sec == 1) {
                    float n2 = 0.f;
#pragma unroll
                    for (int e = 0; e < 8; ++e) n2 = fmaf(v[8 * hs + e], v[8 * hs + e], n2);
                    n2 = dpp_max<false>(sum_halves(n2));
                    if (lane == 31) gst32(ws + WS_KM + (head0 + hs) * 256 + tile * 4, 0, __float_as_uint(n2));
                }
            }
        }
    } else {
        const float csn = csl[ct * 32 + r];
#pragma unroll
        for (int a = 0; a < 2; ++a) {
            const int tl = 2 * th + a, tile = tb * 4 + tl;
            const f32x16& acc = a ? acc1 : acc0;
            float v[16];
#pragma unroll
            for (int g = 0; g < 4; ++g) {
                const LAS f32x4* sp = (const LAS f32x4*)(st + tl * 32 + 8 * g + 4 * hh);
                const f32x4 pa = sp[0], pb = sp[1];
                v[4 * g] = (acc[4 * g] - pa[0] * csn) * pa[1]; v[4 * g + 1] = (acc[4 * g + 1] - pa[2] * csn) * pa[3];
                v[4 * g + 2] = (acc[4 * g + 2] - pb[0] * csn) * pb[1]; v[4 * g + 3] = (acc[4 * g + 3] - pb[2] * csn) * pb[3];
            }
            char* dst = ws + WS_VF + ((size_t)head0 * 64 + tile) * 1024;
            const unsigned vo = (r >> 4) * 65536 + hh * 256 + (r & 15) * 16;
#pragma unroll
            for (int s = 0; s < 2; ++s) {
                u32x4 o = {cvtpk(v[8 * s], v[8 * s + 1]), cvtpk(v[8 * s + 2], v[8 * s + 3]), cvtpk(v[8 * s + 4], v[8 * s + 5]), cvtpk(v[8 * s + 6], v[8 * s + 7])};
                gst128(dst + s * 512, vo, o);
            }
        }
    }
}

DI void gst_granule(char* sbase, unsigned voff, float v, unsigned tag) { u32x2 g = {__float_as_uint(v), tag}; gst64(sbase, voff, g); }
typedef const __attribute__((address_space(1))) char* gcptr;
template <int NROW16>
DI void colsum32(gcptr W, unsigned rs, int lane, float (&s)[8]) {
    const unsigned vo = (lane & 15) * rs + (lane >> 4) * 16;
    u32x4 v[NROW16];
#pragma unroll
    for (int i = 0; i < NROW16; ++i) v[i] = *(const __attribute__((address_space(1))) u32x4*)(W + (size_t)i * 16 * rs + vo);
    __builtin_amdgcn_sched_barrier(0);
#pragma unroll
    for (int e = 0; e < 8; ++e) s[e] = 0.f;
#pragma unroll
    for (int i = 0; i < NROW16; ++i)
#pragma unroll
        for (int e = 0; e < 4; ++e) { s[2 * e] += bflo(v[i][e]); s[2 * e + 1] += bfhi(v[i][e]); }
#pragma unroll
    for (int e = 0; e < 8; ++e) s[e] = dpp_add<0x118>(dpp_add<0x114>(dpp_add<0x112>(dpp_add<0x111>(s[e]))));
}
DI void p1_helper(const char* px, const char* pqkv, const char* pg, const char* pu, char* ws, int h, int w, int lane) {
    if (w < 7) {
        const unsigned tag = __hip_atomic_load((const __attribute__((address_space(1))) unsigned*)(ws + WS_EPOCH), __ATOMIC_RELAXED, __HIP_MEMORY_SCOPE_AGENT) + 1;
        const int u = w * 64 + h;
        if (u < 256) {
            const int rl = lane >> 3, seg = lane & 7;
            const char* src = px + (size_t)(u * 8 + rl) * 1024 + seg * 16;
            u32x4 v[8];
#pragma unroll
            for (int i = 0; i < 8; ++i) v[i] = *(const u32x4*)(src + i * 128);
            float s1 = 0.f, s2 = 0.f;
#pragma unroll
            for (int i = 0; i < 8; ++i)
#pragma unroll
                for (int e = 0; e < 4; ++e) { s1 = dot2(v[i][e], ONES, s1); s2 = dot2(v[i][e], v[i][e], s2); }
            s1 = dpp_add<0x114>(dpp_add<0x112>(dpp_add<0x111>(s1))); s2 = dpp_add<0x114>(dpp_add<0x112>(dpp_add<0x111>(s2)));
            const float mean = s1 * (1.f / DM);
            const float rstd = __builtin_amdgcn_rsqf(fmaxf(s2 * (1.f / DM) - mean * mean, 0.f) + LN_EPS);
            if (seg == 7) { gst_granule(ws + WS_G1, (u * 8 + rl) * 16, mean, tag); gst_granule(ws + WS_G1, (u * 8 + rl) * 16 + 8, rstd, tag); }
        } else {
            const int q = u - 256, blk = q >> 2, kq = q & 3;
            float s[8];
            colsum32<8>((gcptr)(pqkv + (size_t)kq * 128 * 3072 + blk * 64), 3072, lane, s);
            if ((lane & 15) == 15) {
#pragma unroll
                for (int e = 0; e < 8; ++e) gst_granule(ws + WS_G2, ((blk * 32 + (lane >> 4) * 8 + e) * 4 + kq) * 8, s[e], tag);
            }
        }
    }
    const int job = w == 7 ? h : (w == 0 && h < 22) ? 64 + h : -1;
    if (job >= 0) {
        const int blk = job < 43 ? job : job - 43;
        const uintptr_t wa = (uintptr_t)pg + (job < 43 ? 0 : (uintptr_t)pu - (uintptr_t)pg);
        float s[8];
        colsum32<32>((gcptr)wa + blk * 64, FF * 2, lane, s);
        if ((lane & 15) == 15) {
            char* dst = ws + WS_CS + (job < 43 ? 0 : FF) * 4;
            const int c0 = blk * 32 + (lane >> 4) * 8;
            u32x4 a = {__float_as_uint(s[0]), __float_as_uint(s[1]), __float_as_uint(s[2]), __float_as_uint(s[3])};
            u32x4 c = {__float_as_uint(s[4]), __float_as_uint(s[5]), __float_as_uint(s[6]), __float_as_uint(s[7])};
            gst128(dst, c0 * 4, a); gst128(dst, c0 * 4 + 16, c);
        }
    }
}

DI unsigned p1_load(const Ptrs& P, int L, int tb, int cb, int rot, int sidx, unsigned lds0, int lane) {
    unsigned vo[4];
    const char* base;
    unsigned cstep, gstep;
    if (L == 0) {
        const int rl = lane >> 3, cs = lane & 7;
#pragma unroll
        for (int i = 0; i < 4; ++i) vo[i] = (8 * i + rl) * 1024 + ((cs ^ ((4 * (i & 1) + (rl >> 1)) & 7)) << 4) - i * 1024;
        base = P.x + (size_t)tb * 128 * 1024; cstep = 128; gstep = 4 * 8192;
    } else {
        const int nb = lane >> 4, q = (lane >> 2) & 3, c = lane & 3;
#pragma unroll
        for (int i = 0; i < 4; ++i) vo[i] = (4 * i + q) * 3072 + nb * 64 + c * 16 - i * 1024;
        base = P.wqkv + cb * 256; cstep = 64 * 3072; gstep = 16 * 3072;
    }
    auto issue = [&](int c, unsigned B) {
        const char* src = base + (size_t)((c + rot) & 7) * cstep;
        const unsigned dst = lds0 + B + L * 16384;
#pragma unroll
        for (int g = 0; g < 4; ++g) dma4(src + (size_t)g * gstep, vo[0], vo[1], vo[2], vo[3], dst + g * 4096);
    };
    issue(0, 0);
    {
        asm volatile("s_mov_b32 m0, %0\n\ts_nop 0" ::"s"(lds0 + LDS_BYTES - 256) : "memory");
        if (L == 0) {
#pragma unroll
            for (int i = 0; i < 3; ++i) { int ln = sidx * 171 + i * 64 + lane; ln = ln > 4095 ? 4095 : ln; gtouch(P.x + (size_t)(tb & ~3) * 128 * 1024, ln * 128); }
        } else {
#pragma unroll
            for (int i = 0; i < 4; ++i) { const int ln = sidx * 256 + i * 64 + lane; gtouch(P.wqkv + (cb / 6) * 1536, (ln / 12) * 3072 + (ln % 12) * 128); }
        }
    }
#pragma unroll
    for (int c = 1; c < P1NB - 1; ++c) issue(c, c * P1_BUF);
    unsigned B = (P1NB - 1) * P1_BUF;
    constexpr int LA = P1NB - 1;
#pragma unroll 1
    for (int c = 0; c < 8; ++c) {
        const int y = 7 - c < LA - 1 ? 7 - c : LA - 1;
        if (c == 0) lbar<16 * (LA - 1) + T1W - 16>(); else if (y == 3) lbar<48>(); else if (y == 2) lbar<32>(); else if (y == 1) lbar<16>(); else lbar<0>();
        if (c + LA < 8) { issue(c + LA, B); B = B == (P1NB - 1) * P1_BUF ? 0 : B + P1_BUF; }
    }
    const unsigned tag = rfl(gld32_wait(P.ws + WS_EPOCH, 0)) + 1;
    if (L == 1) {
        u32x4 a, b, c0, c1, c2, c3;
        for (unsigned spins = 0; spins < SPIN_CAP; ++spins) {
            asm volatile("global_load_dwordx4 %0, %6, %7 sc1\n\tglobal_load_dwordx4 %1, %6, %7 offset:16 sc1\n\t"
                         "global_load_dwordx4 %2, %8, %9 sc1\n\tglobal_load_dwordx4 %3, %8, %9 offset:16 sc1\n\t"
                         "global_load_dwordx4 %4, %8, %9 offset:32 sc1\n\tglobal_load_dwordx4 %5, %8, %9 offset:48 sc1\n\ts_waitcnt vmcnt(0)"
                         : "=&v"(a), "=&v"(b), "=&v"(c0), "=&v"(c1), "=&v"(c2), "=&v"(c3)
                         : "v"(lane * 32), "s"(P.ws + WS_G1 + tb * 128 * 16), "v"(lane * 64), "s"(P.ws + WS_G2 + cb * 128 * 32) : "memory");
            if (__all(a[1] == tag && a[3] == tag && b[1] == tag && b[3] == tag && c0[1] == tag && c0[3] == tag && c1[1] == tag && c1[3] == tag &&
                      c2[1] == tag && c2[3] == tag && c3[1] == tag && c3[3] == tag)) break;
            __builtin_amdgcn_s_sleep(1);
        }
        { char* ws = P.ws; ST(13); }
        u32x4 ms = {a[0], a[2], b[0], b[2]};
        u32x2 cs = {__float_as_uint(__uint_as_float(c0[0]) + __uint_as_float(c0[2]) + __uint_as_float(c1[0]) + __uint_as_float(c1[2])),
                    __float_as_uint(__uint_as_float(c2[0]) + __uint_as_float(c2[2]) + __uint_as_float(c3[0]) + __uint_as_float(c3[2]))};
        *(LAS u32x4*)(uintptr_t)(lds0 + P1_MISC + lane * 16) = ms;
        *(LAS u32x2*)(uintptr_t)(lds0 + P1_MISC + 1024 + lane * 8) = cs;
        asm volatile("s_waitcnt lgkmcnt(0)" ::: "memory");
    }
    bar();
    return tag;
}

constexpr int P2_X = 131072 + 2048;
constexpr int P2_KM = P2_X + 8 * 2560;
constexpr float SAFE_RANGE = 80.f;

DI void p2_compute(char* ws, char* smem, int head, int qb, int w, int lane) {
    const int r = lane & 31, hh = lane >> 5;
    const unsigned lds0 = (unsigned)(uintptr_t)smem;
    const int qg = w & 3, kh = w >> 2;
    const int la = qg * 2 + kh, lb = qg * 2 + (1 - kh);
    cbar();
    ST(13);
    const u32x4 qa = *(LAS const u32x4*)(uintptr_t)(lds0 + P2_X + la * 1024 + lane * 16), qbv = *(LAS const u32x4*)(uintptr_t)(lds0 + P2_X + lb * 1024 + lane * 16);
    const float kmax2 = __int_as_float(__builtin_amdgcn_readlane(__float_as_int(dpp_max<true>(*(LAS const float*)(uintptr_t)(lds0 + P2_KM + lane * 4))), 63));
    float qa2 = 0.f, qb2 = 0.f;
#pragma unroll
    for (int e = 0; e < 4; ++e) { qa2 = dot2(qa[e], qa[e], qa2); qb2 = dot2(qbv[e], qbv[e], qb2); }
    const float bnda = __builtin_amdgcn_sqrtf(sum_halves(qa2) * kmax2) * 1.01f, bndb = __builtin_amdgcn_sqrtf(sum_halves(qb2) * kmax2) * 1.01f;
    const bool fast = __all(2.f * bnda <= SAFE_RANGE && 2.f * bndb <= SAFE_RANGE);
    const unsigned ka = lds0 + kh * 32768 + lane * 16, va = lds0 + 65536 + kh * 32768 + hh * 256 + (r & 15) * 16;
    const unsigned xw = lds0 + P2_X + w * 2560 + lane * 16, xr = lds0 + P2_X + (w ^ 4) * 2560 + lane * 16;
    float o[9], ma;
    ST(5);
    if (fast) {
        ma = bnda;
        unsigned kp = ka, vp = va, cnt;
#define EXP(r) "v_exp_f32 v" #r ", v" #r "\n\t"
#define CVT(d, a, b) "v_cvt_pk_bf16_f32 v" #d ", v" #a ", v" #b "\n\t"
#define MSQ(SN, KN, Q, C) "v_mfma_f32_32x32x16_bf16 " SN ", " KN ", " Q ", " C "\n\t"
#define MPO(O, V, P) "v_mfma_f32_32x32x16_bf16 " O ", " V ", " P ", " O "\n\t"
#define RDV(VN0, VN1, VOFF) "s_mov_b64 exec, %[msk]\n\t" "ds_read_b128 " VN0 ", %[vp] offset:" #VOFF "\n\t" "ds_read_b128 " VN1 ", %[vp] offset:" #VOFF "+512\n\t" "s_mov_b64 exec, -1\n\t"
#define RDK(KNN, KOFF) "ds_read_b128 " KNN ", %[kp] offset:" #KOFF "\n\t"
#define STEP(PRE, c0, c1, c2, c3, c4, c5, c6, c7, c8, c9, c10, c11, c12, c13, c14, c15, p14, p15, SN, KN, Q, CC, O, OP, VC0, VP1)  \
        EXP(c0) EXP(c1) CVT(127, p14, p15) EXP(c2) EXP(c3) MPO(OP, VP1, "v[124:127]") PRE                                    \
        CVT(120, c0, c1) EXP(c4) EXP(c5) CVT(121, c2, c3) EXP(c6)                                                            \
        "s_waitcnt lgkmcnt(3)\n\t" MSQ(SN, KN, Q, CC)                                                                        \
        EXP(c7) CVT(122, c4, c5) EXP(c8) EXP(c9) CVT(123, c6, c7) EXP(c10) EXP(c11)                                          \
        MPO(O, VC0, "v[120:123]")                                                                                            \
        CVT(124, c8, c9) EXP(c12) EXP(c13) CVT(125, c10, c11) EXP(c14) EXP(c15) CVT(126, c12, c13)
#define SA_ 32, 33, 34, 35, 36, 37, 38, 39, 40, 41, 42, 43, 44, 45, 46, 47
#define SB_ 48, 49, 50, 51, 52, 53, 54, 55, 56, 57, 58, 59, 60, 61, 62, 63
#define CALL(M, ...) M(__VA_ARGS__)
#define TE(KOFF, VOFF) CALL(STEP, RDV("v[112:115]", "v[116:119]", VOFF), SA_, 62, 63, "v[48:63]", "v[96:99]", "%[qb]", "v[144:159]", "v[64:79]", "v[80:95]", "v[104:107]", "v[116:119]") \
                       CALL(STEP, RDK("v[96:99]", KOFF), SB_, 46, 47, "v[32:47]", "v[100:103]", "%[qa]", "v[128:143]", "v[80:95]", "v[64:79]", "v[104:107]", "v[108:111]")
#define TO(KOFF, VOFF) CALL(STEP, RDV("v[104:107]", "v[108:111]", VOFF), SA_, 62, 63, "v[48:63]", "v[100:103]", "%[qb]", "v[144:159]", "v[64:79]", "v[80:95]", "v[112:115]", "v[108:111]") \
                       CALL(STEP, RDK("v[100:103]", KOFF), SB_, 46, 47, "v[32:47]", "v[96:99]", "%[qa]", "v[128:143]", "v[80:95]", "v[64:79]", "v[112:115]", "v[116:119]")
#define M64(a, b, c, d) "v_mov_b64 v[" #a ":" #b "], v[" #c ":" #d "]\n\t"
#define MOV4(a, b, c, d, src) "v_mov_b32 v" #a ", " src "\n\tv_mov_b32 v" #b ", " src "\n\tv_mov_b32 v" #c ", " src "\n\tv_mov_b32 v" #d ", " src "\n\t"
        asm volatile(
            "s_setprio 3\n\t"
            "ds_read_b128 v[96:99], %[kp]\n\t"
            "v_mov_b32 v128, %[na]\n\tv_mov_b32 v129, %[na]\n\tv_mov_b32 v144, %[nb]\n\tv_mov_b32 v145, %[nb]\n\t"
            "v_mov_b32 v104, %[one]\n\tv_mov_b32 v105, %[one]\n\tv_mov_b64 v[64:65], 0\n\t"
            M64(106, 107, 104, 105) M64(108, 109, 104, 105) M64(110, 111, 104, 105) M64(112, 113, 104, 105) M64(114, 115, 104, 105) M64(116, 117, 104, 105) M64(118, 119, 104, 105)
            "s_mov_b64 exec, %[msk]\n\t"
            "ds_read_b128 v[104:107], %[vp]\n\t"
            "ds_read_b128 v[108:111], %[vp] offset:512\n\t"
            "s_mov_b64 exec, -1\n\t"
            "ds_read_b128 v[100:103], %[kp] offset:1024\n\t"
            M64(130, 131, 128, 129) M64(132, 133, 128, 129) M64(134, 135, 128, 129) M64(136, 137, 128, 129) M64(138, 139, 128, 129) M64(140, 141, 128, 129) M64(142, 143, 128, 129)
            "s_waitcnt lgkmcnt(3)\n\t"
            "s_nop 1\n\t"
            "v_mfma_f32_32x32x16_bf16 v[32:47], v[96:99], %[qa], v[128:143]\n\t"
            M64(146, 147, 144, 145) M64(148, 149, 144, 145) M64(150, 151, 144, 145) M64(152, 153, 144, 145) M64(154, 155, 144, 145) M64(156, 157, 144, 145) M64(158, 159, 144, 145)
            M64(66, 67, 64, 65) M64(68, 69, 64, 65) M64(70, 71, 64, 65) M64(72, 73, 64, 65) M64(74, 75, 64, 65) M64(76, 77, 64, 65) M64(78, 79, 64, 65)
            M64(80, 81, 64, 65) M64(82, 83, 64, 65) M64(84, 85, 64, 65) M64(86, 87, 64, 65) M64(88, 89, 64, 65) M64(90, 91, 64, 65) M64(92, 93, 64, 65) M64(94, 95, 64, 65)
            M64(62, 63, 64, 65) M64(124, 125, 64, 65) M64(126, 127, 64, 65)
            "s_mov_b32 %[cnt], 0\n\t"
            "L_att_%=:\n\t"
            TE(2048, 1024) TO(3072, 2048) TE(4096, 3072) TO(5120, 4096)
            "v_add_u32 %[kp], 0x1000, %[kp]\n\t"
            "v_add_u32 %[vp], 0x1000, %[vp]\n\t"
            "s_cmp_lg_u32 %[cnt], 0\n\t"
            "s_cbranch_scc1 L_att_nb_%=\n\t"
            "s_barrier\n\t"
            "L_att_nb_%=:\n\t"
            "s_add_u32 %[cnt], %[cnt], 1\n\t"
            "s_cmp_eq_u32 %[cnt], 3\n\ts_cbranch_scc0 L_p1_%=\n\ts_setprio 2\n\tL_p1_%=:\n\t"
            "s_cmp_eq_u32 %[cnt], 6\n\ts_cbranch_scc0 L_p2_%=\n\ts_setprio 1\n\tL_p2_%=:\n\t"
            "s_cmp_eq_u32 %[cnt], 7\n\ts_cbranch_scc0 L_p3_%=\n\ts_setprio 0\n\tL_p3_%=:\n\t"
            "s_cmp_lt_u32 %[cnt], 8\n\t"
            "s_cbranch_scc1 L_att_%=\n\t"
            CVT(127, 62, 63)
            "s_nop 1\n\t"
            MPO("v[80:95]", "v[116:119]", "v[124:127]")
            "s_waitcnt lgkmcnt(0)\n\t"
            "s_nop 15\n\t"
            "v_mov_b32 v89, %[bb]\n\t"
            "v_mov_b32 %[o0], v64\n\tv_mov_b32 %[o1], v65\n\tv_mov_b32 %[o2], v66\n\tv_mov_b32 %[o3], v67\n\t"
            "v_mov_b32 %[o4], v68\n\tv_mov_b32 %[o5], v69\n\tv_mov_b32 %[o6], v70\n\tv_mov_b32 %[o7], v71\n\tv_mov_b32 %[o8], v72\n\t"
            "ds_write_b128 %[xw], v[80:83]\n\t"
            "ds_write_b128 %[xw], v[84:87] offset:1024\n\t"
            "ds_write_b64 %[xw2], v[88:89]\n\t"
            : [o0] "=&v"(o[0]), [o1] "=&v"(o[1]), [o2] "=&v"(o[2]), [o3] "=&v"(o[3]), [o4] "=&v"(o[4]), [o5] "=&v"(o[5]), [o6] "=&v"(o[6]), [o7] "=&v"(o[7]), [o8] "=&v"(o[8]),
              [kp] "+v"(kp), [vp] "+v"(vp), [cnt] "=&s"(cnt)
            : [qa] "v"(qa), [qb] "v"(qbv), [na] "v"(-bnda), [nb] "v"(-bndb), [bb] "v"(bndb), [msk] "s"(0x0000ffff0000ffffull), [xw] "v"(xw), [xw2] "v"(xw + 2048 - lane * 8), [one] "v"((r == 16 || r == 20) ? ONES : 0u)
            : "memory", "scc",
              "v32", "v33", "v34", "v35", "v36", "v37", "v38", "v39", "v40", "v41", "v42", "v43", "v44", "v45", "v46", "v47",
              "v48", "v49", "v50", "v51", "v52", "v53", "v54", "v55", "v56", "v57", "v58", "v59", "v60", "v61", "v62", "v63",
              "v64", "v65", "v66", "v67", "v68", "v69", "v70", "v71", "v72", "v73", "v74", "v75", "v76", "v77", "v78", "v79",
              "v80", "v81", "v82", "v83", "v84", "v85", "v86", "v87", "v88", "v89", "v90", "v91", "v92", "v93", "v94", "v95",
              "v96", "v97", "v98", "v99", "v100", "v101", "v102", "v103", "v104", "v105", "v106", "v107", "v108", "v109", "v110", "v111",
              "v112", "v113", "v114", "v115", "v116", "v117", "v118", "v119", "v120", "v121", "v122", "v123", "v124", "v125", "v126", "v127",
              "v128", "v129", "v130", "v131", "v132", "v133", "v134", "v135", "v136", "v137", "v138", "v139", "v140", "v141", "v142", "v143",
              "v144", "v145", "v146", "v147", "v148", "v149", "v150", "v151", "v152", "v153", "v154", "v155", "v156", "v157", "v158", "v159");
    } else {
        bar();
        const u32x4 ones = {ONES, ONES, ONES, ONES};
        const f32x16 zero = {};
#pragma unroll 1
        for (int sl = 1; sl >= 0; --sl) {
            const bf16x8 qf = __builtin_bit_cast(bf16x8, sl ? qbv : qa);
            bf16x8 v0 = __builtin_bit_cast(bf16x8, ones), v1 = v0, kf;
            f32x16 oo = {};
            float m = -INFINITY;
#pragma unroll 1
            for (int t = 0; t < 32; ++t) {
                asm volatile("ds_read_b128 %0, %3\n\ts_mov_b64 exec, %5\n\tds_read_b128 %1, %4\n\tds_read_b128 %2, %4 offset:512\n\ts_mov_b64 exec, -1\n\ts_waitcnt lgkmcnt(0)"
                             : "=&v"(kf), "+v"(v0), "+v"(v1) : "v"(ka + t * 1024), "v"(va + t * 1024), "s"(0x0000ffff0000ffffull) : "memory");
                const f32x16 s = MFMA(kf, qf, zero);
                float tm = s[0];
#pragma unroll
                for (int i = 1; i < 16; ++i) tm = fmaxf(tm, s[i]);
                tm = fmaxf(tm, other_half(tm));
                const float mn = fmaxf(m, tm);
                const float alpha = __builtin_amdgcn_exp2f(m - mn);
                m = mn;
#pragma unroll
                for (int i = 0; i < 16; ++i) oo[i] *= alpha;
                u32x4 p0, p1;
#pragma unroll
                for (int i = 0; i < 4; ++i) p0[i] = cvtpk(__builtin_amdgcn_exp2f(s[2 * i] - m), __builtin_amdgcn_exp2f(s[2 * i + 1] - m));
#pragma unroll
                for (int i = 0; i < 4; ++i) p1[i] = cvtpk(__builtin_amdgcn_exp2f(s[8 + 2 * i] - m), __builtin_amdgcn_exp2f(s[9 + 2 * i] - m));
                oo = MFMA(v0, __builtin_bit_cast(bf16x8, p0), oo);
                oo = MFMA(v1, __builtin_bit_cast(bf16x8, p1), oo);
            }
            if (sl == 1) {
                f32x4 t0 = {oo[0], oo[1], oo[2], oo[3]}, t1 = {oo[4], oo[5], oo[6], oo[7]};
                f32x2 t2 = {oo[8], m};
                *(LAS f32x4*)(uintptr_t)xw = t0; *(LAS f32x4*)(uintptr_t)(xw + 1024) = t1; *(LAS f32x2*)(uintptr_t)(xw + 2048 - lane * 8) = t2;
            } else {
#pragma unroll
                for (int i = 0; i < 9; ++i) o[i] = oo[i];
                ma = m;
            }
        }
    }
    ST(14);
    cbar();
    {
        const f32x4 t0 = *(LAS const f32x4*)(uintptr_t)xr, t1 = *(LAS const f32x4*)(uintptr_t)(xr + 1024);
        const f32x2 t2 = *(LAS const f32x2*)(uintptr_t)(xr + 2048 - lane * 8);
        const float mo = t2[1], mn = fmaxf(ma, mo);
        const float fa = __builtin_amdgcn_exp2f(ma - mn), fo = __builtin_amdgcn_exp2f(mo - mn);
        const float inv = __builtin_amdgcn_rcpf(o[8] * fa + t2[0] * fo);
        const float ia = fa * inv, io = fo * inv;
        const u32x4 ov = pack_swap(o[0] * ia + t0[0] * io, o[1] * ia + t0[1] * io, o[2] * ia + t0[2] * io, o[3] * ia + t0[3] * io,
                                   o[4] * ia + t1[0] * io, o[5] * ia + t1[1] * io, o[6] * ia + t1[2] * io, o[7] * ia + t1[3] * io);
        gst128(ws + WS_OF + ((size_t)(qb * 8 + la) * 32 + head) * 1024, lane * 16, ov);
    }
}

constexpr int P3_X = 153600;
template <int MODE>
DI void p35_compute(const Ptrs& P, char* smem, int tb, int cb, int w, int lane, u32x4& hkeep) {
    constexpr int NFULL = MODE == 0 ? 4 : 10;
    constexpr int A0 = MODE == 0 ? 65536 : 98304, NA = MODE == 0 ? 4 : 3, NW = MODE == 0 ? 4 : 6, XA = MODE == 0 ? 131072 : 147456;
    char* ws = P.ws;
    const int r = lane & 31, hh = lane >> 5;
    const unsigned lds0 = (unsigned)(uintptr_t)smem;
    const int ta = w & 1, cj = (w >> 1) & 1, kh = w >> 2;
    const int tile = tb * 2 + ta, token = tile * 32 + r, n0 = cb * 64 + cj * 32 + kh * 16;
    u32x2 res[2];
    const unsigned aro = lds0 + A0 + ta * 8192 + lane * 16 + kh * 4096;
    const unsigned wro = lds0 + hh * 1024 + ((lane & 15) >> 2) * 64 + ((lane >> 4) & 1) * 32 + (lane & 3) * 8 + cj * 256 + kh * 8192;
    f32x16 acc = {};
    bf16x8 A[4], Wf[4];
#define P35_LD(S, BA, BW, ks) { A[S] = lds_b128(aro + (BA) + (ks) * 1024); Wf[S] = lds_trfrag(wro + (BW) + (ks) * 2048, 512); }
    cbar();
    ST(MODE == 0 ? 7 : 8);
    P35_LD(0, 0, 0, 0); P35_LD(1, 0, 0, 1);
    unsigned BA = 0, BW = 0;
#pragma unroll 1
    for (int ch = 0; ch < NFULL; ++ch) {
        P35_LD(2, BA, BW, 2); SB(); acc = MFMA(Wf[0], A[0], acc); SB();
        P35_LD(3, BA, BW, 3); SB(); acc = MFMA(Wf[1], A[1], acc); SB();
        if (MODE == 1 || ch < NFULL - 1) {
            cbar();
            BA = BA == (NA - 1) * 16384 ? 0 : BA + 16384; BW = BW == (NW - 1) * 16384 ? 0 : BW + 16384;
            if (ch < NFULL - 1) { P35_LD(0, BA, BW, 0); }
        }
        SB(); acc = MFMA(Wf[2], A[2], acc); SB();
        if (ch < NFULL - 1) { P35_LD(1, BA, BW, 1); }
        SB(); acc = MFMA(Wf[3], A[3], acc); SB();
    }
    if constexpr (MODE == 1) {
#pragma unroll
        for (int j = 0; j < 3; ++j) {
            const bf16x8 af = lds_b128(aro + BA - kh * 4096 + (kh * 3 + j) * 1024);
            const bf16x8 wf = lds_trfrag(wro + BW - kh * 8192 + (kh * 3 + j) * 2048, 512);
            acc = MFMA(wf, af, acc);
        }
    }
    ST(MODE == 0 ? 9 : 0);
    {
        LAS f32x4* xw = (LAS f32x4*)(smem + XA + w * 2048 + lane * 16);
        f32x4 s0, s1;
#pragma unroll
        for (int e = 0; e < 4; ++e) { s0[e] = kh ? acc[e] : acc[8 + e]; s1[e] = kh ? acc[4 + e] : acc[12 + e]; }
        xw[0] = s0; xw[64] = s1;
    }
    cbar();
    float v[8];
    {
        LAS const f32x4* xr = (LAS const f32x4*)(smem + XA + (w ^ 4) * 2048 + lane * 16);
        const f32x4 s0 = xr[0], s1 = xr[64];
        if constexpr (MODE == 0) {
            const int row = ta * 32 + r;
#pragma unroll
            for (int g = 0; g < 2; ++g)
                res[g] = *(LAS const u32x2*)(smem + P3_X + row * 128 + (((cj * 4 + kh * 2 + g) ^ ((row >> 1) & 7)) << 4) + hh * 8);
        } else { res[0][0] = hkeep[0]; res[0][1] = hkeep[1]; res[1][0] = hkeep[2]; res[1][1] = hkeep[3]; }
#pragma unroll
        for (int e = 0; e < 4; ++e) { v[e] = (kh ? acc[8 + e] : acc[e]) + s0[e]; v[4 + e] = (kh ? acc[12 + e] : acc[4 + e]) + s1[e]; }
#pragma unroll
        for (int g = 0; g < 2; ++g) {
            v[4 * g] += bflo(res[g][0]); v[4 * g + 1] += bfhi(res[g][0]); v[4 * g + 2] += bflo(res[g][1]); v[4 * g + 3] += bfhi(res[g][1]);
        }
    }
    const unsigned p0 = cvtpk(v[0], v[1]), p1 = cvtpk(v[2], v[3]), p2 = cvtpk(v[4], v[5]), p3 = cvtpk(v[6], v[7]);
    const u32x4 o = swap4(p0, p1, p2, p3);
    if constexpr (MODE == 0) {
        hkeep[0] = p0; hkeep[1] = p1; hkeep[2] = p2; hkeep[3] = p3;
        gst128(ws + WS_HF + ((size_t)tile * 32 + (n0 >> 4)) * 1024, lane * 16, o);
        float s1 = 0.f, s2 = 0.f;
#pragma unroll
        for (int e = 0; e < 4; ++e) { s1 = dot2(o[e], ONES, s1); s2 = dot2(o[e], o[e], s2); }
        s1 = sum_halves(s1); s2 = sum_halves(s2);
        LAS f32x2* sl = (LAS f32x2*)(smem + XA + 8 * 2048);
        if (lane < 32) { f32x2 sv = {s1, s2}; sl[(ta * 32 + r) * 4 + cj * 2 + kh] = sv; }
        cbar();
        if (w == 0) {
            const f32x4 p = *(LAS const f32x4*)(sl + lane * 4), q = *(LAS const f32x4*)(sl + lane * 4 + 2);
            u32x2 sv = {__float_as_uint(p[0] + p[2] + q[0] + q[2]), __float_as_uint(p[1] + p[3] + q[1] + q[3])};
            gst64(ws + WS_ST + (size_t)tb * 64 * 64 + cb * 8, lane * 64, sv);
        }
    } else {
        *(u32x4*)(P.out + (size_t)token * 1024 + (n0 + 8 * hh) * 2) = o;
    }
}

constexpr int P4_ST = 147456, P4_CS = P4_ST + 8192;
template <int NB>
DI void p4_compute(char* ws, char* smem, int tb, int cg, int w, int lane) {
    const int r = lane & 31, hh = lane >> 5, i = w & 3, j0 = NB == 2 ? 0 : 2;
    const unsigned lds0 = (unsigned)(uintptr_t)smem;
    const int tile = tb * 4 + i;
    const unsigned hro = lds0 + 98304 + i * 4096 + lane * 16;
    const unsigned wro = lds0 + hh * 1536 + ((lane & 15) >> 2) * 64 + ((lane >> 4) & 1) * 32 + (lane & 3) * 8 + j0 * 256;
    f32x16 ag[NB], au[NB];
#pragma unroll
    for (int j = 0; j < NB; ++j) { ag[j] = f32x16{}; au[j] = f32x16{}; }
    bf16x8 H[4], G[4][NB], U[4][NB];
#define P4_LOAD(S, BH, BW, ks)                                                                                                    \
    {                                                                                                                             \
        H[S] = lds_b128(hro + (BH) + (ks) * 1024);                                                                                 \
        _Pragma("unroll") for (int j = 0; j < NB; ++j) { G[S][j] = lds_trfrag(wro + (BW) + (ks) * 3072 + j * 256, 768); U[S][j] = lds_trfrag(wro + (BW) + 12288 + (ks) * 3072 + j * 256, 768); } \
    }
#define P4_MMA(S) { _Pragma("unroll") for (int j = 0; j < NB; ++j) { ag[j] = MFMA(G[S][j], H[S], ag[j]); au[j] = MFMA(U[S][j], H[S], au[j]); } }
    cbar();
    ST(6);
    P4_LOAD(0, 0, 0, 0); P4_LOAD(1, 0, 0, 1);
    unsigned BH = 0, BW = 0;
#pragma unroll 1
    for (int ch = 0; ch < 8; ++ch) {
        P4_LOAD(2, BH, BW, 2); SB(); P4_MMA(0); SB();
        P4_LOAD(3, BH, BW, 3); SB(); P4_MMA(1); SB();
        cbar();
        if (ch < 7) {
            BH = BH == 2 * 16384 ? 0 : BH + 16384; BW = BW == 3 * 24576 ? 0 : BW + 24576;
            P4_LOAD(0, BH, BW, 0);
        }
        SB(); P4_MMA(2); SB();
        if (ch < 7) { P4_LOAD(1, BH, BW, 1); }
        SB(); P4_MMA(3); SB();
    }
    ST(15);
    float mean, rstd;
    {
        float s1 = 0.f, s2 = 0.f;
        const LAS f32x4* sp = (const LAS f32x4*)(smem + P4_ST + (i * 32 + r) * 64);
#pragma unroll
        for (int k = 0; k < 4; ++k) { const f32x4 v = sp[k]; s1 += v[0] + v[2]; s2 += v[1] + v[3]; }
        mean = s1 * (1.f / DM);
        rstd = __builtin_amdgcn_rsqf(fmaxf(s2 * (1.f / DM) - mean * mean, 0.f) + LN_EPS);
    }
    constexpr float C1 = -2.f * 0.7978845608028654f * LOG2E, C2 = C1 * 0.044715f;
    const float k1 = C1 * rstd, k2 = C2 * rstd * rstd * rstd, k3 = __builtin_amdgcn_rcpf(rstd * rstd);
    const f32x2 nm2 = {-mean, -mean}, k12 = {k1, k1}, k22 = {k2, k2}, k32 = {k3, k3};
#pragma unroll
    for (int j = 0; j < NB; ++j) {
        const int blk = cg * 3 + j0 + j;
        float v[16];
#pragma unroll
        for (int g = 0; g < 4; ++g) {
            const LAS f32x4* cp = (const LAS f32x4*)(smem + P4_CS + ((j0 + j) * 32 + 8 * g + 4 * hh) * 4);
            const f32x4 cgf = cp[0], cuf = cp[32];
#pragma unroll
            for (int e = 0; e < 4; e += 2) {
                const f32x2 ga = {ag[j][4 * g + e], ag[j][4 * g + e + 1]}, ua = {au[j][4 * g + e], au[j][4 * g + e + 1]};
                const f32x2 cg2 = {cgf[e], cgf[e + 1]}, cu2 = {cuf[e], cuf[e + 1]};
                const f32x2 a = __builtin_elementwise_fma(nm2, cg2, ga), bq = __builtin_elementwise_fma(nm2, cu2, ua);
                const f32x2 arg = a * __builtin_elementwise_fma(a * a, k22, k12);
                const f32x2 ex = {__builtin_amdgcn_exp2f(arg[0]), __builtin_amdgcn_exp2f(arg[1])};
                const f32x2 den = __builtin_elementwise_fma(ex, k32, k32);
                const f32x2 rc = {__builtin_amdgcn_rcpf(den[0]), __builtin_amdgcn_rcpf(den[1])};
                const f32x2 y = (a * rc) * bq;
                v[4 * g + e] = y[0]; v[4 * g + e + 1] = y[1];
            }
        }
        if (blk < 43) {
#pragma unroll
            for (int g = 0; g < 2; ++g) {
                const u32x4 o = pack_swap(v[8 * g], v[8 * g + 1], v[8 * g + 2], v[8 * g + 3], v[8 * g + 4], v[8 * g + 5], v[8 * g + 6], v[8 * g + 7]);
                gst128(ws + WS_AF + ((size_t)tile * 86 + blk * 2 + g) * 1024, lane * 16, o);
            }
        }
    }
}

__global__ __launch_bounds__(640, 3) void k_block(const char* __restrict__ x, const char* __restrict__ wqkv, const char* __restrict__ wo, const char* __restrict__ wg,
                                                  const char* __restrict__ wu, const char* __restrict__ wd, char* __restrict__ ws, char* __restrict__ out) {
    extern __shared__ __attribute__((aligned(16))) char smem[];
    const Ptrs P = {x, wqkv, wo, wg, wu, wd, ws, out};
    const int b = blockIdx.x, xcd = b & 7, idx = b >> 3;
    const int lane = threadIdx.x & 63, w = rfl(threadIdx.x >> 6);
    const unsigned lds0 = (unsigned)(uintptr_t)smem;
    if (w >= 8 && blockIdx.x < 64) {
        const char* pc = (const char*)(__builtin_amdgcn_s_getpc() & ~127ull);
        asm volatile("s_mov_b32 m0, %0\n\ts_nop 0" ::"s"(lds0 + LDS_BYTES - 256) : "memory");
#pragma unroll
        for (int i = 0; i < (CODE_LINES + 63) / 64; ++i) { const int ln = i * 64 + lane; gtouch(pc, (ln < CODE_LINES ? ln : CODE_LINES - 1) * 128); }
    }
    const bool p1_gemm = idx >= 8;
    const int gi = idx - 8;
    const int tb1 = (xcd >> 1) * 4 + (gi & 3), cb1 = (xcd & 1) * 6 + (gi >> 2);
    const int head = xcd * 4 + (idx >> 3), qb = idx & 7;
    const int tb3 = xcd * 4 + (idx & 3), cb3 = idx >> 2;
    const bool p4_on = !(xcd & 1) || idx < 28;
    const int tb4 = (xcd >> 1) * 4 + (idx & 3), cg4 = (xcd & 1) * 8 + (idx >> 2);
    ST(0);

    if (w < 8) {
        if (p1_gemm) {
            if (cb1 < 8) p1_compute<false>(ws, tb1, cb1, smem, w, lane); else p1_compute<true>(ws, tb1, cb1, smem, w, lane);
        } else p1_helper(x, wqkv, wg, wu, ws, idx * 8 + xcd, w, lane);
        ST(1);
        pbar();
        p2_compute(ws, smem, head, qb, w, lane);
        ST(2);
        pbar();
        u32x4 hkeep;
        p35_compute<0>(P, smem, tb3, cb3, w, lane, hkeep);
        ST(3);
        pbar();
        if (p4_on) {
            if (w < 4) p4_compute<2>(ws, smem, tb4, cg4, w, lane); else p4_compute<1>(ws, smem, tb4, cg4, w, lane);
        } else bar();
        ST(4);
        pbar();
        p35_compute<1>(P, smem, tb3, cb3, w, lane, hkeep);
        ST(10);
        return;
    }

    const int L = w - 8;
    const char* flags = ws + WS_FLAG;
    const unsigned lin = lane * 16;
    const unsigned tag = p1_gemm ? p1_load(P, L, tb1, cb1, P1ROT, gi, lds0, lane) : rfl(gld32_wait(ws + WS_EPOCH, 0)) + 1;
    bar();
    if (L == 0 && lane == 0) gst32(ws + WS_FLAG, b * 128, tag);
    ST(1);
    {
        const int ptb = lane < 32 ? (lane & 15) : 2 * qb + (lane & 1), pcb = (lane < 16 ? 4 : lane < 32 ? 8 : 0) + (head >> 3);
        const int pb = ((pcb % 6) * 4 + (ptb & 3) + 8) * 8 + (ptb >> 2) * 2 + pcb / 6;
        poll(flags, pb * 128, lane < 34, tag);
        ST(6);
        const int rot = qb * 4;
        const char* src = ws + (L == 0 ? WS_KF : WS_VF) + (size_t)head * 65536;
        const unsigned dst = lds0 + L * 65536;
        {
            const char* q = ws + WS_QF + ((size_t)head * 64 + qb * 8) * 1024 + L * 4096;
            dma4lin(q, lin, lds0 + P2_X + L * 4096);
            if (L == 0) dma1dw(ws + WS_KM + head * 256, lane * 4, lds0 + P2_KM);
        }
#pragma unroll 1
        for (int g = 0; g < 8; ++g) {
            const unsigned so = ((g * 4 + rot) & 31) * 1024;
            dma4lin(src + so, lin, dst + g * 4096); dma4lin(src + 32768 + so, lin, dst + 32768 + g * 4096);
            if (g == 1) lbar<0>();
        }
        ST(7);
        if (L == 1) {
            asm volatile("s_waitcnt vmcnt(0)" ::: "memory");
        }
        lbar<0>();
        if (L == 1) {
#pragma unroll 1
            for (int g = 0; g < 4; ++g) poll(flags, (g * 64 + lane) * 128, true, tag);
            if (b == 0 && lane == 0) gst32(ws + WS_EPOCH, 0, tag);
            asm volatile("s_mov_b32 m0, %0\n\ts_nop 0" ::"s"(lds0 + LDS_BYTES - 256) : "memory");
            gtouch(wo, (idx * 128 + lane) * 128); gtouch(wo, (idx * 128 + 64 + lane) * 128);
            {
                const int nl = (xcd & 1) ? 10 : 12, ln = lane & 15;
                const unsigned co = (xcd & 1) * 1536 + (ln < nl ? ln : 0) * 128;
#pragma unroll
                for (int i = 0; i < 4; ++i) {
                    unsigned o_ = (idx * 16 + i * 4 + (lane >> 4)) * (FF * 2) + co;
                    o_ = o_ > 512u * FF * 2 - 4 ? 512u * FF * 2 - 4 : o_;
                    gtouch(wg, o_); gtouch(wu, o_);
                }
            }
#pragma unroll
            for (int i = 0; i < 6; ++i) { int ln = i * 64 + lane; ln = ln > 343 ? 343 : ln; gtouch(wd, (idx * 344 + ln) * 128); }
        }
        bar();
    }
    unsigned wvo[4];
    {
        const unsigned wv = ((lane >> 5) * 4 + ((lane >> 2) & 3)) * 1024 + ((lane >> 4) & 1) * 64 + (lane & 3) * 16;
#pragma unroll
        for (int i = 0; i < 4; ++i) wvo[i] = wv + i * 8192 - i * 1024;
    }
    {
        const int rot = cb3 & 3;
        bar();
        if (L == 1) {
            {
                const int rl = lane >> 3, cs = lane & 7;
                unsigned xv[4];
#pragma unroll
                for (int i = 0; i < 4; ++i) xv[i] = (8 * i + rl) * 1024 + ((cs ^ ((4 * (i & 1) + (rl >> 1)) & 7)) << 4) - i * 1024;
                const char* xs = x + (size_t)tb3 * 64 * 1024 + cb3 * 128;
                dma4(xs, xv[0], xv[1], xv[2], xv[3], lds0 + P3_X); dma4(xs + 32768, xv[0], xv[1], xv[2], xv[3], lds0 + P3_X + 4096);
            }
#pragma unroll 1
            for (int c = 0; c < 4; ++c) {
                const char* src = wo + cb3 * 128 + (size_t)((c + rot) & 3) * 128 * 1024;
#pragma unroll
                for (int g = 0; g < 4; ++g) dma4(src + g * 32768, wvo[0], wvo[1], wvo[2], wvo[3], lds0 + c * 16384 + g * 4096);
            }
        }
        if (L == 0) {
            if (lane == 0) gst32(ws + WS_FLAG + 256 * 128, b * 128, tag);
            ST(2);
            const int ph = lane & 31;
            poll(flags + 256 * 128, (((ph & 3) * 8 + (tb3 >> 2)) * 8 + (ph >> 2)) * 128, true, tag);
            ST(8);
            const char* ab = ws + WS_OF + (size_t)tb3 * 2 * 32 * 1024;
#pragma unroll 1
            for (int c = 0; c < 4; ++c) {
                const char* src = ab + ((c + rot) & 3) * 8192;
                const unsigned dst = lds0 + 65536 + c * 16384;
                dma4lin(src, lin, dst); dma4lin(src + 4096, lin, dst + 4096); dma4lin(src + 32768, lin, dst + 8192); dma4lin(src + 32768 + 4096, lin, dst + 12288);
                if (c == 1) lbar<16>();
            }
            lbar<32>(); lbar<16>(); lbar<0>();
        } else {
            lbar<0>(); bar(); bar(); bar();
        }
        bar();
    }
    {
        const int rot = cg4 & 7;
        unsigned gvo[12];
        if (L == 1 && p4_on) {
#pragma unroll
            for (int j = 0; j < 12; ++j) {
                const int s = j * 64 + lane, st = s >> 4, kb = st / 3, nb = st % 3, q = (s >> 2) & 3, c = s & 3;
                int blk = cg4 * 3 + nb; blk = blk > 42 ? 42 : blk;
                gvo[j] = (4 * kb + q) * (FF * 2) + blk * 64 + c * 16 - (j & 3) * 1024;
            }
        }
        auto issue_w = [&](int c, unsigned B) {
            const size_t so = (size_t)((c + rot) & 7) * 64 * (FF * 2);
#pragma unroll
            for (int g = 0; g < 3; ++g) dma4(wg + so, gvo[4 * g], gvo[4 * g + 1], gvo[4 * g + 2], gvo[4 * g + 3], lds0 + B + g * 4096);
#pragma unroll
            for (int g = 0; g < 3; ++g) dma4(wu + so, gvo[4 * g], gvo[4 * g + 1], gvo[4 * g + 2], gvo[4 * g + 3], lds0 + B + 12288 + g * 4096);
        };
        bar();
        bar();
        if (L == 1 && p4_on) { issue_w(0, 0); issue_w(1, 24576); issue_w(2, 2 * 24576); issue_w(3, 3 * 24576); }
        if (L == 0) { if (lane == 0) gst32(ws + WS_FLAG + 2 * 256 * 128, b * 128, tag); ST(3); }
        if (p4_on) {
            if (L == 0) {
                const int ptb = tb4 * 2 + (lane & 1), pcb = (lane >> 1) & 7;
                poll(flags + 2 * 256 * 128, ((pcb * 4 + (ptb & 3)) * 8 + (ptb >> 2)) * 128, true, tag);
                ST(9);
                const char* hb = ws + WS_HF + (size_t)tb4 * 4 * 32 * 1024;
                auto issue_h = [&](int c, unsigned B) {
                    const char* src = hb + ((c + rot) & 7) * 4096;
#pragma unroll
                    for (int i = 0; i < 4; ++i) dma4lin(src + i * 32768, lin, lds0 + 98304 + B + i * 4096);
                };
                issue_h(0, 0); issue_h(1, 16384);
                lbar<16>();
                {
                    const char* sb = ws + WS_ST + (size_t)tb4 * 128 * 64;
                    dma4lin(sb, lin, lds0 + P4_ST); dma4lin(sb + 4096, lin, lds0 + P4_ST + 4096);
                    const char* cb_ = ws + WS_CS + cg4 * 384;
                    dma1dw(cb_, lane * 4, lds0 + P4_CS); dma1dw(cb_ + 256, lane * 4, lds0 + P4_CS + 256);
                    dma1dw(cb_ + FF * 4, lane * 4, lds0 + P4_CS + 512); dma1dw(cb_ + FF * 4 + 256, lane * 4, lds0 + P4_CS + 768);
                }
                issue_h(2, 2 * 16384);
                unsigned B = 0;
#pragma unroll 1
                for (int c = 1; c < 8; ++c) {
                    if (c < 7) lbar<16>(); else lbar<0>();
                    if (c + 2 < 8) { issue_h(c + 2, B); B = B == 2 * 16384 ? 0 : B + 16384; }
                }
            } else {
                unsigned B = 0;
#pragma unroll 1
                for (int c = 0; c < 8; ++c) {
                    if (c == 0) lbar<63>(); else if (c < 6) lbar<48>(); else if (c == 6) lbar<24>(); else lbar<0>();
                    if (c >= 1 && c + 3 < 8) { issue_w(c + 3, B); B = B == 3 * 24576 ? 0 : B + 24576; }
                }
            }
            bar();
        } else bar();
    }
    {
        const int rot = cb3;
        auto issue_w = [&](int c, unsigned B) {
            const int cc = c < 10 ? (c + rot) % 10 : 10;
            const char* src = wd + cb3 * 128 + (size_t)cc * 128 * 1024;
#pragma unroll
            for (int g = 0; g < 3; ++g) dma4(src + g * 32768, wvo[0], wvo[1], wvo[2], wvo[3], lds0 + B + g * 4096);
            dma4(src + (c < 10 ? 3 : 2) * 32768, wvo[0], wvo[1], wvo[2], wvo[3], lds0 + B + 3 * 4096);
        };
        bar();
        if (L == 1) {
#pragma unroll 1
            for (int c = 0; c < 6; ++c) issue_w(c, c * 16384);
        }
        if (L == 0) {
            if (p4_on && lane == 0) gst32(ws + WS_FLAG + 3 * 256 * 128, b * 128, tag);
            ST(4);
            const int ptb = tb3 >> 1, pcg = lane < 15 ? lane : 0;
            poll(flags + 3 * 256 * 128, (((pcg & 7) * 4 + (ptb & 3)) * 8 + (ptb >> 2) * 2 + (pcg >> 3)) * 128, true, tag);
            ST(5);
            const char* ab = ws + WS_AF + (size_t)tb3 * 2 * 86 * 1024;
            auto issue_a = [&](int c, unsigned B) {
                const int cc = c < 10 ? (c + rot) % 10 : 10;
                const char* src = ab + cc * 8192;
                const unsigned dst = lds0 + 98304 + B;
                const int o2 = c < 10 ? 4096 : 2048;
                dma4lin(src, lin, dst); dma4lin(src + o2, lin, dst + o2);
                dma4lin(src + 86 * 1024, lin, dst + 8192); dma4lin(src + 86 * 1024 + o2, lin, dst + 8192 + o2);
            };
            issue_a(0, 0); issue_a(1, 16384);
            unsigned B = 2 * 16384;
#pragma unroll 1
            for (int c = 0; c < 11; ++c) {
                if (c < 10) lbar<16>(); else lbar<0>();
                if (c + 2 < 11) { issue_a(c + 2, B); B = B == 2 * 16384 ? 0 : B + 16384; }
            }
        } else {
            unsigned B = 0;
#pragma unroll 1
            for (int c = 0; c < 11; ++c) {
                if (c < 7) lbar<63>(); else if (c == 7) lbar<48>(); else if (c == 8) lbar<32>(); else if (c == 9) lbar<16>(); else lbar<0>();
                if (c >= 1 && c + 5 < 11) { issue_w(c + 5, B); B = B == 5 * 16384 ? 0 : B + 16384; }
            }
        }
        bar();
    }
}

extern "C" void kernel_launch(void* const* d_in, const int* in_sizes, int n_in, void* d_out, int out_size, void* d_ws, size_t ws_size, hipStream_t stream) {
    (void)hipFuncSetAttribute((const void*)k_block, hipFuncAttributeMaxDynamicSharedMemorySize, LDS_BYTES);
    k_block<<<256, 640, LDS_BYTES, stream>>>((const char*)d_in[0], (const char*)d_in[1], (const char*)d_in[2], (const char*)d_in[4], (const char*)d_in[5], (const char*)d_in[6],
                                             (char*)d_ws, (char*)d_out);
}
```

```cpp
#include <hip/hip_runtime.h>
#include <hip/hip_bf16.h>
#include <stdint.h>

typedef __bf16 bf16x8 __attribute__((ext_vector_type(8)));
typedef __bf16 bf16x2 __attribute__((ext_vector_type(2)));
typedef short s16x4 __attribute__((ext_vector_type(4)));
typedef short s16x8 __attribute__((ext_vector_type(8)));
typedef float f32x16 __attribute__((ext_vector_type(16)));
typedef float f32x4 __attribute__((ext_vector_type(4)));
typedef float f32x2 __attribute__((ext_vector_type(2)));
typedef unsigned u32x4 __attribute__((ext_vector_type(4)));
typedef unsigned u32x2 __attribute__((ext_vector_type(2)));
#define LAS __attribute__((address_space(3)))
#define DI __device__ __forceinline__

constexpr int DM = 512, FF = 1376;
constexpr float LOG2E = 1.4426950408889634f;
constexpr float QSCALE = 0.25f * LOG2E;
constexpr float LN_EPS = 1e-5f;

constexpr size_t WS_QF = 0, WS_KF = 2u << 20, WS_VF = 4u << 20, WS_OF = 6u << 20, WS_HF = 8u << 20, WS_AF = 10u << 20,
                 WS_ST = 16u << 20, WS_CS = 17u << 20, WS_KM = (17u << 20) + 65536, WS_FLAG = 18u << 20, WS_EPOCH = WS_FLAG + 4 * 256 * 128,
                 WS_G1 = 19u << 20, WS_G2 = WS_G1 + 65536;
constexpr int LDS_BYTES = 163840;
constexpr unsigned SPIN_CAP = 1u << 17;
#define CODE_LINES_ 268
constexpr int CODE_LINES = CODE_LINES_;

#define MFMA(a, b, c) __builtin_amdgcn_mfma_f32_32x32x16_bf16(a, b, c, 0, 0, 0)
#define SB() __builtin_amdgcn_sched_barrier(0)
DI void cbar() { asm volatile("s_waitcnt lgkmcnt(0)\n\ts_barrier" ::: "memory"); }
DI void pbar() { asm volatile("s_waitcnt vmcnt(0) lgkmcnt(0)\n\ts_barrier" ::: "memory"); }
template <int N> DI void lbar() { asm volatile("s_waitcnt vmcnt(%0)\n\ts_barrier" ::"i"(N > 63 ? 63 : N) : "memory"); }
DI void bar() { asm volatile("s_barrier" ::: "memory"); }

DI void dma4(const void* sbase, unsigned v0, unsigned v1, unsigned v2, unsigned v3, unsigned dst) {
    asm volatile("s_mov_b32 m0, %5\n\ts_nop 0\n\tglobal_load_lds_dwordx4 %0, %4 sc1\n\tglobal_load_lds_dwordx4 %1, %4 offset:1024 sc1\n\t"
                 "global_load_lds_dwordx4 %2, %4 offset:2048 sc1\n\tglobal_load_lds_dwordx4 %3, %4 offset:3072 sc1"
                 :: "v"(v0), "v"(v1), "v"(v2), "v"(v3), "s"(sbase), "s"(dst) : "memory");
}
DI void dma4lin(const void* sbase, unsigned v, unsigned dst) { dma4(sbase, v, v, v, v, dst); }
DI void dma2lin(const void* sbase, unsigned v, unsigned dst) {
    asm volatile("s_mov_b32 m0, %2\n\ts_nop 0\n\tglobal_load_lds_dwordx4 %0, %1 sc1\n\tglobal_load_lds_dwordx4 %0, %1 offset:1024 sc1" :: "v"(v), "s"(sbase), "s"(dst) : "memory");
}
DI void dma1dw(const void* sbase, unsigned v, unsigned dst) {
    asm volatile("s_mov_b32 m0, %2\n\ts_nop 0\n\tglobal_load_lds_dword %0, %1 sc1" :: "v"(v), "s"(sbase), "s"(dst) : "memory");
}
DI void gtouch(const void* sbase, unsigned voff) {
    asm volatile("global_load_lds_dword %0, %1" :: "v"(voff), "s"(sbase) : "memory");
}
DI unsigned gld32_wait(const void* sbase, unsigned voff) {
    unsigned r; asm volatile("global_load_dword %0, %1, %2 sc1\n\ts_waitcnt vmcnt(0)" : "=&v"(r) : "v"(voff), "s"(sbase) : "memory"); return r;
}
DI void gst128(void* sbase, unsigned voff, u32x4 v) { asm volatile("global_store_dwordx4 %0, %1, %2 sc1\n\ts_nop 1" ::"v"(voff), "v"(v), "s"(sbase) : "memory"); }
DI void gst64(void* sbase, unsigned voff, u32x2 v) { asm volatile("global_store_dwordx2 %0, %1, %2 sc1\n\ts_nop 1" ::"v"(voff), "v"(v), "s"(sbase) : "memory"); }
DI void gst32(void* sbase, unsigned voff, unsigned v) { asm volatile("global_store_dword %0, %1, %2 sc1\n\ts_nop 1" ::"v"(voff), "v"(v), "s"(sbase) : "memory"); }
#define VWAIT_(N) asm volatile("s_waitcnt vmcnt(" #N ")" ::: "memory")
#define VWAIT(N) VWAIT_(N)
#define PIN(x) asm volatile("" : "+v"(x))
DI unsigned rfl(unsigned x) { return __builtin_amdgcn_readfirstlane(x); }
DI unsigned cvtpk(float lo, float hi) {
    f32x2 v = {lo, hi};
    bf16x2 b = __builtin_convertvector(v, bf16x2);
    return __builtin_bit_cast(unsigned, b);
}
DI float bflo(unsigned u) { return __uint_as_float(u << 16); }
DI float bfhi(unsigned u) { return __uint_as_float(u & 0xffff0000u); }
DI bf16x8 lds_b128(unsigned a) { return *(LAS const bf16x8*)(uintptr_t)a; }
typedef short v4i16_t __attribute__((ext_vector_type(4)));
DI s16x4 lds_tr(unsigned a) { return __builtin_bit_cast(s16x4, __builtin_amdgcn_ds_read_tr16_b64_v4i16((LAS v4i16_t*)(uintptr_t)a)); }
DI bf16x8 lds_trfrag(unsigned a, int off2) {
    s16x4 x = lds_tr(a), y = lds_tr(a + off2);
    s16x8 r = __builtin_shufflevector(x, y, 0, 1, 2, 3, 4, 5, 6, 7);
    return __builtin_bit_cast(bf16x8, r);
}
DI float dot2(unsigned a, unsigned b, float c) {
    return __builtin_amdgcn_fdot2_f32_bf16(__builtin_bit_cast(bf16x2, a), __builtin_bit_cast(bf16x2, b), c, false);
}
DI float other_half(float v) {
    auto r = __builtin_amdgcn_permlane32_swap(__float_as_uint(v), __float_as_uint(v), false, false);
    return __uint_as_float((threadIdx.x & 32) ? r[0] : r[1]);
}
DI float sum_halves(float v) {
    auto r = __builtin_amdgcn_permlane32_swap(__float_as_uint(v), __float_as_uint(v), false, false);
    return __uint_as_float(r[0]) + __uint_as_float(r[1]);
}
template <bool ALL> DI float dpp_max(float v) {
#define DPPMAX(ctrl, rm) v = fmaxf(v, __int_as_float(__builtin_amdgcn_update_dpp(0, __float_as_int(v), ctrl, rm, 0xf, true)))
    DPPMAX(0x111, 0xf); DPPMAX(0x112, 0xf); DPPMAX(0x114, 0xf); DPPMAX(0x118, 0xf); DPPMAX(0x142, 0xa);
    if (ALL) DPPMAX(0x143, 0xc);
    return v;
}
template <int CTRL> DI float dpp_add(float v) { return v + __int_as_float(__builtin_amdgcn_update_dpp(0, __float_as_int(v), CTRL, 0xf, 0xf, true)); }
DI u32x4 swap4(unsigned p0, unsigned p1, unsigned p2, unsigned p3) {
    auto s0 = __builtin_amdgcn_permlane32_swap(p0, p2, false, false);
    auto s1 = __builtin_amdgcn_permlane32_swap(p1, p3, false, false);
    u32x4 r = {s0[0], s1[0], s0[1], s1[1]};
    return r;
}
DI u32x4 pack_swap(float a0, float a1, float a2, float a3, float a4, float a5, float a6, float a7) {
    return swap4(cvtpk(a0, a1), cvtpk(a2, a3), cvtpk(a4, a5), cvtpk(a6, a7));
}
constexpr unsigned ONES = 0x3f803f80u;

#define ST(k)
#define ST2(k)

DI void poll(const char* fl, unsigned voff, bool active, unsigned tag) {
    for (unsigned spins = 0; spins < SPIN_CAP; ++spins) {
        unsigned f;
        asm volatile("global_load_dword %0, %1, %2 sc1\n\ts_waitcnt vmcnt(0)" : "=&v"(f) : "v"(voff), "s"(fl) : "memory");
        if (__all(!active || f == tag)) break;
        __builtin_amdgcn_s_sleep(1);
    }
}

struct Ptrs { const char *x, *wqkv, *wo, *wg, *wu, *wd; char* ws; char* out; };

#define P1ROT 0
#define T1W 19
#define P1NB 3
constexpr int P1_BUF = 32768, P1_MISC = P1NB * P1_BUF;

template <bool ISV>
DI void p1_compute(char* ws, int tb, int cb, char* smem, int w, int lane) {
    const int r = lane & 31, hh = lane >> 5;
    const unsigned lds0 = (unsigned)(uintptr_t)smem;
    const int th = w & 1, ct = w >> 1;
    unsigned x0[4], x1[4];
    {
        const int f = (r >> 1) & 7, t0 = 2 * th, t1 = 2 * th + 1;
#pragma unroll
        for (int ks = 0; ks < 4; ++ks) {
            const unsigned sw = ((2 * ks + hh) ^ f) << 4;
            x0[ks] = lds0 + (32 * t0 + r) * 128 + sw; x1[ks] = lds0 + (32 * t1 + r) * 128 + sw;
        }
    }
    const unsigned wro = lds0 + 16384 + hh * 2048 + ((lane & 15) >> 2) * 64 + ((lane >> 4) & 1) * 32 + (lane & 3) * 8 + ct * 256;
    f32x16 acc0 = {}, acc1 = {};
    bf16x8 X0[4], X1[4], W[4];
#define P1_LOAD(S, B, ks) { X0[S] = lds_b128(x0[ks] + (B)); X1[S] = lds_b128(x1[ks] + (B)); W[S] = lds_trfrag(wro + (B) + (ks) * 4096, 1024); }
#define P1_MMA(S)                                                                                                                 \
    {                                                                                                                             \
        acc0 = ISV ? MFMA(X0[S], W[S], acc0) : MFMA(W[S], X0[S], acc0);                                                            \
        acc1 = ISV ? MFMA(X1[S], W[S], acc1) : MFMA(W[S], X1[S], acc1);                                                            \
    }
    cbar();
    ST(11);
    P1_LOAD(0, 0, 0); P1_LOAD(1, 0, 1);
    unsigned B = 0;
#pragma unroll 1
    for (int ch = 0; ch < 8; ++ch) {
        P1_LOAD(2, B, 2); SB(); P1_MMA(0); SB();
        P1_LOAD(3, B, 3); SB(); P1_MMA(1); SB();
        if (ch < 7) {
            cbar();
            B = B == (P1NB - 1) * P1_BUF ? 0 : B + P1_BUF;
            P1_LOAD(0, B, 0);
        }
        SB(); P1_MMA(2); SB();
        if (ch < 7) { P1_LOAD(1, B, 1); }
        SB(); P1_MMA(3); SB();
    }
    ST(12);
    cbar();
    const LAS f32x2* st = (const LAS f32x2*)(smem + P1_MISC);
    const LAS float* csl = (const LAS float*)(smem + P1_MISC + 1024);
    const int sec = cb >> 2, head0 = (cb & 3) * 8 + ct * 2;
    if constexpr (!ISV) {
        f32x4 c4[4];
#pragma unroll
        for (int g = 0; g < 4; ++g) c4[g] = *(LAS const f32x4*)(csl + ct * 32 + 8 * g + 4 * hh);
#pragma unroll
        for (int a = 0; a < 2; ++a) {
            const int tl = 2 * th + a, tile = tb * 4 + tl;
            const f32x16& acc = a ? acc1 : acc0;
            const f32x2 ms = st[tl * 32 + r];
            const float sc = ms[1] * (sec == 0 ? QSCALE : 1.f), msc = -ms[0] * sc;
            float v[16];
#pragma unroll
            for (int i = 0; i < 16; ++i) v[i] = acc[i] * sc + msc * c4[i >> 2][i & 3];
            char* dst = ws + (sec == 0 ? WS_QF : WS_KF) + ((size_t)head0 * 64 + tile) * 1024;
#pragma unroll
            for (int hs = 0; hs < 2; ++hs) {
                const u32x4 o = pack_swap(v[8 * hs], v[8 * hs + 1], v[8 * hs + 2], v[8 * hs + 3], v[8 * hs + 4], v[8 * hs + 5], v[8 * hs + 6], v[8 * hs + 7]);
                gst128(dst + hs * 65536, lane * 16, o);
                if (sec == 1) {
                    float n2 = 0.f;
#pragma unroll
                    for (int e = 0; e < 8; ++e) n2 = fmaf(v[8 * hs + e], v[8 * hs + e], n2);
                    n2 = dpp_max<false>(sum_halves(n2));
                    if (lane == 31) gst32(ws + WS_KM + (head0 + hs) * 256 + tile * 4, 0, __float_as_uint(n2));
                }
            }
        }
    } else {
        const float csn = csl[ct * 32 + r];
#pragma unroll
        for (int a = 0; a < 2; ++a) {
            const int tl = 2 * th + a, tile = tb * 4 + tl;
            const f32x16& acc = a ? acc1 : acc0;
            float v[16];
#pragma unroll
            for (int g = 0; g < 4; ++g) {
                const LAS f32x4* sp = (const LAS f32x4*)(st + tl * 32 + 8 * g + 4 * hh);
                const f32x4 pa = sp[0], pb = sp[1];
                v[4 * g] = (acc[4 * g] - pa[0] * csn) * pa[1]; v[4 * g + 1] = (acc[4 * g + 1] - pa[2] * csn) * pa[3];
                v[4 * g + 2] = (acc[4 * g + 2] - pb[0] * csn) * pb[1]; v[4 * g + 3] = (acc[4 * g + 3] - pb[2] * csn) * pb[3];
            }
            char* dst = ws + WS_VF + ((size_t)head0 * 64 + tile) * 1024;
            const unsigned vo = (r >> 4) * 65536 + hh * 256 + (r & 15) * 16;
#pragma unroll
            for (int s = 0; s < 2; ++s) {
                u32x4 o = {cvtpk(v[8 * s], v[8 * s + 1]), cvtpk(v[8 * s + 2], v[8 * s + 3]), cvtpk(v[8 * s + 4], v[8 * s + 5]), cvtpk(v[8 * s + 6], v[8 * s + 7])};
                gst128(dst + s * 512, vo, o);
            }
        }
    }
}

DI void gst_granule(char* sbase, unsigned voff, float v, unsigned tag) { u32x2 g = {__float_as_uint(v), tag}; gst64(sbase, voff, g); }
typedef const __attribute__((address_space(1))) char* gcptr;
template <int NROW16>
DI void colsum32(gcptr W, unsigned rs, int lane, float (&s)[8]) {
    const unsigned vo = (lane & 15) * rs + (lane >> 4) * 16;
    u32x4 v[NROW16];
#pragma unroll
    for (int i = 0; i < NROW16; ++i) v[i] = *(const __attribute__((address_space(1))) u32x4*)(W + (size_t)i * 16 * rs + vo);
    __builtin_amdgcn_sched_barrier(0);
#pragma unroll
    for (int e = 0; e < 8; ++e) s[e] = 0.f;
#pragma unroll
    for (int i = 0; i < NROW16; ++i)
#pragma unroll
        for (int e = 0; e < 4; ++e) { s[2 * e] += bflo(v[i][e]); s[2 * e + 1] += bfhi(v[i][e]); }
#pragma unroll
    for (int e = 0; e < 8; ++e) s[e] = dpp_add<0x118>(dpp_add<0x114>(dpp_add<0x112>(dpp_add<0x111>(s[e]))));
}
DI void p1_helper(const char* px, const char* pqkv, const char* pg, const char* pu, char* ws, int h, int w, int lane) {
    if (w < 7) {
        const unsigned tag = __hip_atomic_load((const __attribute__((address_space(1))) unsigned*)(ws + WS_EPOCH), __ATOMIC_RELAXED, __HIP_MEMORY_SCOPE_AGENT) + 1;
        const int u = w * 64 + h;
        if (u < 256) {
            const int rl = lane >> 3, seg = lane & 7;
            const char* src = px + (size_t)(u * 8 + rl) * 1024 + seg * 16;
            u32x4 v[8];
#pragma unroll
            for (int i = 0; i < 8; ++i) v[i] = *(const u32x4*)(src + i * 128);
            float s1 = 0.f, s2 = 0.f;
#pragma unroll
            for (int i = 0; i < 8; ++i)
#pragma unroll
                for (int e = 0; e < 4; ++e) { s1 = dot2(v[i][e], ONES, s1); s2 = dot2(v[i][e], v[i][e], s2); }
            s1 = dpp_add<0x114>(dpp_add<0x112>(dpp_add<0x111>(s1))); s2 = dpp_add<0x114>(dpp_add<0x112>(dpp_add<0x111>(s2)));
            const float mean = s1 * (1.f / DM);
            const float rstd = __builtin_amdgcn_rsqf(fmaxf(s2 * (1.f / DM) - mean * mean, 0.f) + LN_EPS);
            if (seg == 7) { gst_granule(ws + WS_G1, (u * 8 + rl) * 16, mean, tag); gst_granule(ws + WS_G1, (u * 8 + rl) * 16 + 8, rstd, tag); }
        } else {
            const int q = u - 256, blk = q >> 2, kq = q & 3;
            float s[8];
            colsum32<8>((gcptr)(pqkv + (size_t)kq * 128 * 3072 + blk * 64), 3072, lane, s);
            if ((lane & 15) == 15) {
#pragma unroll
                for (int e = 0; e < 8; ++e) gst_granule(ws + WS_G2, ((blk * 32 + (lane >> 4) * 8 + e) * 4 + kq) * 8, s[e], tag);
            }
        }
    }
    const int job = w == 7 ? h : (w == 0 && h < 22) ? 64 + h : -1;
    if (job >= 0) {
        const int blk = job < 43 ? job : job - 43;
        const uintptr_t wa = (uintptr_t)pg + (job < 43 ? 0 : (uintptr_t)pu - (uintptr_t)pg);
        float s[8];
        colsum32<32>((gcptr)wa + blk * 64, FF * 2, lane, s);
        if ((lane & 15) == 15) {
            char* dst = ws + WS_CS + (job < 43 ? 0 : FF) * 4;
            const int c0 = blk * 32 + (lane >> 4) * 8;
            u32x4 a = {__float_as_uint(s[0]), __float_as_uint(s[1]), __float_as_uint(s[2]), __float_as_uint(s[3])};
            u32x4 c = {__float_as_uint(s[4]), __float_as_uint(s[5]), __float_as_uint(s[6]), __float_as_uint(s[7])};
            gst128(dst, c0 * 4, a); gst128(dst, c0 * 4 + 16, c);
        }
    }
}

DI unsigned p1_load(const Ptrs& P, int L, int tb, int cb, int rot, int sidx, unsigned lds0, int lane) {
    unsigned vo[4];
    const char* base;
    unsigned cstep, gstep;
    if (L == 0) {
        const int rl = lane >> 3, cs = lane & 7;
#pragma unroll
        for (int i = 0; i < 4; ++i) vo[i] = (8 * i + rl) * 1024 + ((cs ^ ((4 * (i & 1) + (rl >> 1)) & 7)) << 4) - i * 1024;
        base = P.x + (size_t)tb * 128 * 1024; cstep = 128; gstep = 4 * 8192;
    } else {
        const int nb = lane >> 4, q = (lane >> 2) & 3, c = lane & 3;
#pragma unroll
        for (int i = 0; i < 4; ++i) vo[i] = (4 * i + q) * 3072 + nb * 64 + c * 16 - i * 1024;
        base = P.wqkv + cb * 256; cstep = 64 * 3072; gstep = 16 * 3072;
    }
    auto issue = [&](int c, unsigned B) {
        const char* src = base + (size_t)((c + rot) & 7) * cstep;
        const unsigned dst = lds0 + B + L * 16384;
#pragma unroll
        for (int g = 0; g < 4; ++g) dma4(src + (size_t)g * gstep, vo[0], vo[1], vo[2], vo[3], dst + g * 4096);
    };
    issue(0, 0);
    {
        asm volatile("s_mov_b32 m0, %0\n\ts_nop 0" ::"s"(lds0 + LDS_BYTES - 256) : "memory");
        if (L == 0) {
#pragma unroll
            for (int i = 0; i < 3; ++i) { int ln = sidx * 171 + i * 64 + lane; ln = ln > 4095 ? 4095 : ln; gtouch(P.x + (size_t)(tb & ~3) * 128 * 1024, ln * 128); }
        } else {
#pragma unroll
            for (int i = 0; i < 4; ++i) { const int ln = sidx * 256 + i * 64 + lane; gtouch(P.wqkv + (cb / 6) * 1536, (ln / 12) * 3072 + (ln % 12) * 128); }
        }
    }
#pragma unroll
    for (int c = 1; c < P1NB - 1; ++c) issue(c, c * P1_BUF);
    unsigned B = (P1NB - 1) * P1_BUF;
    constexpr int LA = P1NB - 1;
#pragma unroll 1
    for (int c = 0; c < 8; ++c) {
        const int y = 7 - c < LA - 1 ? 7 - c : LA - 1;
        if (c == 0) lbar<16 * (LA - 1) + T1W - 16>(); else if (y == 3) lbar<48>(); else if (y == 2) lbar<32>(); else if (y == 1) lbar<16>(); else lbar<0>();
        if (c + LA < 8) { issue(c + LA, B); B = B == (P1NB - 1) * P1_BUF ? 0 : B + P1_BUF; }
    }
    const unsigned tag = rfl(gld32_wait(P.ws + WS_EPOCH, 0)) + 1;
    if (L == 1) {
        u32x4 a, b, c0, c1, c2, c3;
        for (unsigned spins = 0; spins < SPIN_CAP; ++spins) {
            asm volatile("global_load_dwordx4 %0, %6, %7 sc1\n\tglobal_load_dwordx4 %1, %6, %7 offset:16 sc1\n\t"
                         "global_load_dwordx4 %2, %8, %9 sc1\n\tglobal_load_dwordx4 %3, %8, %9 offset:16 sc1\n\t"
                         "global_load_dwordx4 %4, %8, %9 offset:32 sc1\n\tglobal_load_dwordx4 %5, %8, %9 offset:48 sc1\n\ts_waitcnt vmcnt(0)"
                         : "=&v"(a), "=&v"(b), "=&v"(c0), "=&v"(c1), "=&v"(c2), "=&v"(c3)
                         : "v"(lane * 32), "s"(P.ws + WS_G1 + tb * 128 * 16), "v"(lane * 64), "s"(P.ws + WS_G2 + cb * 128 * 32) : "memory");
            if (__all(a[1] == tag && a[3] == tag && b[1] == tag && b[3] == tag && c0[1] == tag && c0[3] == tag && c1[1] == tag && c1[3] == tag &&
                      c2[1] == tag && c2[3] == tag && c3[1] == tag && c3[3] == tag)) break;
            __builtin_amdgcn_s_sleep(1);
        }
        { char* ws = P.ws; ST(13); }
        u32x4 ms = {a[0], a[2], b[0], b[2]};
        u32x2 cs = {__float_as_uint(__uint_as_float(c0[0]) + __uint_as_float(c0[2]) + __uint_as_float(c1[0]) + __uint_as_float(c1[2])),
                    __float_as_uint(__uint_as_float(c2[0]) + __uint_as_float(c2[2]) + __uint_as_float(c3[0]) + __uint_as_float(c3[2]))};
        *(LAS u32x4*)(uintptr_t)(lds0 + P1_MISC + lane * 16) = ms;
        *(LAS u32x2*)(uintptr_t)(lds0 + P1_MISC + 1024 + lane * 8) = cs;
        asm volatile("s_waitcnt lgkmcnt(0)" ::: "memory");
    }
    bar();
    return tag;
}

constexpr int P2_X = 131072 + 2048;
constexpr int P2_KM = P2_X + 8 * 2560;
constexpr float SAFE_RANGE = 80.f;

DI void p2_compute(char* ws, char* smem, int head, int qb, int w, int lane) {
    const int r = lane & 31, hh = lane >> 5;
    const unsigned lds0 = (unsigned)(uintptr_t)smem;
    const int qg = w & 3, kh = w >> 2;
    const int la = qg * 2 + kh, lb = qg * 2 + (1 - kh);
    cbar();
    ST(13);
    const u32x4 qa = *(LAS const u32x4*)(uintptr_t)(lds0 + P2_X + la * 1024 + lane * 16), qbv = *(LAS const u32x4*)(uintptr_t)(lds0 + P2_X + lb * 1024 + lane * 16);
    const float kmax2 = __int_as_float(__builtin_amdgcn_readlane(__float_as_int(dpp_max<true>(*(LAS const float*)(uintptr_t)(lds0 + P2_KM + lane * 4))), 63));
    float qa2 = 0.f, qb2 = 0.f;
#pragma unroll
    for (int e = 0; e < 4; ++e) { qa2 = dot2(qa[e], qa[e], qa2); qb2 = dot2(qbv[e], qbv[e], qb2); }
    const float bnda = __builtin_amdgcn_sqrtf(sum_halves(qa2) * kmax2) * 1.01f, bndb = __builtin_amdgcn_sqrtf(sum_halves(qb2) * kmax2) * 1.01f;
    const bool fast = __all(2.f * bnda <= SAFE_RANGE && 2.f * bndb <= SAFE_RANGE);
    const unsigned ka = lds0 + kh * 32768 + lane * 16, va = lds0 + 65536 + kh * 32768 + hh * 256 + (r & 15) * 16;
    const unsigned xw = lds0 + P2_X + w * 2560 + lane * 16, xr = lds0 + P2_X + (w ^ 4) * 2560 + lane * 16;
    float o[9], ma;
    ST(5);
    if (fast) {
        ma = bnda;
        unsigned kp = ka, vp = va, cnt;
#define EXP(r) "v_exp_f32 v" #r ", v" #r "\n\t"
#define CVT(d, a, b) "v_cvt_pk_bf16_f32 v" #d ", v" #a ", v" #b "\n\t"
#define MSQ(SN, KN, Q, C) "v_mfma_f32_32x32x16_bf16 " SN ", " KN ", " Q ", " C "\n\t"
#define MPO(O, V, P) "v_mfma_f32_32x32x16_bf16 " O ", " V ", " P ", " O "\n\t"
#define RDV(VN0, VN1, VOFF) "s_mov_b64 exec, %[msk]\n\t" "ds_read_b128 " VN0 ", %[vp] offset:" #VOFF "\n\t" "ds_read_b128 " VN1 ", %[vp] offset:" #VOFF "+512\n\t" "s_mov_b64 exec, -1\n\t"
#define RDK(KNN, KOFF) "ds_read_b128 " KNN ", %[kp] offset:" #KOFF "\n\t"
#define STEP(PRE, c0, c1, c2, c3, c4, c5, c6, c7, c8, c9, c10, c11, c12, c13, c14, c15, p14, p15, SN, KN, Q, CC, O, OP, VC0, VP1)  \
        EXP(c0) EXP(c1) CVT(127, p14, p15) EXP(c2) EXP(c3) MPO(OP, VP1, "v[124:127]") PRE                                    \
        CVT(120, c0, c1) EXP(c4) EXP(c5) CVT(121, c2, c3) EXP(c6)                                                            \
        "s_waitcnt lgkmcnt(3)\n\t" MSQ(SN, KN, Q, CC)                                                                        \
        EXP(c7) CVT(122, c4, c5) EXP(c8) EXP(c9) CVT(123, c6, c7) EXP(c10) EXP(c11)                                          \
        MPO(O, VC0, "v[120:123]")                                                                                            \
        CVT(124, c8, c9) EXP(c12) EXP(c13) CVT(125, c10, c11) EXP(c14) EXP(c15) CVT(126, c12, c13)
#define SA_ 32, 33, 34, 35, 36, 37, 38, 39, 40, 41, 42, 43, 44, 45, 46, 47
#define SB_ 48, 49, 50, 51, 52, 53, 54, 55, 56, 57, 58, 59, 60, 61, 62, 63
#define CALL(M, ...) M(__VA_ARGS__)
#define TE(KOFF, VOFF) CALL(STEP, RDV("v[112:115]", "v[116:119]", VOFF), SA_, 62, 63, "v[48:63]", "v[96:99]", "%[qb]", "v[144:159]", "v[64:79]", "v[80:95]", "v[104:107]", "v[116:119]") \
                       CALL(STEP, RDK("v[96:99]", KOFF), SB_, 46, 47, "v[32:47]", "v[100:103]", "%[qa]", "v[128:143]", "v[80:95]", "v[64:79]", "v[104:107]", "v[108:111]")
#define TO(KOFF, VOFF) CALL(STEP, RDV("v[104:107]", "v[108:111]", VOFF), SA_, 62, 63, "v[48:63]", "v[100:103]", "%[qb]", "v[144:159]", "v[64:79]", "v[80:95]", "v[112:115]", "v[108:111]") \
                       CALL(STEP, RDK("v[100:103]", KOFF), SB_, 46, 47, "v[32:47]", "v[96:99]", "%[qa]", "v[128:143]", "v[80:95]", "v[64:79]", "v[112:115]", "v[116:119]")
#define M64(a, b, c, d) "v_mov_b64 v[" #a ":" #b "], v[" #c ":" #d "]\n\t"
#define MOV4(a, b, c, d, src) "v_mov_b32 v" #a ", " src "\n\tv_mov_b32 v" #b ", " src "\n\tv_mov_b32 v" #c ", " src "\n\tv_mov_b32 v" #d ", " src "\n\t"
        asm volatile(
            "s_setprio 3\n\t"
            "ds_read_b128 v[96:99], %[kp]\n\t"
            "v_mov_b32 v128, %[na]\n\tv_mov_b32 v129, %[na]\n\tv_mov_b32 v144, %[nb]\n\tv_mov_b32 v145, %[nb]\n\t"
            "v_mov_b32 v104, %[one]\n\tv_mov_b32 v105, %[one]\n\tv_mov_b64 v[64:65], 0\n\t"
            M64(106, 107, 104, 105) M64(108, 109, 104, 105) M64(110, 111, 104, 105) M64(112, 113, 104, 105) M64(114, 115, 104, 105) M64(116, 117, 104, 105) M64(118, 119, 104, 105)
            "s_mov_b64 exec, %[msk]\n\t"
            "ds_read_b128 v[104:107], %[vp]\n\t"
            "ds_read_b128 v[108:111], %[vp] offset:512\n\t"
            "s_mov_b64 exec, -1\n\t"
            "ds_read_b128 v[100:103], %[kp] offset:1024\n\t"
            M64(130, 131, 128, 129) M64(132, 133, 128, 129) M64(134, 135, 128, 129) M64(136, 137, 128, 129) M64(138, 139, 128, 129) M64(140, 141, 128, 129) M64(142, 143, 128, 129)
            "s_waitcnt lgkmcnt(3)\n\t"
            "s_nop 1\n\t"
            "v_mfma_f32_32x32x16_bf16 v[32:47], v[96:99], %[qa], v[128:143]\n\t"
            M64(146, 147, 144, 145) M64(148, 149, 144, 145) M64(150, 151, 144, 145) M64(152, 153, 144, 145) M64(154, 155, 144, 145) M64(156, 157, 144, 145) M64(158, 159, 144, 145)
            M64(66, 67, 64, 65) M64(68, 69, 64, 65) M64(70, 71, 64, 65) M64(72, 73, 64, 65) M64(74, 75, 64, 65) M64(76, 77, 64, 65) M64(78, 79, 64, 65)
            M64(80, 81, 64, 65) M64(82, 83, 64, 65) M64(84, 85, 64, 65) M64(86, 87, 64, 65) M64(88, 89, 64, 65) M64(90, 91, 64, 65) M64(92, 93, 64, 65) M64(94, 95, 64, 65)
            M64(62, 63, 64, 65) M64(124, 125, 64, 65) M64(126, 127, 64, 65)
            "s_mov_b32 %[cnt], 0\n\t"
            "L_att_%=:\n\t"
            TE(2048, 1024) TO(3072, 2048) TE(4096, 3072) TO(5120, 4096)
            "v_add_u32 %[kp], 0x1000, %[kp]\n\t"
            "v_add_u32 %[vp], 0x1000, %[vp]\n\t"
            "s_cmp_lg_u32 %[cnt], 0\n\t"
            "s_cbranch_scc1 L_att_nb_%=\n\t"
            "s_barrier\n\t"
            "L_att_nb_%=:\n\t"
            "s_add_u32 %[cnt], %[cnt], 1\n\t"
            "s_cmp_eq_u32 %[cnt], 3\n\ts_cbranch_scc0 L_p1_%=\n\ts_setprio 2\n\tL_p1_%=:\n\t"
            "s_cmp_eq_u32 %[cnt], 6\n\ts_cbranch_scc0 L_p2_%=\n\ts_setprio 1\n\tL_p2_%=:\n\t"
            "s_cmp_eq_u32 %[cnt], 7\n\ts_cbranch_scc0 L_p3_%=\n\ts_setprio 0\n\tL_p3_%=:\n\t"
            "s_cmp_lt_u32 %[cnt], 8\n\t"
            "s_cbranch_scc1 L_att_%=\n\t"
            CVT(127, 62, 63)
            "s_nop 1\n\t"
            MPO("v[80:95]", "v[116:119]", "v[124:127]")
            "s_waitcnt lgkmcnt(0)\n\t"
            "s_nop 15\n\t"
            "v_mov_b32 v89, %[bb]\n\t"
            "v_mov_b32 %[o0], v64\n\tv_mov_b32 %[o1], v65\n\tv_mov_b32 %[o2], v66\n\tv_mov_b32 %[o3], v67\n\t"
            "v_mov_b32 %[o4], v68\n\tv_mov_b32 %[o5], v69\n\tv_mov_b32 %[o6], v70\n\tv_mov_b32 %[o7], v71\n\tv_mov_b32 %[o8], v72\n\t"
            "ds_write_b128 %[xw], v[80:83]\n\t"
            "ds_write_b128 %[xw], v[84:87] offset:1024\n\t"
            "ds_write_b64 %[xw2], v[88:89]\n\t"
            : [o0] "=&v"(o[0]), [o1] "=&v"(o[1]), [o2] "=&v"(o[2]), [o3] "=&v"(o[3]), [o4] "=&v"(o[4]), [o5] "=&v"(o[5]), [o6] "=&v"(o[6]), [o7] "=&v"(o[7]), [o8] "=&v"(o[8]),
              [kp] "+v"(kp), [vp] "+v"(vp), [cnt] "=&s"(cnt)
            : [qa] "v"(qa), [qb] "v"(qbv), [na] "v"(-bnda), [nb] "v"(-bndb), [bb] "v"(bndb), [msk] "s"(0x0000ffff0000ffffull), [xw] "v"(xw), [xw2] "v"(xw + 2048 - lane * 8), [one] "v"((r == 16 || r == 20) ? ONES : 0u)
            : "memory", "scc",
              "v32", "v33", "v34", "v35", "v36", "v37", "v38", "v39", "v40", "v41", "v42", "v43", "v44", "v45", "v46", "v47",
              "v48", "v49", "v50", "v51", "v52", "v53", "v54", "v55", "v56", "v57", "v58", "v59", "v60", "v61", "v62", "v63",
              "v64", "v65", "v66", "v67", "v68", "v69", "v70", "v71", "v72", "v73", "v74", "v75", "v76", "v77", "v78", "v79",
              "v80", "v81", "v82", "v83", "v84", "v85", "v86", "v87", "v88", "v89", "v90", "v91", "v92", "v93", "v94", "v95",
              "v96", "v97", "v98", "v99", "v100", "v101", "v102", "v103", "v104", "v105", "v106", "v107", "v108", "v109", "v110", "v111",
              "v112", "v113", "v114", "v115", "v116", "v117", "v118", "v119", "v120", "v121", "v122", "v123", "v124", "v125", "v126", "v127",
              "v128", "v129", "v130", "v131", "v132", "v133", "v134", "v135", "v136", "v137", "v138", "v139", "v140", "v141", "v142", "v143",
              "v144", "v145", "v146", "v147", "v148", "v149", "v150", "v151", "v152", "v153", "v154", "v155", "v156", "v157", "v158", "v159");
    } else {
        bar();
        const u32x4 ones = {ONES, ONES, ONES, ONES};
        const f32x16 zero = {};
#pragma unroll 1
        for (int sl = 1; sl >= 0; --sl) {
            const bf16x8 qf = __builtin_bit_cast(bf16x8, sl ? qbv : qa);
            bf16x8 v0 = __builtin_bit_cast(bf16x8, ones), v1 = v0, kf;
            f32x16 oo = {};
            float m = -INFINITY;
#pragma unroll 1
            for (int t = 0; t < 32; ++t) {
                asm volatile("ds_read_b128 %0, %3\n\ts_mov_b64 exec, %5\n\tds_read_b128 %1, %4\n\tds_read_b128 %2, %4 offset:512\n\ts_mov_b64 exec, -1\n\ts_waitcnt lgkmcnt(0)"
                             : "=&v"(kf), "+v"(v0), "+v"(v1) : "v"(ka + t * 1024), "v"(va + t * 1024), "s"(0x0000ffff0000ffffull) : "memory");
                const f32x16 s = MFMA(kf, qf, zero);
                float tm = s[0];
#pragma unroll
                for (int i = 1; i < 16; ++i) tm = fmaxf(tm, s[i]);
                tm = fmaxf(tm, other_half(tm));
                const float mn = fmaxf(m, tm);
                const float alpha = __builtin_amdgcn_exp2f(m - mn);
                m = mn;
#pragma unroll
                for (int i = 0; i < 16; ++i) oo[i] *= alpha;
                u32x4 p0, p1;
#pragma unroll
                for (int i = 0; i < 4; ++i) p0[i] = cvtpk(__builtin_amdgcn_exp2f(s[2 * i] - m), __builtin_amdgcn_exp2f(s[2 * i + 1] - m));
#pragma unroll
                for (int i = 0; i < 4; ++i) p1[i] = cvtpk(__builtin_amdgcn_exp2f(s[8 + 2 * i] - m), __builtin_amdgcn_exp2f(s[9 + 2 * i] - m));
                oo = MFMA(v0, __builtin_bit_cast(bf16x8, p0), oo);
                oo = MFMA(v1, __builtin_bit_cast(bf16x8, p1), oo);
            }
            if (sl == 1) {
                f32x4 t0 = {oo[0], oo[1], oo[2], oo[3]}, t1 = {oo[4], oo[5], oo[6], oo[7]};
                f32x2 t2 = {oo[8], m};
                *(LAS f32x4*)(uintptr_t)xw = t0; *(LAS f32x4*)(uintptr_t)(xw + 1024) = t1; *(LAS f32x2*)(uintptr_t)(xw + 2048 - lane * 8) = t2;
            } else {
#pragma unroll
                for (int i = 0; i < 9; ++i) o[i] = oo[i];
                ma = m;
            }
        }
    }
    ST(14);
    cbar();
    {
        const f32x4 t0 = *(LAS const f32x4*)(uintptr_t)xr, t1 = *(LAS const f32x4*)(uintptr_t)(xr + 1024);
        const f32x2 t2 = *(LAS const f32x2*)(uintptr_t)(xr + 2048 - lane * 8);
        const float mo = t2[1], mn = fmaxf(ma, mo);
        const float fa = __builtin_amdgcn_exp2f(ma - mn), fo = __builtin_amdgcn_exp2f(mo - mn);
        const float inv = __builtin_amdgcn_rcpf(o[8] * fa + t2[0] * fo);
        const float ia = fa * inv, io = fo * inv;
        const u32x4 ov = pack_swap(o[0] * ia + t0[0] * io, o[1] * ia + t0[1] * io, o[2] * ia + t0[2] * io, o[3] * ia + t0[3] * io,
                                   o[4] * ia + t1[0] * io, o[5] * ia + t1[1] * io, o[6] * ia + t1[2] * io, o[7] * ia + t1[3] * io);
        gst128(ws + WS_OF + ((size_t)(qb * 8 + la) * 32 + head) * 1024, lane * 16, ov);
    }
}

constexpr int P3_X = 153600;
template <int MODE>
DI void p35_compute(const Ptrs& P, char* smem, int tb, int cb, int w, int lane, u32x4& hkeep) {
    constexpr int NFULL = MODE == 0 ? 4 : 10;
    constexpr int A0 = MODE == 0 ? 65536 : 98304, NA = MODE == 0 ? 4 : 3, NW = MODE == 0 ? 4 : 6, XA = MODE == 0 ? 131072 : 147456;
    char* ws = P.ws;
    const int r = lane & 31, hh = lane >> 5;
    const unsigned lds0 = (unsigned)(uintptr_t)smem;
    const int ta = w & 1, cj = (w >> 1) & 1, kh = w >> 2;
    const int tile = tb * 2 + ta, token = tile * 32 + r, n0 = cb * 64 + cj * 32 + kh * 16;
    u32x2 res[2];
    const unsigned aro = lds0 + A0 + ta * 8192 + lane * 16 + kh * 4096;
    const unsigned wro = lds0 + hh * 1024 + ((lane & 15) >> 2) * 64 + ((lane >> 4) & 1) * 32 + (lane & 3) * 8 + cj * 256 + kh * 8192;
    f32x16 acc = {};
    bf16x8 A[4], Wf[4];
#define P35_LD(S, BA, BW, ks) { A[S] = lds_b128(aro + (BA) + (ks) * 1024); Wf[S] = lds_trfrag(wro + (BW) + (ks) * 2048, 512); }
    cbar();
    ST(MODE == 0 ? 7 : 8);
    P35_LD(0, 0, 0, 0); P35_LD(1, 0, 0, 1);
    unsigned BA = 0, BW = 0;
#pragma unroll 1
    for (int ch = 0; ch < NFULL; ++ch) {
        P35_LD(2, BA, BW, 2); SB(); acc = MFMA(Wf[0], A[0], acc); SB();
        P35_LD(3, BA, BW, 3); SB(); acc = MFMA(Wf[1], A[1], acc); SB();
        if (MODE == 1 || ch < NFULL - 1) {
            cbar();
            BA = BA == (NA - 1) * 16384 ? 0 : BA + 16384; BW = BW == (NW - 1) * 16384 ? 0 : BW + 16384;
            if (ch < NFULL - 1) { P35_LD(0, BA, BW, 0); }
        }
        SB(); acc = MFMA(Wf[2], A[2], acc); SB();
        if (ch < NFULL - 1) { P35_LD(1, BA, BW, 1); }
        SB(); acc = MFMA(Wf[3], A[3], acc); SB();
    }
    if constexpr (MODE == 1) {
#pragma unroll
        for (int j = 0; j < 3; ++j) {
            const bf16x8 af = lds_b128(aro + BA - kh * 4096 + (kh * 3 + j) * 1024);
            const bf16x8 wf = lds_trfrag(wro + BW - kh * 8192 + (kh * 3 + j) * 2048, 512);
            acc = MFMA(wf, af, acc);
        }
    }
    ST(MODE == 0 ? 9 : 0);
    {
        LAS f32x4* xw = (LAS f32x4*)(smem + XA + w * 2048 + lane * 16);
        f32x4 s0, s1;
#pragma unroll
        for (int e = 0; e < 4; ++e) { s0[e] = kh ? acc[e] : acc[8 + e]; s1[e] = kh ? acc[4 + e] : acc[12 + e]; }
        xw[0] = s0; xw[64] = s1;
    }
    cbar();
    float v[8];
    {
        LAS const f32x4* xr = (LAS const f32x4*)(smem + XA + (w ^ 4) * 2048 + lane * 16);
        const f32x4 s0 = xr[0], s1 = xr[64];
        if constexpr (MODE == 0) {
            const int row = ta * 32 + r;
#pragma unroll
            for (int g = 0; g < 2; ++g)
                res[g] = *(LAS const u32x2*)(smem + P3_X + row * 128 + (((cj * 4 + kh * 2 + g) ^ ((row >> 1) & 7)) << 4) + hh * 8);
        } else { res[0][0] = hkeep[0]; res[0][1] = hkeep[1]; res[1][0] = hkeep[2]; res[1][1] = hkeep[3]; }
#pragma unroll
        for (int e = 0; e < 4; ++e) { v[e] = (kh ? acc[8 + e] : acc[e]) + s0[e]; v[4 + e] = (kh ? acc[12 + e] : acc[4 + e]) + s1[e]; }
#pragma unroll
        for (int g = 0; g < 2; ++g) {
            v[4 * g] += bflo(res[g][0]); v[4 * g + 1] += bfhi(res[g][0]); v[4 * g + 2] += bflo(res[g][1]); v[4 * g + 3] += bfhi(res[g][1]);
        }
    }
    const unsigned p0 = cvtpk(v[0], v[1]), p1 = cvtpk(v[2], v[3]), p2 = cvtpk(v[4], v[5]), p3 = cvtpk(v[6], v[7]);
    const u32x4 o = swap4(p0, p1, p2, p3);
    if constexpr (MODE == 0) {
        hkeep[0] = p0; hkeep[1] = p1; hkeep[2] = p2; hkeep[3] = p3;
        gst128(ws + WS_HF + ((size_t)tile * 32 + (n0 >> 4)) * 1024, lane * 16, o);
        float s1 = 0.f, s2 = 0.f;
#pragma unroll
        for (int e = 0; e < 4; ++e) { s1 = dot2(o[e], ONES, s1); s2 = dot2(o[e], o[e], s2); }
        s1 = sum_halves(s1); s2 = sum_halves(s2);
        LAS f32x2* sl = (LAS f32x2*)(smem + XA + 8 * 2048);
        if (lane < 32) { f32x2 sv = {s1, s2}; sl[(ta * 32 + r) * 4 + cj * 2 + kh] = sv; }
        cbar();
        if (w == 0) {
            const f32x4 p = *(LAS const f32x4*)(sl + lane * 4), q = *(LAS const f32x4*)(sl + lane * 4 + 2);
            u32x2 sv = {__float_as_uint(p[0] + p[2] + q[0] + q[2]), __float_as_uint(p[1] + p[3] + q[1] + q[3])};
            gst64(ws + WS_ST + (size_t)tb * 64 * 64 + cb * 8, lane * 64, sv);
        }
    } else {
        *(u32x4*)(P.out + (size_t)token * 1024 + (n0 + 8 * hh) * 2) = o;
    }
}

constexpr int P4_ST = 147456, P4_CS = P4_ST + 8192;
template <int NB>
DI void p4_compute(char* ws, char* smem, int tb, int cg, int w, int lane) {
    const int r = lane & 31, hh = lane >> 5, i = w & 3, j0 = NB == 2 ? 0 : 2;
    const unsigned lds0 = (unsigned)(uintptr_t)smem;
    const int tile = tb * 4 + i;
    const unsigned hro = lds0 + 98304 + i * 4096 + lane * 16;
    const unsigned wro = lds0 + hh * 1536 + ((lane & 15) >> 2) * 64 + ((lane >> 4) & 1) * 32 + (lane & 3) * 8 + j0 * 256;
    f32x16 ag[NB], au[NB];
#pragma unroll
    for (int j = 0; j < NB; ++j) { ag[j] = f32x16{}; au[j] = f32x16{}; }
    bf16x8 H[4], G[4][NB], U[4][NB];
#define P4_LOAD(S, BH, BW, ks)                                                                                                    \
    {                                                                                                                             \
        H[S] = lds_b128(hro + (BH) + (ks) * 1024);                                                                                 \
        _Pragma("unroll") for (int j = 0; j < NB; ++j) { G[S][j] = lds_trfrag(wro + (BW) + (ks) * 3072 + j * 256, 768); U[S][j] = lds_trfrag(wro + (BW) + 12288 + (ks) * 3072 + j * 256, 768); } \
    }
#define P4_MMA(S) { _Pragma("unroll") for (int j = 0; j < NB; ++j) { ag[j] = MFMA(G[S][j], H[S], ag[j]); au[j] = MFMA(U[S][j], H[S], au[j]); } }
    cbar();
    ST(6);
    P4_LOAD(0, 0, 0, 0); P4_LOAD(1, 0, 0, 1);
    unsigned BH = 0, BW = 0;
#pragma unroll 1
    for (int ch = 0; ch < 8; ++ch) {
        P4_LOAD(2, BH, BW, 2); SB(); P4_MMA(0); SB();
        P4_LOAD(3, BH, BW, 3); SB(); P4_MMA(1); SB();
        cbar();
        if (ch < 7) {
            BH = BH == 2 * 16384 ? 0 : BH + 16384; BW = BW == 3 * 24576 ? 0 : BW + 24576;
            P4_LOAD(0, BH, BW, 0);
        }
        SB(); P4_MMA(2); SB();
        if (ch < 7) { P4_LOAD(1, BH, BW, 1); }
        SB(); P4_MMA(3); SB();
    }
    ST(15);
    float mean, rstd;
    {
        float s1 = 0.f, s2 = 0.f;
        const LAS f32x4* sp = (const LAS f32x4*)(smem + P4_ST + (i * 32 + r) * 64);
#pragma unroll
        for (int k = 0; k < 4; ++k) { const f32x4 v = sp[k]; s1 += v[0] + v[2]; s2 += v[1] + v[3]; }
        mean = s1 * (1.f / DM);
        rstd = __builtin_amdgcn_rsqf(fmaxf(s2 * (1.f / DM) - mean * mean, 0.f) + LN_EPS);
    }
    constexpr float C1 = -2.f * 0.7978845608028654f * LOG2E, C2 = C1 * 0.044715f;
    const float k1 = C1 * rstd, k2 = C2 * rstd * rstd * rstd, k3 = __builtin_amdgcn_rcpf(rstd * rstd);
    const f32x2 nm2 = {-mean, -mean}, k12 = {k1, k1}, k22 = {k2, k2}, k32 = {k3, k3};
#pragma unroll
    for (int j = 0; j < NB; ++j) {
        const int blk = cg * 3 + j0 + j;
        float v[16];
#pragma unroll
        for (int g = 0; g < 4; ++g) {
            const LAS f32x4* cp = (const LAS f32x4*)(smem + P4_CS + ((j0 + j) * 32 + 8 * g + 4 * hh) * 4);
            const f32x4 cgf = cp[0], cuf = cp[32];
#pragma unroll
            for (int e = 0; e < 4; e += 2) {
                const f32x2 ga = {ag[j][4 * g + e], ag[j][4 * g + e + 1]}, ua = {au[j][4 * g + e], au[j][4 * g + e + 1]};
                const f32x2 cg2 = {cgf[e], cgf[e + 1]}, cu2 = {cuf[e], cuf[e + 1]};
                const f32x2 a = __builtin_elementwise_fma(nm2, cg2, ga), bq = __builtin_elementwise_fma(nm2, cu2, ua);
                const f32x2 arg = a * __builtin_elementwise_fma(a * a, k22, k12);
                const f32x2 ex = {__builtin_amdgcn_exp2f(arg[0]), __builtin_amdgcn_exp2f(arg[1])};
                const f32x2 den = __builtin_elementwise_fma(ex, k32, k32);
                const f32x2 rc = {__builtin_amdgcn_rcpf(den[0]), __builtin_amdgcn_rcpf(den[1])};
                const f32x2 y = (a * rc) * bq;
                v[4 * g + e] = y[0]; v[4 * g + e + 1] = y[1];
            }
        }
        if (blk < 43) {
#pragma unroll
            for (int g = 0; g < 2; ++g) {
                const u32x4 o = pack_swap(v[8 * g], v[8 * g + 1], v[8 * g + 2], v[8 * g + 3], v[8 * g + 4], v[8 * g + 5], v[8 * g + 6], v[8 * g + 7]);
                gst128(ws + WS_AF + ((size_t)tile * 86 + blk * 2 + g) * 1024, lane * 16, o);
            }
        }
    }
}

__global__ __launch_bounds__(640, 3) void k_block(const char* __restrict__ x, const char* __restrict__ wqkv, const char* __restrict__ wo, const char* __restrict__ wg,
                                                  const char* __restrict__ wu, const char* __restrict__ wd, char* __restrict__ ws, char* __restrict__ out) {
    extern __shared__ __attribute__((aligned(16))) char smem[];
    const Ptrs P = {x, wqkv, wo, wg, wu, wd, ws, out};
    const int b = blockIdx.x, xcd = b & 7, idx = b >> 3;
    const int lane = threadIdx.x & 63, w = rfl(threadIdx.x >> 6);
    const unsigned lds0 = (unsigned)(uintptr_t)smem;
    if (w >= 8 && blockIdx.x < 64) {
        const char* pc = (const char*)(__builtin_amdgcn_s_getpc() & ~127ull);
        asm volatile("s_mov_b32 m0, %0\n\ts_nop 0" ::"s"(lds0 + LDS_BYTES - 256) : "memory");
#pragma unroll
        for (int i = 0; i < (CODE_LINES + 63) / 64; ++i) { const int ln = i * 64 + lane; gtouch(pc, (ln < CODE_LINES ? ln : CODE_LINES - 1) * 128); }
    }
    const bool p1_gemm = idx >= 8;
    const int gi = idx - 8;
    const int tb1 = (xcd >> 1) * 4 + (gi & 3), cb1 = (xcd & 1) * 6 + (gi >> 2);
    const int head = xcd * 4 + (idx >> 3), qb = idx & 7;
    const int tb3 = xcd * 4 + (idx & 3), cb3 = idx >> 2;
    const bool p4_on = !(xcd & 1) || idx < 28;
    const int tb4 = (xcd >> 1) * 4 + (idx & 3), cg4 = (xcd & 1) * 8 + (idx >> 2);
    ST(0);

    if (w < 8) {
        if (p1_gemm) {
            if (cb1 < 8) p1_compute<false>(ws, tb1, cb1, smem, w, lane); else p1_compute<true>(ws, tb1, cb1, smem, w, lane);
        } else p1_helper(x, wqkv, wg, wu, ws, idx * 8 + xcd, w, lane);
        ST(1);
        pbar();
        p2_compute(ws, smem, head, qb, w, lane);
        ST(2);
        pbar();
        u32x4 hkeep;
        p35_compute<0>(P, smem, tb3, cb3, w, lane, hkeep);
        ST(3);
        pbar();
        if (p4_on) {
            if (w < 4) p4_compute<2>(ws, smem, tb4, cg4, w, lane); else p4_compute<1>(ws, smem, tb4, cg4, w, lane);
        } else bar();
        ST(4);
        pbar();
        p35_compute<1>(P, smem, tb3, cb3, w, lane, hkeep);
        ST(10);
        return;
    }

    const int L = w - 8;
    if (L == 0) {
        unsigned o_ = (unsigned)WS_FLAG + (lane & 3) * 256 * 128 + b * 128;
        if (lane == 4) o_ = (unsigned)WS_G1 + b * 128;
        if (lane >= 5 && lane < 9) o_ = (unsigned)WS_ST + (b * 4 + lane - 5) * 128;
        if (lane == 9) o_ = (unsigned)WS_KM + (b & 63) * 128;
        if (lane == 10) o_ = (unsigned)WS_CS + (b < 86 ? b : 0) * 128;
        if (lane >= 11 && lane < 17) o_ = (unsigned)WS_G2 + (b % 64) * 768 + (lane - 11) * 128;
        asm volatile("s_mov_b32 m0, %0\n\ts_nop 0\n\tglobal_load_lds_dword %1, %2 sc1" ::"s"(lds0 + LDS_BYTES - 256), "v"(o_), "s"(ws) : "memory");
    }
    const char* flags = ws + WS_FLAG;
    const unsigned lin = lane * 16;
    const unsigned tag = p1_gemm ? p1_load(P, L, tb1, cb1, P1ROT, gi, lds0, lane) : rfl(gld32_wait(ws + WS_EPOCH, 0)) + 1;
    bar();
    if (L == 0 && lane == 0) gst32(ws + WS_FLAG, b * 128, tag);
    ST(1);
    {
        const int ptb = lane < 32 ? (lane & 15) : 2 * qb + (lane & 1), pcb = (lane < 16 ? 4 : lane < 32 ? 8 : 0) + (head >> 3);
        const int pb = ((pcb % 6) * 4 + (ptb & 3) + 8) * 8 + (ptb >> 2) * 2 + pcb / 6;
        poll(flags, pb * 128, lane < 34, tag);
        ST(6);
        const int rot = qb * 4;
        const char* src = ws + (L == 0 ? WS_KF : WS_VF) + (size_t)head * 65536;
        const unsigned dst = lds0 + L * 65536;
        {
            const char* q = ws + WS_QF + ((size_t)head * 64 + qb * 8) * 1024 + L * 4096;
            dma4lin(q, lin, lds0 + P2_X + L * 4096);
            if (L == 0) dma1dw(ws + WS_KM + head * 256, lane * 4, lds0 + P2_KM);
        }
#pragma unroll 1
        for (int g = 0; g < 8; ++g) {
            const unsigned so = ((g * 4 + rot) & 31) * 1024;
            dma4lin(src + so, lin, dst + g * 4096); dma4lin(src + 32768 + so, lin, dst + 32768 + g * 4096);
            if (g == 1) lbar<0>();
        }
        ST(7);
        if (L == 1) {
            asm volatile("s_waitcnt vmcnt(0)" ::: "memory");
        }
        lbar<0>();
        if (L == 1) {
#pragma unroll 1
            for (int g = 0; g < 4; ++g) poll(flags, (g * 64 + lane) * 128, true, tag);
            if (b == 0 && lane == 0) gst32(ws + WS_EPOCH, 0, tag);
            asm volatile("s_mov_b32 m0, %0\n\ts_nop 0" ::"s"(lds0 + LDS_BYTES - 256) : "memory");
            gtouch(wo, (idx * 128 + lane) * 128); gtouch(wo, (idx * 128 + 64 + lane) * 128);
            {
                const int nl = (xcd & 1) ? 10 : 12, ln = lane & 15;
                const unsigned co = (xcd & 1) * 1536 + (ln < nl ? ln : 0) * 128;
#pragma unroll
                for (int i = 0; i < 4; ++i) {
                    unsigned o_ = (idx * 16 + i * 4 + (lane >> 4)) * (FF * 2) + co;
                    o_ = o_ > 512u * FF * 2 - 4 ? 512u * FF * 2 - 4 : o_;
                    gtouch(wg, o_); gtouch(wu, o_);
                }
            }
#pragma unroll
            for (int i = 0; i < 6; ++i) { int ln = i * 64 + lane; ln = ln > 343 ? 343 : ln; gtouch(wd, (idx * 344 + ln) * 128); }
        }
        bar();
    }
    unsigned wvo[4];
    {
        const unsigned wv = ((lane >> 5) * 4 + ((lane >> 2) & 3)) * 1024 + ((lane >> 4) & 1) * 64 + (lane & 3) * 16;
#pragma unroll
        for (int i = 0; i < 4; ++i) wvo[i] = wv + i * 8192 - i * 1024;
    }
    {
        const int rot = cb3 & 3;
        bar();
        if (L == 1) {
            {
                const int rl = lane >> 3, cs = lane & 7;
                unsigned xv[4];
#pragma unroll
                for (int i = 0; i < 4; ++i) xv[i] = (8 * i + rl) * 1024 + ((cs ^ ((4 * (i & 1) + (rl >> 1)) & 7)) << 4) - i * 1024;
                const char* xs = x + (size_t)tb3 * 64 * 1024 + cb3 * 128;
                dma4(xs, xv[0], xv[1], xv[2], xv[3], lds0 + P3_X); dma4(xs + 32768, xv[0], xv[1], xv[2], xv[3], lds0 + P3_X + 4096);
            }
#pragma unroll 1
            for (int c = 0; c < 4; ++c) {
                const char* src = wo + cb3 * 128 + (size_t)((c + rot) & 3) * 128 * 1024;
#pragma unroll
                for (int g = 0; g < 4; ++g) dma4(src + g * 32768, wvo[0], wvo[1], wvo[2], wvo[3], lds0 + c * 16384 + g * 4096);
            }
        }
        if (L == 0) {
            if (lane == 0) gst32(ws + WS_FLAG + 256 * 128, b * 128, tag);
            ST(2);
            const int ph = lane & 31;
            poll(flags + 256 * 128, (((ph & 3) * 8 + (tb3 >> 2)) * 8 + (ph >> 2)) * 128, true, tag);
            ST(8);
            const char* ab = ws + WS_OF + (size_t)tb3 * 2 * 32 * 1024;
#pragma unroll 1
            for (int c = 0; c < 4; ++c) {
                const char* src = ab + ((c + rot) & 3) * 8192;
                const unsigned dst = lds0 + 65536 + c * 16384;
                dma4lin(src, lin, dst); dma4lin(src + 4096, lin, dst + 4096); dma4lin(src + 32768, lin, dst + 8192); dma4lin(src + 32768 + 4096, lin, dst + 12288);
                if (c == 1) lbar<16>();
            }
            lbar<32>(); lbar<16>(); lbar<0>();
        } else {
            lbar<0>(); bar(); bar(); bar();
        }
        bar();
    }
    {
        const int rot = cg4 & 7;
        unsigned gvo[12];
        if (L == 1 && p4_on) {
#pragma unroll
            for (int j = 0; j < 12; ++j) {
                const int s = j * 64 + lane, st = s >> 4, kb = st / 3, nb = st % 3, q = (s >> 2) & 3, c = s & 3;
                int blk = cg4 * 3 + nb; blk = blk > 42 ? 42 : blk;
                gvo[j] = (4 * kb + q) * (FF * 2) + blk * 64 + c * 16 - (j & 3) * 1024;
            }
        }
        auto issue_w = [&](int c, unsigned B) {
            const size_t so = (size_t)((c + rot) & 7) * 64 * (FF * 2);
#pragma unroll
            for (int g = 0; g < 3; ++g) dma4(wg + so, gvo[4 * g], gvo[4 * g + 1], gvo[4 * g + 2], gvo[4 * g + 3], lds0 + B + g * 4096);
#pragma unroll
            for (int g = 0; g < 3; ++g) dma4(wu + so, gvo[4 * g], gvo[4 * g + 1], gvo[4 * g + 2], gvo[4 * g + 3], lds0 + B + 12288 + g * 4096);
        };
        bar();
        bar();
        if (L == 1 && p4_on) { issue_w(0, 0); issue_w(1, 24576); issue_w(2, 2 * 24576); issue_w(3, 3 * 24576); }
        if (L == 0) { if (lane == 0) gst32(ws + WS_FLAG + 2 * 256 * 128, b * 128, tag); ST(3); }
        if (p4_on) {
            if (L == 0) {
                const int ptb = tb4 * 2 + (lane & 1), pcb = (lane >> 1) & 7;
                poll(flags + 2 * 256 * 128, ((pcb * 4 + (ptb & 3)) * 8 + (ptb >> 2)) * 128, true, tag);
                ST(9);
                const char* hb = ws + WS_HF + (size_t)tb4 * 4 * 32 * 1024;
                auto issue_h = [&](int c, unsigned B) {
                    const char* src = hb + ((c + rot) & 7) * 4096;
#pragma unroll
                    for (int i = 0; i < 4; ++i) dma4lin(src + i * 32768, lin, lds0 + 98304 + B + i * 4096);
                };
                issue_h(0, 0); issue_h(1, 16384);
                lbar<16>();
                {
                    const char* sb = ws + WS_ST + (size_t)tb4 * 128 * 64;
                    dma4lin(sb, lin, lds0 + P4_ST); dma4lin(sb + 4096, lin, lds0 + P4_ST + 4096);
                    const char* cb_ = ws + WS_CS + cg4 * 384;
                    dma1dw(cb_, lane * 4, lds0 + P4_CS); dma1dw(cb_ + 256, lane * 4, lds0 + P4_CS + 256);
                    dma1dw(cb_ + FF * 4, lane * 4, lds0 + P4_CS + 512); dma1dw(cb_ + FF * 4 + 256, lane * 4, lds0 + P4_CS + 768);
                }
                issue_h(2, 2 * 16384);
                unsigned B = 0;
#pragma unroll 1
                for (int c = 1; c < 8; ++c) {
                    if (c < 7) lbar<16>(); else lbar<0>();
                    if (c + 2 < 8) { issue_h(c + 2, B); B = B == 2 * 16384 ? 0 : B + 16384; }
                }
            } else {
                unsigned B = 0;
#pragma unroll 1
                for (int c = 0; c < 8; ++c) {
                    if (c == 0) lbar<63>(); else if (c < 6) lbar<48>(); else if (c == 6) lbar<24>(); else lbar<0>();
                    if (c >= 1 && c + 3 < 8) { issue_w(c + 3, B); B = B == 3 * 24576 ? 0 : B + 24576; }
                }
            }
            bar();
        } else bar();
    }
    {
        const int rot = cb3;
        auto issue_w = [&](int c, unsigned B) {
            const int cc = c < 10 ? (c + rot) % 10 : 10;
            const char* src = wd + cb3 * 128 + (size_t)cc * 128 * 1024;
#pragma unroll
            for (int g = 0; g < 3; ++g) dma4(src + g * 32768, wvo[0], wvo[1], wvo[2], wvo[3], lds0 + B + g * 4096);
            dma4(src + (c < 10 ? 3 : 2) * 32768, wvo[0], wvo[1], wvo[2], wvo[3], lds0 + B + 3 * 4096);
        };
        bar();
        if (L == 1) {
#pragma unroll 1
            for (int c = 0; c < 6; ++c) issue_w(c, c * 16384);
        }
        if (L == 0) {
            if (p4_on && lane == 0) gst32(ws + WS_FLAG + 3 * 256 * 128, b * 128, tag);
            ST(4);
            const int ptb = tb3 >> 1, pcg = lane < 15 ? lane : 0;
            poll(flags + 3 * 256 * 128, (((pcg & 7) * 4 + (ptb & 3)) * 8 + (ptb >> 2) * 2 + (pcg >> 3)) * 128, true, tag);
            ST(5);
            const char* ab = ws + WS_AF + (size_t)tb3 * 2 * 86 * 1024;
            auto issue_a = [&](int c, unsigned B) {
                const int cc = c < 10 ? (c + rot) % 10 : 10;
                const char* src = ab + cc * 8192;
                const unsigned dst = lds0 + 98304 + B;
                const int o2 = c < 10 ? 4096 : 2048;
                dma4lin(src, lin, dst); dma4lin(src + o2, lin, dst + o2);
                dma4lin(src + 86 * 1024, lin, dst + 8192); dma4lin(src + 86 * 1024 + o2, lin, dst + 8192 + o2);
            };
            issue_a(0, 0); issue_a(1, 16384);
            unsigned B = 2 * 16384;
#pragma unroll 1
            for (int c = 0; c < 11; ++c) {
                if (c < 10) lbar<16>(); else lbar<0>();
                if (c + 2 < 11) { issue_a(c + 2, B); B = B == 2 * 16384 ? 0 : B + 16384; }
            }
        } else {
            unsigned B = 0;
#pragma unroll 1
            for (int c = 0; c < 11; ++c) {
                if (c < 7) lbar<63>(); else if (c == 7) lbar<48>(); else if (c == 8) lbar<32>(); else if (c == 9) lbar<16>(); else lbar<0>();
                if (c >= 1 && c + 5 < 11) { issue_w(c + 5, B); B = B == 5 * 16384 ? 0 : B + 16384; }
            }
        }
        bar();
    }
}

extern "C" void kernel_launch(void* const* d_in, const int* in_sizes, int n_in, void* d_out, int out_size, void* d_ws, size_t ws_size, hipStream_t stream) {
    (void)hipFuncSetAttribute((const void*)k_block, hipFuncAttributeMaxDynamicSharedMemorySize, LDS_BYTES);
    k_block<<<256, 640, LDS_BYTES, stream>>>((const char*)d_in[0], (const char*)d_in[1], (const char*)d_in[2], (const char*)d_in[4], (const char*)d_in[5], (const char*)d_in[6],
                                             (char*)d_ws, (char*)d_out);
}
```

```cpp
#include <hip/hip_runtime.h>
#include <hip/hip_bf16.h>
#include <stdint.h>

typedef __bf16 bf16x8 __attribute__((ext_vector_type(8)));
typedef __bf16 bf16x2 __attribute__((ext_vector_type(2)));
typedef short s16x4 __attribute__((ext_vector_type(4)));
typedef short s16x8 __attribute__((ext_vector_type(8)));
typedef float f32x16 __attribute__((ext_vector_type(16)));
typedef float f32x4 __attribute__((ext_vector_type(4)));
typedef float f32x2 __attribute__((ext_vector_type(2)));
typedef unsigned u32x4 __attribute__((ext_vector_type(4)));
typedef unsigned u32x2 __attribute__((ext_vector_type(2)));
#define LAS __attribute__((address_space(3)))
#define DI __device__ __forceinline__

constexpr int DM = 512, FF = 1376;
constexpr float LOG2E = 1.4426950408889634f;
constexpr float QSCALE = 0.25f * LOG2E;
constexpr float LN_EPS = 1e-5f;

constexpr size_t WS_QF = 0, WS_KF = 2u << 20, WS_VF = 4u << 20, WS_OF = 6u << 20, WS_HF = 8u << 20, WS_AF = 10u << 20,
                 WS_ST = 16u << 20, WS_CS = 17u << 20, WS_KM = (17u << 20) + 65536, WS_FLAG = 18u << 20, WS_EPOCH = WS_FLAG + 4 * 256 * 128,
                 WS_G1 = 19u << 20, WS_G2 = WS_G1 + 65536;
constexpr int LDS_BYTES = 163840;
constexpr unsigned SPIN_CAP = 1u << 17;
#define CODE_LINES_ 280
constexpr int CODE_LINES = CODE_LINES_;

#define MFMA(a, b, c) __builtin_amdgcn_mfma_f32_32x32x16_bf16(a, b, c, 0, 0, 0)
#define SB() __builtin_amdgcn_sched_barrier(0)
DI void cbar() { asm volatile("s_waitcnt lgkmcnt(0)\n\ts_barrier" ::: "memory"); }
DI void pbar() { asm volatile("s_waitcnt vmcnt(0) lgkmcnt(0)\n\ts_barrier" ::: "memory"); }
template <int N> DI void lbar() { asm volatile("s_waitcnt vmcnt(%0)\n\ts_barrier" ::"i"(N > 63 ? 63 : N) : "memory"); }
DI void bar() { asm volatile("s_barrier" ::: "memory"); }

DI void dma4(const void* sbase, unsigned v0, unsigned v1, unsigned v2, unsigned v3, unsigned dst) {
    asm volatile("s_mov_b32 m0, %5\n\ts_nop 0\n\tglobal_load_lds_dwordx4 %0, %4 sc1\n\tglobal_load_lds_dwordx4 %1, %4 offset:1024 sc1\n\t"
                 "global_load_lds_dwordx4 %2, %4 offset:2048 sc1\n\tglobal_load_lds_dwordx4 %3, %4 offset:3072 sc1"
                 :: "v"(v0), "v"(v1), "v"(v2), "v"(v3), "s"(sbase), "s"(dst) : "memory");
}
DI void dma4lin(const void* sbase, unsigned v, unsigned dst) { dma4(sbase, v, v, v, v, dst); }
DI void dma2lin(const void* sbase, unsigned v, unsigned dst) {
    asm volatile("s_mov_b32 m0, %2\n\ts_nop 0\n\tglobal_load_lds_dwordx4 %0, %1 sc1\n\tglobal_load_lds_dwordx4 %0, %1 offset:1024 sc1" :: "v"(v), "s"(sbase), "s"(dst) : "memory");
}
DI void dma1dw(const void* sbase, unsigned v, unsigned dst) {
    asm volatile("s_mov_b32 m0, %2\n\ts_nop 0\n\tglobal_load_lds_dword %0, %1 sc1" :: "v"(v), "s"(sbase), "s"(dst) : "memory");
}
DI void gtouch(const void* sbase, unsigned voff) {
    asm volatile("global_load_lds_dword %0, %1" :: "v"(voff), "s"(sbase) : "memory");
}
DI unsigned gld32_wait(const void* sbase, unsigned voff) {
    unsigned r; asm volatile("global_load_dword %0, %1, %2 sc1\n\ts_waitcnt vmcnt(0)" : "=&v"(r) : "v"(voff), "s"(sbase) : "memory"); return r;
}
DI void gst128(void* sbase, unsigned voff, u32x4 v) { asm volatile("global_store_dwordx4 %0, %1, %2 sc1\n\ts_nop 1" ::"v"(voff), "v"(v), "s"(sbase) : "memory"); }
DI void gst64(void* sbase, unsigned voff, u32x2 v) { asm volatile("global_store_dwordx2 %0, %1, %2 sc1\n\ts_nop 1" ::"v"(voff), "v"(v), "s"(sbase) : "memory"); }
DI void gst32(void* sbase, unsigned voff, unsigned v) { asm volatile("global_store_dword %0, %1, %2 sc1\n\ts_nop 1" ::"v"(voff), "v"(v), "s"(sbase) : "memory"); }
#define VWAIT_(N) asm volatile("s_waitcnt vmcnt(" #N ")" ::: "memory")
#define VWAIT(N) VWAIT_(N)
#define PIN(x) asm volatile("" : "+v"(x))
DI unsigned rfl(unsigned x) { return __builtin_amdgcn_readfirstlane(x); }
DI unsigned cvtpk(float lo, float hi) {
    f32x2 v = {lo, hi};
    bf16x2 b = __builtin_convertvector(v, bf16x2);
    return __builtin_bit_cast(unsigned, b);
}
DI float bflo(unsigned u) { return __uint_as_float(u << 16); }
DI float bfhi(unsigned u) { return __uint_as_float(u & 0xffff0000u); }
DI bf16x8 lds_b128(unsigned a) { return *(LAS const bf16x8*)(uintptr_t)a; }
typedef short v4i16_t __attribute__((ext_vector_type(4)));
DI s16x4 lds_tr(unsigned a) { return __builtin_bit_cast(s16x4, __builtin_amdgcn_ds_read_tr16_b64_v4i16((LAS v4i16_t*)(uintptr_t)a)); }
DI bf16x8 lds_trfrag(unsigned a, int off2) {
    s16x4 x = lds_tr(a), y = lds_tr(a + off2);
    s16x8 r = __builtin_shufflevector(x, y, 0, 1, 2, 3, 4, 5, 6, 7);
    return __builtin_bit_cast(bf16x8, r);
}
DI float dot2(unsigned a, unsigned b, float c) {
    return __builtin_amdgcn_fdot2_f32_bf16(__builtin_bit_cast(bf16x2, a), __builtin_bit_cast(bf16x2, b), c, false);
}
DI float other_half(float v) {
    auto r = __builtin_amdgcn_permlane32_swap(__float_as_uint(v), __float_as_uint(v), false, false);
    return __uint_as_float((threadIdx.x & 32) ? r[0] : r[1]);
}
DI float sum_halves(float v) {
    auto r = __builtin_amdgcn_permlane32_swap(__float_as_uint(v), __float_as_uint(v), false, false);
    return __uint_as_float(r[0]) + __uint_as_float(r[1]);
}
template <bool ALL> DI float dpp_max(float v) {
#define DPPMAX(ctrl, rm) v = fmaxf(v, __int_as_float(__builtin_amdgcn_update_dpp(0, __float_as_int(v), ctrl, rm, 0xf, true)))
    DPPMAX(0x111, 0xf); DPPMAX(0x112, 0xf); DPPMAX(0x114, 0xf); DPPMAX(0x118, 0xf); DPPMAX(0x142, 0xa);
    if (ALL) DPPMAX(0x143, 0xc);
    return v;
}
template <int CTRL> DI float dpp_add(float v) { return v + __int_as_float(__builtin_amdgcn_update_dpp(0, __float_as_int(v), CTRL, 0xf, 0xf, true)); }
DI u32x4 swap4(unsigned p0, unsigned p1, unsigned p2, unsigned p3) {
    auto s0 = __builtin_amdgcn_permlane32_swap(p0, p2, false, false);
    auto s1 = __builtin_amdgcn_permlane32_swap(p1, p3, false, false);
    u32x4 r = {s0[0], s1[0], s0[1], s1[1]};
    return r;
}
DI u32x4 pack_swap(float a0, float a1, float a2, float a3, float a4, float a5, float a6, float a7) {
    return swap4(cvtpk(a0, a1), cvtpk(a2, a3), cvtpk(a4, a5), cvtpk(a6, a7));
}
constexpr unsigned ONES = 0x3f803f80u;

#define ST(k)
#define ST2(k)

DI void poll(const char* fl, unsigned voff, bool active, unsigned tag) {
    for (unsigned spins = 0; spins < SPIN_CAP; ++spins) {
        unsigned f;
        asm volatile("global_load_dword %0, %1, %2 sc1\n\ts_waitcnt vmcnt(0)" : "=&v"(f) : "v"(voff), "s"(fl) : "memory");
        if (__all(!active || f == tag)) break;
        __builtin_amdgcn_s_sleep(1);
    }
}

struct Ptrs { const char *x, *wqkv, *wo, *wg, *wu, *wd; char* ws; char* out; };

#define NOTOUCH1
#define P1ROT 0
#define T1W 16
#define P1NB 4
constexpr int P1_BUF = 32768, P1_MISC = P1NB * P1_BUF;

DI void gst_granule(char* sbase, unsigned voff, float v, unsigned tag) { u32x2 g = {__float_as_uint(v), tag}; gst64(sbase, voff, g); }
DI float sum_x16(float v) {
    auto r = __builtin_amdgcn_permlane16_swap(__float_as_uint(v), __float_as_uint(v), false, false);
    return __uint_as_float(r[0]) + __uint_as_float(r[1]);
}
template <bool ISV>
DI void p1_epilogue(char* ws, int tb, int cb, char* smem, int tl, int cp, const f32x16& accA, const f32x16& accB, int lane) {
    const int r = lane & 31, hh = lane >> 5;
    const LAS f32x2* st = (const LAS f32x2*)(smem + P1_MISC);
    const LAS float* csl = (const LAS float*)(smem + P1_MISC + 1024);
    const int sec = cb >> 2, tile = tb * 4 + tl;
#pragma unroll
    for (int b = 0; b < 2; ++b) {
        const f32x16& acc = b ? accB : accA;
        const int ct = 2 * cp + b, head0 = (cb & 3) * 8 + ct * 2;
        float v[16];
        if constexpr (!ISV) {
            const f32x2 ms = st[tl * 32 + r];
            const float sc = ms[1] * (sec == 0 ? QSCALE : 1.f), msc = -ms[0] * sc;
#pragma unroll
            for (int g = 0; g < 4; ++g) {
                const f32x4 c4 = *(LAS const f32x4*)(csl + ct * 32 + 8 * g + 4 * hh);
#pragma unroll
                for (int e = 0; e < 4; ++e) v[4 * g + e] = acc[4 * g + e] * sc + msc * c4[e];
            }
            char* dst = ws + (sec == 0 ? WS_QF : WS_KF) + ((size_t)head0 * 64 + tile) * 1024;
#pragma unroll
            for (int hs = 0; hs < 2; ++hs) {
                const u32x4 o = pack_swap(v[8 * hs], v[8 * hs + 1], v[8 * hs + 2], v[8 * hs + 3], v[8 * hs + 4], v[8 * hs + 5], v[8 * hs + 6], v[8 * hs + 7]);
                gst128(dst + hs * 65536, lane * 16, o);
                if (sec == 1) {
                    float n2 = 0.f;
#pragma unroll
                    for (int e = 0; e < 8; ++e) n2 = fmaf(v[8 * hs + e], v[8 * hs + e], n2);
                    n2 = dpp_max<false>(sum_halves(n2));
                    if (lane == 31) gst32(ws + WS_KM + (head0 + hs) * 256 + tile * 4, 0, __float_as_uint(n2));
                }
            }
        } else {
            const float csn = csl[ct * 32 + r];
#pragma unroll
            for (int g = 0; g < 4; ++g) {
                const LAS f32x4* sp = (const LAS f32x4*)(st + tl * 32 + 8 * g + 4 * hh);
                const f32x4 pa = sp[0], pb = sp[1];
                v[4 * g] = (acc[4 * g] - pa[0] * csn) * pa[1]; v[4 * g + 1] = (acc[4 * g + 1] - pa[2] * csn) * pa[3];
                v[4 * g + 2] = (acc[4 * g + 2] - pb[0] * csn) * pb[1]; v[4 * g + 3] = (acc[4 * g + 3] - pb[2] * csn) * pb[3];
            }
            char* dst = ws + WS_VF + ((size_t)head0 * 64 + tile) * 1024;
            const unsigned vo = (r >> 4) * 65536 + hh * 256 + (r & 15) * 16;
#pragma unroll
            for (int s = 0; s < 2; ++s) {
                u32x4 o = {cvtpk(v[8 * s], v[8 * s + 1]), cvtpk(v[8 * s + 2], v[8 * s + 3]), cvtpk(v[8 * s + 4], v[8 * s + 5]), cvtpk(v[8 * s + 6], v[8 * s + 7])};
                gst128(dst + s * 512, vo, o);
            }
        }
    }
}
constexpr int P1_XCH = (5 % P1NB) * P1_BUF;

DI void p1_colsum(const char* wqkv, char* ws, int tb, int cb, char* smem, int j, int lane, f32x16& accA, f32x16& accB) {
    const unsigned tag = __hip_atomic_load((const __attribute__((address_space(1))) unsigned*)(ws + WS_EPOCH), __ATOMIC_RELAXED, __HIP_MEMORY_SCOPE_AGENT) + 1;
    const char* src = wqkv + (size_t)((tb & 3) * 128 + j * 32 + (lane >> 4)) * 3072 + cb * 256 + (lane & 15) * 16;
    u32x4 v[8];
#pragma unroll
    for (int i = 0; i < 8; ++i) v[i] = *(const u32x4*)(src + (size_t)i * 4 * 3072);
    cbar(); cbar();
    float s[8] = {};
#pragma unroll
    for (int i = 0; i < 8; ++i)
#pragma unroll
        for (int e = 0; e < 4; ++e) { s[2 * e] += bflo(v[i][e]); s[2 * e + 1] += bfhi(v[i][e]); }
#pragma unroll
    for (int e = 0; e < 8; ++e) s[e] = sum_halves(sum_x16(s[e]));
    LAS float* part = (LAS float*)(smem + P1_MISC + 2048);
    if (lane < 16) {
        f32x4 a = {s[0], s[1], s[2], s[3]}, c = {s[4], s[5], s[6], s[7]};
        *(LAS f32x4*)(part + j * 128 + lane * 8) = a; *(LAS f32x4*)(part + j * 128 + lane * 8 + 4) = c;
    }
    cbar();
    if (j == 0) {
#pragma unroll
        for (int k = 0; k < 2; ++k) {
            const int c = lane + 64 * k;
            gst_granule(ws + WS_G2, ((cb * 128 + c) * 4 + (tb & 3)) * 8, part[c] + part[128 + c] + part[256 + c] + part[384 + c], tag);
        }
    }
    cbar(); cbar(); cbar(); cbar(); cbar();
    cbar();
    f32x16 acc[2];
    {
        const LAS f32x4* xr = (const LAS f32x4*)(smem + P1_XCH + j * 8192 + lane * 16);
#pragma unroll
        for (int b = 0; b < 2; ++b)
#pragma unroll
            for (int g = 0; g < 4; ++g) { const f32x4 t = xr[(b * 4 + g) * 64]; acc[b][4 * g] = t[0]; acc[b][4 * g + 1] = t[1]; acc[b][4 * g + 2] = t[2]; acc[b][4 * g + 3] = t[3]; }
    }
    accA = acc[0]; accB = acc[1];
}

template <bool ISV>
DI void p1_compute(char* ws, int tb, int cb, char* smem, int w, int lane, f32x16& accA, f32x16& accB) {
    const int r = lane & 31, hh = lane >> 5;
    const unsigned lds0 = (unsigned)(uintptr_t)smem;
    const int tp = w & 1, cp = w >> 1;
    unsigned x0[4];
    {
        const int f = (r >> 1) & 7;
#pragma unroll
        for (int ks = 0; ks < 4; ++ks) x0[ks] = lds0 + (64 * tp + r) * 128 + (((2 * ks + hh) ^ f) << 4);
    }
    const unsigned wro = lds0 + 16384 + hh * 2048 + ((lane & 15) >> 2) * 64 + ((lane >> 4) & 1) * 32 + (lane & 3) * 8 + cp * 512;
    f32x16 acc[2][2] = {};
    bf16x8 X[4][2], W[4][2];
#define P1_LOAD(S, B, ks) { X[S][0] = lds_b128(x0[ks] + (B)); X[S][1] = lds_b128(x0[ks] + (B) + 4096); W[S][0] = lds_trfrag(wro + (B) + (ks) * 4096, 1024); W[S][1] = lds_trfrag(wro + (B) + (ks) * 4096 + 256, 1024); }
#define P1_MMA(S)                                                                                                                 \
    {                                                                                                                             \
        _Pragma("unroll") for (int a = 0; a < 2; ++a) _Pragma("unroll") for (int b = 0; b < 2; ++b)                                \
            acc[a][b] = ISV ? MFMA(X[S][a], W[S][b], acc[a][b]) : MFMA(W[S][b], X[S][a], acc[a][b]);                               \
    }
#define P1_ILV() { __builtin_amdgcn_sched_group_barrier(0x8, 1, 0); __builtin_amdgcn_sched_group_barrier(0x100, 2, 0); __builtin_amdgcn_sched_group_barrier(0x8, 1, 0); __builtin_amdgcn_sched_group_barrier(0x100, 2, 0); \
                   __builtin_amdgcn_sched_group_barrier(0x8, 1, 0); __builtin_amdgcn_sched_group_barrier(0x100, 2, 0); __builtin_amdgcn_sched_group_barrier(0x8, 1, 0); SB(); }
    cbar();
    ST(11);
    P1_LOAD(0, 0, 0); P1_LOAD(1, 0, 1);
    SB();
    unsigned B = 0;
#pragma unroll 1
    for (int ch = 0; ch < 7; ++ch) {
        P1_LOAD(2, B, 2); P1_MMA(0); P1_ILV();
        P1_LOAD(3, B, 3); P1_MMA(1); P1_ILV();
        cbar();
        B = B == (P1NB - 1) * P1_BUF ? 0 : B + P1_BUF;
        P1_LOAD(0, B, 0); P1_MMA(2); P1_ILV();
        P1_LOAD(1, B, 1); P1_MMA(3); P1_ILV();
    }
    P1_LOAD(2, B, 2); P1_MMA(0); P1_ILV();
    P1_LOAD(3, B, 3); P1_MMA(1); P1_ILV();
    P1_MMA(2); P1_MMA(3);
    ST(12);
    {
        LAS f32x4* xw = (LAS f32x4*)(smem + P1_XCH + w * 8192 + lane * 16);
#pragma unroll
        for (int b = 0; b < 2; ++b)
#pragma unroll
            for (int g = 0; g < 4; ++g) { f32x4 t = {acc[1][b][4 * g], acc[1][b][4 * g + 1], acc[1][b][4 * g + 2], acc[1][b][4 * g + 3]}; xw[(b * 4 + g) * 64] = t; }
    }
    cbar();
    accA = acc[0][0]; accB = acc[0][1];
}

typedef const __attribute__((address_space(1))) char* gcptr;
template <int NROW16>
DI void colsum32(gcptr W, unsigned rs, int lane, float (&s)[8]) {
    const unsigned vo = (lane & 15) * rs + (lane >> 4) * 16;
    u32x4 v[NROW16];
#pragma unroll
    for (int i = 0; i < NROW16; ++i) v[i] = *(const __attribute__((address_space(1))) u32x4*)(W + (size_t)i * 16 * rs + vo);
    __builtin_amdgcn_sched_barrier(0);
#pragma unroll
    for (int e = 0; e < 8; ++e) s[e] = 0.f;
#pragma unroll
    for (int i = 0; i < NROW16; ++i)
#pragma unroll
        for (int e = 0; e < 4; ++e) { s[2 * e] += bflo(v[i][e]); s[2 * e + 1] += bfhi(v[i][e]); }
#pragma unroll
    for (int e = 0; e < 8; ++e) s[e] = dpp_add<0x118>(dpp_add<0x114>(dpp_add<0x112>(dpp_add<0x111>(s[e]))));
}
DI void p1_helper(const char* px, const char* pqkv, const char* pg, const char* pu, char* ws, int h, int w, int lane) {
    if (w < 4) {
        const unsigned tag = __hip_atomic_load((const __attribute__((address_space(1))) unsigned*)(ws + WS_EPOCH), __ATOMIC_RELAXED, __HIP_MEMORY_SCOPE_AGENT) + 1;
        const int u = w * 64 + h;
        if (u < 256) {
            const int rl = lane >> 3, seg = lane & 7;
            const char* src = px + (size_t)(u * 8 + rl) * 1024 + seg * 16;
            u32x4 v[8];
#pragma unroll
            for (int i = 0; i < 8; ++i) v[i] = *(const u32x4*)(src + i * 128);
            float s1 = 0.f, s2 = 0.f;
#pragma unroll
            for (int i = 0; i < 8; ++i)
#pragma unroll
                for (int e = 0; e < 4; ++e) { s1 = dot2(v[i][e], ONES, s1); s2 = dot2(v[i][e], v[i][e], s2); }
            s1 = dpp_add<0x114>(dpp_add<0x112>(dpp_add<0x111>(s1))); s2 = dpp_add<0x114>(dpp_add<0x112>(dpp_add<0x111>(s2)));
            const float mean = s1 * (1.f / DM);
            const float rstd = __builtin_amdgcn_rsqf(fmaxf(s2 * (1.f / DM) - mean * mean, 0.f) + LN_EPS);
            if (seg == 7) { gst_granule(ws + WS_G1, (u * 8 + rl) * 16, mean, tag); gst_granule(ws + WS_G1, (u * 8 + rl) * 16 + 8, rstd, tag); }
        }
    }
    const int job = w == 4 ? h : (w == 5 && h < 22) ? 64 + h : -1;
    if (job >= 0) {
        const int blk = job < 43 ? job : job - 43;
        const uintptr_t wa = (uintptr_t)pg + (job < 43 ? 0 : (uintptr_t)pu - (uintptr_t)pg);
        float s[8];
        colsum32<32>((gcptr)wa + blk * 64, FF * 2, lane, s);
        if ((lane & 15) == 15) {
            char* dst = ws + WS_CS + (job < 43 ? 0 : FF) * 4;
            const int c0 = blk * 32 + (lane >> 4) * 8;
            u32x4 a = {__float_as_uint(s[0]), __float_as_uint(s[1]), __float_as_uint(s[2]), __float_as_uint(s[3])};
            u32x4 c = {__float_as_uint(s[4]), __float_as_uint(s[5]), __float_as_uint(s[6]), __float_as_uint(s[7])};
            gst128(dst, c0 * 4, a); gst128(dst, c0 * 4 + 16, c);
        }
    }
}

DI unsigned p1_load(const Ptrs& P, int L, int tb, int cb, int rot, int sidx, unsigned lds0, int lane) {
    unsigned vo[4];
    const char* base;
    unsigned cstep, gstep;
    if (L == 0) {
        const int rl = lane >> 3, cs = lane & 7;
#pragma unroll
        for (int i = 0; i < 4; ++i) vo[i] = (8 * i + rl) * 1024 + ((cs ^ ((4 * (i & 1) + (rl >> 1)) & 7)) << 4) - i * 1024;
        base = P.x + (size_t)tb * 128 * 1024; cstep = 128; gstep = 4 * 8192;
    } else {
        const int nb = lane >> 4, q = (lane >> 2) & 3, c = lane & 3;
#pragma unroll
        for (int i = 0; i < 4; ++i) vo[i] = (4 * i + q) * 3072 + nb * 64 + c * 16 - i * 1024;
        base = P.wqkv + cb * 256; cstep = 64 * 3072; gstep = 16 * 3072;
    }
    auto issue = [&](int c, unsigned B) {
        const char* src = base + (size_t)((c + rot) & 7) * cstep;
        const unsigned dst = lds0 + B + L * 16384;
#pragma unroll
        for (int g = 0; g < 4; ++g) dma4(src + (size_t)g * gstep, vo[0], vo[1], vo[2], vo[3], dst + g * 4096);
    };
    issue(0, 0);
#pragma unroll
    for (int c = 1; c < P1NB - 1; ++c) issue(c, c * P1_BUF);
    unsigned B = (P1NB - 1) * P1_BUF;
    constexpr int LA = P1NB - 1;
#pragma unroll 1
    for (int c = 0; c < 8; ++c) {
        const int y = 7 - c < LA - 1 ? 7 - c : LA - 1;
        if (c == 0) lbar<16 * (LA - 1) + T1W - 16>(); else if (y == 3) lbar<48>(); else if (y == 2) lbar<32>(); else if (y == 1) lbar<16>(); else lbar<0>();
        if (c + LA < 8) { issue(c + LA, B); B = B == (P1NB - 1) * P1_BUF ? 0 : B + P1_BUF; }
    }
    const unsigned tag = rfl(gld32_wait(P.ws + WS_EPOCH, 0)) + 1;
    if (L == 1) {
        u32x4 a, b, c0, c1, c2, c3;
        for (unsigned spins = 0; spins < SPIN_CAP; ++spins) {
            asm volatile("global_load_dwordx4 %0, %6, %7 sc1\n\tglobal_load_dwordx4 %1, %6, %7 offset:16 sc1\n\t"
                         "global_load_dwordx4 %2, %8, %9 sc1\n\tglobal_load_dwordx4 %3, %8, %9 offset:16 sc1\n\t"
                         "global_load_dwordx4 %4, %8, %9 offset:32 sc1\n\tglobal_load_dwordx4 %5, %8, %9 offset:48 sc1\n\ts_waitcnt vmcnt(0)"
                         : "=&v"(a), "=&v"(b), "=&v"(c0), "=&v"(c1), "=&v"(c2), "=&v"(c3)
                         : "v"(lane * 32), "s"(P.ws + WS_G1 + tb * 128 * 16), "v"(lane * 64), "s"(P.ws + WS_G2 + cb * 128 * 32) : "memory");
            if (__all(a[1] == tag && a[3] == tag && b[1] == tag && b[3] == tag && c0[1] == tag && c0[3] == tag && c1[1] == tag && c1[3] == tag &&
                      c2[1] == tag && c2[3] == tag && c3[1] == tag && c3[3] == tag)) break;
            __builtin_amdgcn_s_sleep(1);
        }
        { char* ws = P.ws; ST(13); }
        u32x4 ms = {a[0], a[2], b[0], b[2]};
        u32x2 cs = {__float_as_uint(__uint_as_float(c0[0]) + __uint_as_float(c0[2]) + __uint_as_float(c1[0]) + __uint_as_float(c1[2])),
                    __float_as_uint(__uint_as_float(c2[0]) + __uint_as_float(c2[2]) + __uint_as_float(c3[0]) + __uint_as_float(c3[2]))};
        *(LAS u32x4*)(uintptr_t)(lds0 + P1_MISC + lane * 16) = ms;
        *(LAS u32x2*)(uintptr_t)(lds0 + P1_MISC + 1024 + lane * 8) = cs;
        asm volatile("s_waitcnt lgkmcnt(0)" ::: "memory");
    }
    bar();
    return tag;
}

constexpr int P2_X = 131072 + 2048;
constexpr int P2_KM = P2_X + 8 * 2560;
constexpr float SAFE_RANGE = 80.f;

DI void p2_compute(char* ws, char* smem, int head, int qb, int w, int lane) {
    const int r = lane & 31, hh = lane >> 5;
    const unsigned lds0 = (unsigned)(uintptr_t)smem;
    const int qg = w & 3, kh = w >> 2;
    const int la = qg * 2 + kh, lb = qg * 2 + (1 - kh);
    cbar();
    ST(13);
    const u32x4 qa = *(LAS const u32x4*)(uintptr_t)(lds0 + P2_X + la * 1024 + lane * 16), qbv = *(LAS const u32x4*)(uintptr_t)(lds0 + P2_X + lb * 1024 + lane * 16);
    const float kmax2 = __int_as_float(__builtin_amdgcn_readlane(__float_as_int(dpp_max<true>(*(LAS const float*)(uintptr_t)(lds0 + P2_KM + lane * 4))), 63));
    float qa2 = 0.f, qb2 = 0.f;
#pragma unroll
    for (int e = 0; e < 4; ++e) { qa2 = dot2(qa[e], qa[e], qa2); qb2 = dot2(qbv[e], qbv[e], qb2); }
    const float bnda = __builtin_amdgcn_sqrtf(sum_halves(qa2) * kmax2) * 1.01f, bndb = __builtin_amdgcn_sqrtf(sum_halves(qb2) * kmax2) * 1.01f;
    const bool fast = __all(2.f * bnda <= SAFE_RANGE && 2.f * bndb <= SAFE_RANGE);
    const unsigned ka = lds0 + kh * 32768 + lane * 16, va = lds0 + 65536 + kh * 32768 + hh * 256 + (r & 15) * 16;
    const unsigned xw = lds0 + P2_X + w * 2560 + lane * 16, xr = lds0 + P2_X + (w ^ 4) * 2560 + lane * 16;
    float o[9], ma;
    ST(5);
    if (fast) {
        ma = bnda;
        unsigned kp = ka, vp = va, cnt;
#define EXP(r) "v_exp_f32 v" #r ", v" #r "\n\t"
#define CVT(d, a, b) "v_cvt_pk_bf16_f32 v" #d ", v" #a ", v" #b "\n\t"
#define MSQ(SN, KN, Q, C) "v_mfma_f32_32x32x16_bf16 " SN ", " KN ", " Q ", " C "\n\t"
#define MPO(O, V, P) "v_mfma_f32_32x32x16_bf16 " O ", " V ", " P ", " O "\n\t"
#define RDV(VN0, VN1, VOFF) "s_mov_b64 exec, %[msk]\n\t" "ds_read_b128 " VN0 ", %[vp] offset:" #VOFF "\n\t" "ds_read_b128 " VN1 ", %[vp] offset:" #VOFF "+512\n\t" "s_mov_b64 exec, -1\n\t"
#define RDK(KNN, KOFF) "ds_read_b128 " KNN ", %[kp] offset:" #KOFF "\n\t"
#define STEP(PRE, c0, c1, c2, c3, c4, c5, c6, c7, c8, c9, c10, c11, c12, c13, c14, c15, p14, p15, SN, KN, Q, CC, O, OP, VC0, VP1)  \
        EXP(c0) EXP(c1) CVT(127, p14, p15) EXP(c2) EXP(c3) MPO(OP, VP1, "v[124:127]") PRE                                    \
        CVT(120, c0, c1) EXP(c4) EXP(c5) CVT(121, c2, c3) EXP(c6)                                                            \
        "s_waitcnt lgkmcnt(3)\n\t" MSQ(SN, KN, Q, CC)                                                                        \
        EXP(c7) CVT(122, c4, c5) EXP(c8) EXP(c9) CVT(123, c6, c7) EXP(c10) EXP(c11)                                          \
        MPO(O, VC0, "v[120:123]")                                                                                            \
        CVT(124, c8, c9) EXP(c12) EXP(c13) CVT(125, c10, c11) EXP(c14) EXP(c15) CVT(126, c12, c13)
#define SA_ 32, 33, 34, 35, 36, 37, 38, 39, 40, 41, 42, 43, 44, 45, 46, 47
#define SB_ 48, 49, 50, 51, 52, 53, 54, 55, 56, 57, 58, 59, 60, 61, 62, 63
#define CALL(M, ...) M(__VA_ARGS__)
#define TE(KOFF, VOFF) CALL(STEP, RDV("v[112:115]", "v[116:119]", VOFF), SA_, 62, 63, "v[48:63]", "v[96:99]", "%[qb]", "v[144:159]", "v[64:79]", "v[80:95]", "v[104:107]", "v[116:119]") \
                       CALL(STEP, RDK("v[96:99]", KOFF), SB_, 46, 47, "v[32:47]", "v[100:103]", "%[qa]", "v[128:143]", "v[80:95]", "v[64:79]", "v[104:107]", "v[108:111]")
#define TO(KOFF, VOFF) CALL(STEP, RDV("v[104:107]", "v[108:111]", VOFF), SA_, 62, 63, "v[48:63]", "v[100:103]", "%[qb]", "v[144:159]", "v[64:79]", "v[80:95]", "v[112:115]", "v[108:111]") \
                       CALL(STEP, RDK("v[100:103]", KOFF), SB_, 46, 47, "v[32:47]", "v[96:99]", "%[qa]", "v[128:143]", "v[80:95]", "v[64:79]", "v[112:115]", "v[116:119]")
#define M64(a, b, c, d) "v_mov_b64 v[" #a ":" #b "], v[" #c ":" #d "]\n\t"
#define MOV4(a, b, c, d, src) "v_mov_b32 v" #a ", " src "\n\tv_mov_b32 v" #b ", " src "\n\tv_mov_b32 v" #c ", " src "\n\tv_mov_b32 v" #d ", " src "\n\t"
        asm volatile(
            "s_setprio 3\n\t"
            "ds_read_b128 v[96:99], %[kp]\n\t"
            "v_mov_b32 v128, %[na]\n\tv_mov_b32 v129, %[na]\n\tv_mov_b32 v144, %[nb]\n\tv_mov_b32 v145, %[nb]\n\t"
            "v_mov_b32 v104, %[one]\n\tv_mov_b32 v105, %[one]\n\tv_mov_b64 v[64:65], 0\n\t"
            M64(106, 107, 104, 105) M64(108, 109, 104, 105) M64(110, 111, 104, 105) M64(112, 113, 104, 105) M64(114, 115, 104, 105) M64(116, 117, 104, 105) M64(118, 119, 104, 105)
            "s_mov_b64 exec, %[msk]\n\t"
            "ds_read_b128 v[104:107], %[vp]\n\t"
            "ds_read_b128 v[108:111], %[vp] offset:512\n\t"
            "s_mov_b64 exec, -1\n\t"
            "ds_read_b128 v[100:103], %[kp] offset:1024\n\t"
            M64(130, 131, 128, 129) M64(132, 133, 128, 129) M64(134, 135, 128, 129) M64(136, 137, 128, 129) M64(138, 139, 128, 129) M64(140, 141, 128, 129) M64(142, 143, 128, 129)
            "s_waitcnt lgkmcnt(3)\n\t"
            "s_nop 1\n\t"
            "v_mfma_f32_32x32x16_bf16 v[32:47], v[96:99], %[qa], v[128:143]\n\t"
            M64(146, 147, 144, 145) M64(148, 149, 144, 145) M64(150, 151, 144, 145) M64(152, 153, 144, 145) M64(154, 155, 144, 145) M64(156, 157, 144, 145) M64(158, 159, 144, 145)
            M64(66, 67, 64, 65) M64(68, 69, 64, 65) M64(70, 71, 64, 65) M64(72, 73, 64, 65) M64(74, 75, 64, 65) M64(76, 77, 64, 65) M64(78, 79, 64, 65)
            M64(80, 81, 64, 65) M64(82, 83, 64, 65) M64(84, 85, 64, 65) M64(86, 87, 64, 65) M64(88, 89, 64, 65) M64(90, 91, 64, 65) M64(92, 93, 64, 65) M64(94, 95, 64, 65)
            M64(62, 63, 64, 65) M64(124, 125, 64, 65) M64(126, 127, 64, 65)
            "s_mov_b32 %[cnt], 0\n\t"
            "L_att_%=:\n\t"
            TE(2048, 1024) TO(3072, 2048) TE(4096, 3072) TO(5120, 4096)
            "v_add_u32 %[kp], 0x1000, %[kp]\n\t"
            "v_add_u32 %[vp], 0x1000, %[vp]\n\t"
            "s_cmp_lg_u32 %[cnt], 0\n\t"
            "s_cbranch_scc1 L_att_nb_%=\n\t"
            "s_barrier\n\t"
            "L_att_nb_%=:\n\t"
            "s_add_u32 %[cnt], %[cnt], 1\n\t"
            "s_cmp_eq_u32 %[cnt], 3\n\ts_cbranch_scc0 L_p1_%=\n\ts_setprio 2\n\tL_p1_%=:\n\t"
            "s_cmp_eq_u32 %[cnt], 6\n\ts_cbranch_scc0 L_p2_%=\n\ts_setprio 1\n\tL_p2_%=:\n\t"
            "s_cmp_eq_u32 %[cnt], 7\n\ts_cbranch_scc0 L_p3_%=\n\ts_setprio 0\n\tL_p3_%=:\n\t"
            "s_cmp_lt_u32 %[cnt], 8\n\t"
            "s_cbranch_scc1 L_att_%=\n\t"
            CVT(127, 62, 63)
            "s_nop 1\n\t"
            MPO("v[80:95]", "v[116:119]", "v[124:127]")
            "s_waitcnt lgkmcnt(0)\n\t"
            "s_nop 15\n\t"
            "v_mov_b32 v89, %[bb]\n\t"
            "v_mov_b32 %[o0], v64\n\tv_mov_b32 %[o1], v65\n\tv_mov_b32 %[o2], v66\n\tv_mov_b32 %[o3], v67\n\t"
            "v_mov_b32 %[o4], v68\n\tv_mov_b32 %[o5], v69\n\tv_mov_b32 %[o6], v70\n\tv_mov_b32 %[o7], v71\n\tv_mov_b32 %[o8], v72\n\t"
            "ds_write_b128 %[xw], v[80:83]\n\t"
            "ds_write_b128 %[xw], v[84:87] offset:1024\n\t"
            "ds_write_b64 %[xw2], v[88:89]\n\t"
            : [o0] "=&v"(o[0]), [o1] "=&v"(o[1]), [o2] "=&v"(o[2]), [o3] "=&v"(o[3]), [o4] "=&v"(o[4]), [o5] "=&v"(o[5]), [o6] "=&v"(o[6]), [o7] "=&v"(o[7]), [o8] "=&v"(o[8]),
              [kp] "+v"(kp), [vp] "+v"(vp), [cnt] "=&s"(cnt)
            : [qa] "v"(qa), [qb] "v"(qbv), [na] "v"(-bnda), [nb] "v"(-bndb), [bb] "v"(bndb), [msk] "s"(0x0000ffff0000ffffull), [xw] "v"(xw), [xw2] "v"(xw + 2048 - lane * 8), [one] "v"((r == 16 || r == 20) ? ONES : 0u)
            : "memory", "scc",
              "v32", "v33", "v34", "v35", "v36", "v37", "v38", "v39", "v40", "v41", "v42", "v43", "v44", "v45", "v46", "v47",
              "v48", "v49", "v50", "v51", "v52", "v53", "v54", "v55", "v56", "v57", "v58", "v59", "v60", "v61", "v62", "v63",
              "v64", "v65", "v66", "v67", "v68", "v69", "v70", "v71", "v72", "v73", "v74", "v75", "v76", "v77", "v78", "v79",
              "v80", "v81", "v82", "v83", "v84", "v85", "v86", "v87", "v88", "v89", "v90", "v91", "v92", "v93", "v94", "v95",
              "v96", "v97", "v98", "v99", "v100", "v101", "v102", "v103", "v104", "v105", "v106", "v107", "v108", "v109", "v110", "v111",
              "v112", "v113", "v114", "v115", "v116", "v117", "v118", "v119", "v120", "v121", "v122", "v123", "v124", "v125", "v126", "v127",
              "v128", "v129", "v130", "v131", "v132", "v133", "v134", "v135", "v136", "v137", "v138", "v139", "v140", "v141", "v142", "v143",
              "v144", "v145", "v146", "v147", "v148", "v149", "v150", "v151", "v152", "v153", "v154", "v155", "v156", "v157", "v158", "v159");
    } else {
        bar();
        const u32x4 ones = {ONES, ONES, ONES, ONES};
        const f32x16 zero = {};
#pragma unroll 1
        for (int sl = 1; sl >= 0; --sl) {
            const bf16x8 qf = __builtin_bit_cast(bf16x8, sl ? qbv : qa);
            bf16x8 v0 = __builtin_bit_cast(bf16x8, ones), v1 = v0, kf;
            f32x16 oo = {};
            float m = -INFINITY;
#pragma unroll 1
            for (int t = 0; t < 32; ++t) {
                asm volatile("ds_read_b128 %0, %3\n\ts_mov_b64 exec, %5\n\tds_read_b128 %1, %4\n\tds_read_b128 %2, %4 offset:512\n\ts_mov_b64 exec, -1\n\ts_waitcnt lgkmcnt(0)"
                             : "=&v"(kf), "+v"(v0), "+v"(v1) : "v"(ka + t * 1024), "v"(va + t * 1024), "s"(0x0000ffff0000ffffull) : "memory");
                const f32x16 s = MFMA(kf, qf, zero);
                float tm = s[0];
#pragma unroll
                for (int i = 1; i < 16; ++i) tm = fmaxf(tm, s[i]);
                tm = fmaxf(tm, other_half(tm));
                const float mn = fmaxf(m, tm);
                const float alpha = __builtin_amdgcn_exp2f(m - mn);
                m = mn;
#pragma unroll
                for (int i = 0; i < 16; ++i) oo[i] *= alpha;
                u32x4 p0, p1;
#pragma unroll
                for (int i = 0; i < 4; ++i) p0[i] = cvtpk(__builtin_amdgcn_exp2f(s[2 * i] - m), __builtin_amdgcn_exp2f(s[2 * i + 1] - m));
#pragma unroll
                for (int i = 0; i < 4; ++i) p1[i] = cvtpk(__builtin_amdgcn_exp2f(s[8 + 2 * i] - m), __builtin_amdgcn_exp2f(s[9 + 2 * i] - m));
                oo = MFMA(v0, __builtin_bit_cast(bf16x8, p0), oo);
                oo = MFMA(v1, __builtin_bit_cast(bf16x8, p1), oo);
            }
            if (sl == 1) {
                f32x4 t0 = {oo[0], oo[1], oo[2], oo[3]}, t1 = {oo[4], oo[5], oo[6], oo[7]};
                f32x2 t2 = {oo[8], m};
                *(LAS f32x4*)(uintptr_t)xw = t0; *(LAS f32x4*)(uintptr_t)(xw + 1024) = t1; *(LAS f32x2*)(uintptr_t)(xw + 2048 - lane * 8) = t2;
            } else {
#pragma unroll
                for (int i = 0; i < 9; ++i) o[i] = oo[i];
                ma = m;
            }
        }
    }
    ST(14);
    cbar();
    {
        const f32x4 t0 = *(LAS const f32x4*)(uintptr_t)xr, t1 = *(LAS const f32x4*)(uintptr_t)(xr + 1024);
        const f32x2 t2 = *(LAS const f32x2*)(uintptr_t)(xr + 2048 - lane * 8);
        const float mo = t2[1], mn = fmaxf(ma, mo);
        const float fa = __builtin_amdgcn_exp2f(ma - mn), fo = __builtin_amdgcn_exp2f(mo - mn);
        const float inv = __builtin_amdgcn_rcpf(o[8] * fa + t2[0] * fo);
        const float ia = fa * inv, io = fo * inv;
        const u32x4 ov = pack_swap(o[0] * ia + t0[0] * io, o[1] * ia + t0[1] * io, o[2] * ia + t0[2] * io, o[3] * ia + t0[3] * io,
                                   o[4] * ia + t1[0] * io, o[5] * ia + t1[1] * io, o[6] * ia + t1[2] * io, o[7] * ia + t1[3] * io);
        gst128(ws + WS_OF + ((size_t)(qb * 8 + la) * 32 + head) * 1024, lane * 16, ov);
    }
}

constexpr int P3_X = 153600;
template <int MODE>
DI void p35_compute(const Ptrs& P, char* smem, int tb, int cb, int w, int lane, u32x4& hkeep) {
    constexpr int NFULL = MODE == 0 ? 4 : 10;
    constexpr int A0 = MODE == 0 ? 65536 : 98304, NA = MODE == 0 ? 4 : 3, NW = MODE == 0 ? 4 : 6, XA = MODE == 0 ? 131072 : 147456;
    char* ws = P.ws;
    const int r = lane & 31, hh = lane >> 5;
    const unsigned lds0 = (unsigned)(uintptr_t)smem;
    const int ta = w & 1, cj = (w >> 1) & 1, kh = w >> 2;
    const int tile = tb * 2 + ta, token = tile * 32 + r, n0 = cb * 64 + cj * 32 + kh * 16;
    u32x2 res[2];
    const unsigned aro = lds0 + A0 + ta * 8192 + lane * 16 + kh * 4096;
    const unsigned wro = lds0 + hh * 1024 + ((lane & 15) >> 2) * 64 + ((lane >> 4) & 1) * 32 + (lane & 3) * 8 + cj * 256 + kh * 8192;
    f32x16 acc = {};
    bf16x8 A[4], Wf[4];
#define P35_LD(S, BA, BW, ks) { A[S] = lds_b128(aro + (BA) + (ks) * 1024); Wf[S] = lds_trfrag(wro + (BW) + (ks) * 2048, 512); }
    cbar();
    ST(MODE == 0 ? 7 : 8);
    P35_LD(0, 0, 0, 0); P35_LD(1, 0, 0, 1);
    unsigned BA = 0, BW = 0;
#pragma unroll 1
    for (int ch = 0; ch < NFULL; ++ch) {
        P35_LD(2, BA, BW, 2); SB(); acc = MFMA(Wf[0], A[0], acc); SB();
        P35_LD(3, BA, BW, 3); SB(); acc = MFMA(Wf[1], A[1], acc); SB();
        if (MODE == 1 || ch < NFULL - 1) {
            cbar();
            BA = BA == (NA - 1) * 16384 ? 0 : BA + 16384; BW = BW == (NW - 1) * 16384 ? 0 : BW + 16384;
            if (ch < NFULL - 1) { P35_LD(0, BA, BW, 0); }
        }
        SB(); acc = MFMA(Wf[2], A[2], acc); SB();
        if (ch < NFULL - 1) { P35_LD(1, BA, BW, 1); }
        SB(); acc = MFMA(Wf[3], A[3], acc); SB();
    }
    if constexpr (MODE == 1) {
#pragma unroll
        for (int j = 0; j < 3; ++j) {
            const bf16x8 af = lds_b128(aro + BA - kh * 4096 + (kh * 3 + j) * 1024);
            const bf16x8 wf = lds_trfrag(wro + BW - kh * 8192 + (kh * 3 + j) * 2048, 512);
            acc = MFMA(wf, af, acc);
        }
    }
    ST(MODE == 0 ? 9 : 0);
    {
        LAS f32x4* xw = (LAS f32x4*)(smem + XA + w * 2048 + lane * 16);
        f32x4 s0, s1;
#pragma unroll
        for (int e = 0; e < 4; ++e) { s0[e] = kh ? acc[e] : acc[8 + e]; s1[e] = kh ? acc[4 + e] : acc[12 + e]; }
        xw[0] = s0; xw[64] = s1;
    }
    cbar();
    float v[8];
    {
        LAS const f32x4* xr = (LAS const f32x4*)(smem + XA + (w ^ 4) * 2048 + lane * 16);
        const f32x4 s0 = xr[0], s1 = xr[64];
        if constexpr (MODE == 0) {
            const int row = ta * 32 + r;
#pragma unroll
            for (int g = 0; g < 2; ++g)
                res[g] = *(LAS const u32x2*)(smem + P3_X + row * 128 + (((cj * 4 + kh * 2 + g) ^ ((row >> 1) & 7)) << 4) + hh * 8);
        } else { res[0][0] = hkeep[0]; res[0][1] = hkeep[1]; res[1][0] = hkeep[2]; res[1][1] = hkeep[3]; }
#pragma unroll
        for (int e = 0; e < 4; ++e) { v[e] = (kh ? acc[8 + e] : acc[e]) + s0[e]; v[4 + e] = (kh ? acc[12 + e] : acc[4 + e]) + s1[e]; }
#pragma unroll
        for (int g = 0; g < 2; ++g) {
            v[4 * g] += bflo(res[g][0]); v[4 * g + 1] += bfhi(res[g][0]); v[4 * g + 2] += bflo(res[g][1]); v[4 * g + 3] += bfhi(res[g][1]);
        }
    }
    const unsigned p0 = cvtpk(v[0], v[1]), p1 = cvtpk(v[2], v[3]), p2 = cvtpk(v[4], v[5]), p3 = cvtpk(v[6], v[7]);
    const u32x4 o = swap4(p0, p1, p2, p3);
    if constexpr (MODE == 0) {
        hkeep[0] = p0; hkeep[1] = p1; hkeep[2] = p2; hkeep[3] = p3;
        gst128(ws + WS_HF + ((size_t)tile * 32 + (n0 >> 4)) * 1024, lane * 16, o);
        float s1 = 0.f, s2 = 0.f;
#pragma unroll
        for (int e = 0; e < 4; ++e) { s1 = dot2(o[e], ONES, s1); s2 = dot2(o[e], o[e], s2); }
        s1 = sum_halves(s1); s2 = sum_halves(s2);
        LAS f32x2* sl = (LAS f32x2*)(smem + XA + 8 * 2048);
        if (lane < 32) { f32x2 sv = {s1, s2}; sl[(ta * 32 + r) * 4 + cj * 2 + kh] = sv; }
        cbar();
        if (w == 0) {
            const f32x4 p = *(LAS const f32x4*)(sl + lane * 4), q = *(LAS const f32x4*)(sl + lane * 4 + 2);
            u32x2 sv = {__float_as_uint(p[0] + p[2] + q[0] + q[2]), __float_as_uint(p[1] + p[3] + q[1] + q[3])};
            gst64(ws + WS_ST + (size_t)tb * 64 * 64 + cb * 8, lane * 64, sv);
        }
    } else {
        *(u32x4*)(P.out + (size_t)token * 1024 + (n0 + 8 * hh) * 2) = o;
    }
}

constexpr int P4_ST = 147456, P4_CS = P4_ST + 8192;
template <int NB>
DI void p4_compute(char* ws, char* smem, int tb, int cg, int w, int lane) {
    const int r = lane & 31, hh = lane >> 5, i = w & 3, j0 = NB == 2 ? 0 : 2;
    const unsigned lds0 = (unsigned)(uintptr_t)smem;
    const int tile = tb * 4 + i;
    const unsigned hro = lds0 + 98304 + i * 4096 + lane * 16;
    const unsigned wro = lds0 + hh * 1536 + ((lane & 15) >> 2) * 64 + ((lane >> 4) & 1) * 32 + (lane & 3) * 8 + j0 * 256;
    f32x16 ag[NB], au[NB];
#pragma unroll
    for (int j = 0; j < NB; ++j) { ag[j] = f32x16{}; au[j] = f32x16{}; }
    bf16x8 H[4], G[4][NB], U[4][NB];
#define P4_LOAD(S, BH, BW, ks)                                                                                                    \
    {                                                                                                                             \
        H[S] = lds_b128(hro + (BH) + (ks) * 1024);                                                                                 \
        _Pragma("unroll") for (int j = 0; j < NB; ++j) { G[S][j] = lds_trfrag(wro + (BW) + (ks) * 3072 + j * 256, 768); U[S][j] = lds_trfrag(wro + (BW) + 12288 + (ks) * 3072 + j * 256, 768); } \
    }
#define P4_MMA(S) { _Pragma("unroll") for (int j = 0; j < NB; ++j) { ag[j] = MFMA(G[S][j], H[S], ag[j]); au[j] = MFMA(U[S][j], H[S], au[j]); } }
    cbar();
    ST(6);
    P4_LOAD(0, 0, 0, 0); P4_LOAD(1, 0, 0, 1);
    unsigned BH = 0, BW = 0;
#pragma unroll 1
    for (int ch = 0; ch < 8; ++ch) {
        P4_LOAD(2, BH, BW, 2); SB(); P4_MMA(0); SB();
        P4_LOAD(3, BH, BW, 3); SB(); P4_MMA(1); SB();
        cbar();
        if (ch < 7) {
            BH = BH == 2 * 16384 ? 0 : BH + 16384; BW = BW == 3 * 24576 ? 0 : BW + 24576;
            P4_LOAD(0, BH, BW, 0);
        }
        SB(); P4_MMA(2); SB();
        if (ch < 7) { P4_LOAD(1, BH, BW, 1); }
        SB(); P4_MMA(3); SB();
    }
    ST(15);
    float mean, rstd;
    {
        float s1 = 0.f, s2 = 0.f;
        const LAS f32x4* sp = (const LAS f32x4*)(smem + P4_ST + (i * 32 + r) * 64);
#pragma unroll
        for (int k = 0; k < 4; ++k) { const f32x4 v = sp[k]; s1 += v[0] + v[2]; s2 += v[1] + v[3]; }
        mean = s1 * (1.f / DM);
        rstd = __builtin_amdgcn_rsqf(fmaxf(s2 * (1.f / DM) - mean * mean, 0.f) + LN_EPS);
    }
    constexpr float C1 = -2.f * 0.7978845608028654f * LOG2E, C2 = C1 * 0.044715f;
    const float k1 = C1 * rstd, k2 = C2 * rstd * rstd * rstd, k3 = __builtin_amdgcn_rcpf(rstd * rstd);
    const f32x2 nm2 = {-mean, -mean}, k12 = {k1, k1}, k22 = {k2, k2}, k32 = {k3, k3};
#pragma unroll
    for (int j = 0; j < NB; ++j) {
        const int blk = cg * 3 + j0 + j;
        float v[16];
#pragma unroll
        for (int g = 0; g < 4; ++g) {
            const LAS f32x4* cp = (const LAS f32x4*)(smem + P4_CS + ((j0 + j) * 32 + 8 * g + 4 * hh) * 4);
            const f32x4 cgf = cp[0], cuf = cp[32];
#pragma unroll
            for (int e = 0; e < 4; e += 2) {
                const f32x2 ga = {ag[j][4 * g + e], ag[j][4 * g + e + 1]}, ua = {au[j][4 * g + e], au[j][4 * g + e + 1]};
                const f32x2 cg2 = {cgf[e], cgf[e + 1]}, cu2 = {cuf[e], cuf[e + 1]};
                const f32x2 a = __builtin_elementwise_fma(nm2, cg2, ga), bq = __builtin_elementwise_fma(nm2, cu2, ua);
                const f32x2 arg = a * __builtin_elementwise_fma(a * a, k22, k12);
                const f32x2 ex = {__builtin_amdgcn_exp2f(arg[0]), __builtin_amdgcn_exp2f(arg[1])};
                const f32x2 den = __builtin_elementwise_fma(ex, k32, k32);
                const f32x2 rc = {__builtin_amdgcn_rcpf(den[0]), __builtin_amdgcn_rcpf(den[1])};
                const f32x2 y = (a * rc) * bq;
                v[4 * g + e] = y[0]; v[4 * g + e + 1] = y[1];
            }
        }
        if (blk < 43) {
#pragma unroll
            for (int g = 0; g < 2; ++g) {
                const u32x4 o = pack_swap(v[8 * g], v[8 * g + 1], v[8 * g + 2], v[8 * g + 3], v[8 * g + 4], v[8 * g + 5], v[8 * g + 6], v[8 * g + 7]);
                gst128(ws + WS_AF + ((size_t)tile * 86 + blk * 2 + g) * 1024, lane * 16, o);
            }
        }
    }
}

__global__ __launch_bounds__(640, 3) void k_block(const char* __restrict__ x, const char* __restrict__ wqkv, const char* __restrict__ wo, const char* __restrict__ wg,
                                                  const char* __restrict__ wu, const char* __restrict__ wd, char* __restrict__ ws, char* __restrict__ out) {
    extern __shared__ __attribute__((aligned(16))) char smem[];
    const Ptrs P = {x, wqkv, wo, wg, wu, wd, ws, out};
    const int b = blockIdx.x, xcd = b & 7, idx = b >> 3;
    const int lane = threadIdx.x & 63, w = rfl(threadIdx.x >> 6);
    const unsigned lds0 = (unsigned)(uintptr_t)smem;
    if (w >= 8 && blockIdx.x < 64) {
        const char* pc = (const char*)(__builtin_amdgcn_s_getpc() & ~127ull);
        asm volatile("s_mov_b32 m0, %0\n\ts_nop 0" ::"s"(lds0 + LDS_BYTES - 256) : "memory");
#pragma unroll
        for (int i = 0; i < (CODE_LINES + 63) / 64; ++i) { const int ln = i * 64 + lane; gtouch(pc, (ln < CODE_LINES ? ln : CODE_LINES - 1) * 128); }
    }
    const bool p1_gemm = idx >= 8;
    const int gi = idx - 8;
    const int tb1 = (xcd >> 1) * 4 + (gi & 3), cb1 = (xcd & 1) * 6 + (gi >> 2);
    const int head = xcd * 4 + (idx >> 3), qb = idx & 7;
    const int tb3 = xcd * 4 + (idx & 3), cb3 = idx >> 2;
    const bool p4_on = !(xcd & 1) || idx < 28;
    const int tb4 = (xcd >> 1) * 4 + (idx & 3), cg4 = (xcd & 1) * 8 + (idx >> 2);
    ST(0);

    if (w < 8) {
        if (p1_gemm) {
            f32x16 accA, accB;
            if (w >= 4) p1_colsum(wqkv, ws, tb1, cb1, smem, w - 4, lane, accA, accB);
            else if (cb1 < 8) p1_compute<false>(ws, tb1, cb1, smem, w, lane, accA, accB); else p1_compute<true>(ws, tb1, cb1, smem, w, lane, accA, accB);
            const int tl = 2 * (w & 1) + (w >> 2), cp = (w >> 1) & 1;
            if (cb1 < 8) p1_epilogue<false>(ws, tb1, cb1, smem, tl, cp, accA, accB, lane); else p1_epilogue<true>(ws, tb1, cb1, smem, tl, cp, accA, accB, lane);
        } else p1_helper(x, wqkv, wg, wu, ws, idx * 8 + xcd, w, lane);
        ST(1);
        pbar();
        p2_compute(ws, smem, head, qb, w, lane);
        ST(2);
        pbar();
        u32x4 hkeep;
        p35_compute<0>(P, smem, tb3, cb3, w, lane, hkeep);
        ST(3);
        pbar();
        if (p4_on) {
            if (w < 4) p4_compute<2>(ws, smem, tb4, cg4, w, lane); else p4_compute<1>(ws, smem, tb4, cg4, w, lane);
        } else bar();
        ST(4);
        pbar();
        p35_compute<1>(P, smem, tb3, cb3, w, lane, hkeep);
        ST(10);
        return;
    }

    const int L = w - 8;
    if (L == 0) {
        unsigned o_ = (unsigned)WS_FLAG + (lane & 3) * 256 * 128 + b * 128;
        if (lane == 4) o_ = (unsigned)WS_G1 + b * 128;
        if (lane >= 5 && lane < 9) o_ = (unsigned)WS_ST + (b * 4 + lane - 5) * 128;
        if (lane == 9) o_ = (unsigned)WS_KM + (b & 63) * 128;
        if (lane == 10) o_ = (unsigned)WS_CS + (b < 86 ? b : 0) * 128;
        if (lane >= 11 && lane < 17) o_ = (unsigned)WS_G2 + (b % 64) * 768 + (lane - 11) * 128;
        if (lane >= 17 && lane < 57) o_ = (idx * 40 + lane - 17) * 16384 + 4096;
        asm volatile("s_mov_b32 m0, %0\n\ts_nop 0\n\tglobal_load_lds_dword %1, %2 sc1" ::"s"(lds0 + LDS_BYTES - 256), "v"(o_), "s"(ws) : "memory");
        asm volatile("global_load_lds_dword %0, %1 sc1" ::"v"(idx * 65536 + (lane & 3) * 16384), "s"(out) : "memory");
    }
    const char* flags = ws + WS_FLAG;
    const unsigned lin = lane * 16;
    const unsigned tag = p1_gemm ? p1_load(P, L, tb1, cb1, P1ROT, gi, lds0, lane) : rfl(gld32_wait(ws + WS_EPOCH, 0)) + 1;
    bar();
    if (L == 0 && lane == 0) gst32(ws + WS_FLAG, b * 128, tag);
    ST(1);
    {
        const int ptb = lane < 32 ? (lane & 15) : 2 * qb + (lane & 1), pcb = (lane < 16 ? 4 : lane < 32 ? 8 : 0) + (head >> 3);
        const int pb = ((pcb % 6) * 4 + (ptb & 3) + 8) * 8 + (ptb >> 2) * 2 + pcb / 6;
        poll(flags, pb * 128, lane < 34, tag);
        ST(6);
        const int rot = qb * 4;
        const char* src = ws + (L == 0 ? WS_KF : WS_VF) + (size_t)head * 65536;
        const unsigned dst = lds0 + L * 65536;
        {
            const char* q = ws + WS_QF + ((size_t)head * 64 + qb * 8) * 1024 + L * 4096;
            dma4lin(q, lin, lds0 + P2_X + L * 4096);
            if (L == 0) dma1dw(ws + WS_KM + head * 256, lane * 4, lds0 + P2_KM);
        }
#pragma unroll 1
        for (int g = 0; g < 8; ++g) {
            const unsigned so = ((g * 4 + rot) & 31) * 1024;
            dma4lin(src + so, lin, dst + g * 4096); dma4lin(src + 32768 + so, lin, dst + 32768 + g * 4096);
            if (g == 1) lbar<0>();
        }
        ST(7);
        if (L == 1) {
            asm volatile("s_waitcnt vmcnt(0)" ::: "memory");
        }
        lbar<0>();
        if (L == 1) {
#pragma unroll 1
            for (int g = 0; g < 4; ++g) poll(flags, (g * 64 + lane) * 128, true, tag);
            if (b == 0 && lane == 0) gst32(ws + WS_EPOCH, 0, tag);
            asm volatile("s_mov_b32 m0, %0\n\ts_nop 0" ::"s"(lds0 + LDS_BYTES - 256) : "memory");
            gtouch(wo, (idx * 128 + lane) * 128); gtouch(wo, (idx * 128 + 64 + lane) * 128);
            {
                const int nl = (xcd & 1) ? 10 : 12, ln = lane & 15;
                const unsigned co = (xcd & 1) * 1536 + (ln < nl ? ln : 0) * 128;
#pragma unroll
                for (int i = 0; i < 4; ++i) {
                    unsigned o_ = (idx * 16 + i * 4 + (lane >> 4)) * (FF * 2) + co;
                    o_ = o_ > 512u * FF * 2 - 4 ? 512u * FF * 2 - 4 : o_;
                    gtouch(wg, o_); gtouch(wu, o_);
                }
            }
#pragma unroll
            for (int i = 0; i < 6; ++i) { int ln = i * 64 + lane; ln = ln > 343 ? 343 : ln; gtouch(wd, (idx * 344 + ln) * 128); }
        }
        bar();
    }
    unsigned wvo[4];
    {
        const unsigned wv = ((lane >> 5) * 4 + ((lane >> 2) & 3)) * 1024 + ((lane >> 4) & 1) * 64 + (lane & 3) * 16;
#pragma unroll
        for (int i = 0; i < 4; ++i) wvo[i] = wv + i * 8192 - i * 1024;
    }
    {
        const int rot = cb3 & 3;
        bar();
        if (L == 1) {
            {
                const int rl = lane >> 3, cs = lane & 7;
                unsigned xv[4];
#pragma unroll
                for (int i = 0; i < 4; ++i) xv[i] = (8 * i + rl) * 1024 + ((cs ^ ((4 * (i & 1) + (rl >> 1)) & 7)) << 4) - i * 1024;
                const char* xs = x + (size_t)tb3 * 64 * 1024 + cb3 * 128;
                dma4(xs, xv[0], xv[1], xv[2], xv[3], lds0 + P3_X); dma4(xs + 32768, xv[0], xv[1], xv[2], xv[3], lds0 + P3_X + 4096);
            }
#pragma unroll 1
            for (int c = 0; c < 4; ++c) {
                const char* src = wo + cb3 * 128 + (size_t)((c + rot) & 3) * 128 * 1024;
#pragma unroll
                for (int g = 0; g < 4; ++g) dma4(src + g * 32768, wvo[0], wvo[1], wvo[2], wvo[3], lds0 + c * 16384 + g * 4096);
            }
        }
        if (L == 0) {
            if (lane == 0) gst32(ws + WS_FLAG + 256 * 128, b * 128, tag);
            ST(2);
            const int ph = lane & 31;
            poll(flags + 256 * 128, (((ph & 3) * 8 + (tb3 >> 2)) * 8 + (ph >> 2)) * 128, true, tag);
            ST(8);
            const char* ab = ws + WS_OF + (size_t)tb3 * 2 * 32 * 1024;
#pragma unroll 1
            for (int c = 0; c < 4; ++c) {
                const char* src = ab + ((c + rot) & 3) * 8192;
                const unsigned dst = lds0 + 65536 + c * 16384;
                dma4lin(src, lin, dst); dma4lin(src + 4096, lin, dst + 4096); dma4lin(src + 32768, lin, dst + 8192); dma4lin(src + 32768 + 4096, lin, dst + 12288);
                if (c == 1) lbar<16>();
            }
            lbar<32>(); lbar<16>(); lbar<0>();
        } else {
            lbar<0>(); bar(); bar(); bar();
        }
        bar();
    }
    {
        const int rot = cg4 & 7;
        unsigned gvo[12];
        if (L == 1 && p4_on) {
#pragma unroll
            for (int j = 0; j < 12; ++j) {
                const int s = j * 64 + lane, st = s >> 4, kb = st / 3, nb = st % 3, q = (s >> 2) & 3, c = s & 3;
                int blk = cg4 * 3 + nb; blk = blk > 42 ? 42 : blk;
                gvo[j] = (4 * kb + q) * (FF * 2) + blk * 64 + c * 16 - (j & 3) * 1024;
            }
        }
        auto issue_w = [&](int c, unsigned B) {
            const size_t so = (size_t)((c + rot) & 7) * 64 * (FF * 2);
#pragma unroll
            for (int g = 0; g < 3; ++g) dma4(wg + so, gvo[4 * g], gvo[4 * g + 1], gvo[4 * g + 2], gvo[4 * g + 3], lds0 + B + g * 4096);
#pragma unroll
            for (int g = 0; g < 3; ++g) dma4(wu + so, gvo[4 * g], gvo[4 * g + 1], gvo[4 * g + 2], gvo[4 * g + 3], lds0 + B + 12288 + g * 4096);
        };
        bar();
        bar();
        if (L == 1 && p4_on) { issue_w(0, 0); issue_w(1, 24576); issue_w(2, 2 * 24576); issue_w(3, 3 * 24576); }
        if (L == 0) { if (lane == 0) gst32(ws + WS_FLAG + 2 * 256 * 128, b * 128, tag); ST(3); }
        if (p4_on) {
            if (L == 0) {
                const int ptb = tb4 * 2 + (lane & 1), pcb = (lane >> 1) & 7;
                poll(flags + 2 * 256 * 128, ((pcb * 4 + (ptb & 3)) * 8 + (ptb >> 2)) * 128, true, tag);
                ST(9);
                const char* hb = ws + WS_HF + (size_t)tb4 * 4 * 32 * 1024;
                auto issue_h = [&](int c, unsigned B) {
                    const char* src = hb + ((c + rot) & 7) * 4096;
#pragma unroll
                    for (int i = 0; i < 4; ++i) dma4lin(src + i * 32768, lin, lds0 + 98304 + B + i * 4096);
                };
                issue_h(0, 0); issue_h(1, 16384);
                lbar<16>();
                {
                    const char* sb = ws + WS_ST + (size_t)tb4 * 128 * 64;
                    dma4lin(sb, lin, lds0 + P4_ST); dma4lin(sb + 4096, lin, lds0 + P4_ST + 4096);
                    const char* cb_ = ws + WS_CS + cg4 * 384;
                    dma1dw(cb_, lane * 4, lds0 + P4_CS); dma1dw(cb_ + 256, lane * 4, lds0 + P4_CS + 256);
                    dma1dw(cb_ + FF * 4, lane * 4, lds0 + P4_CS + 512); dma1dw(cb_ + FF * 4 + 256, lane * 4, lds0 + P4_CS + 768);
                }
                issue_h(2, 2 * 16384);
                unsigned B = 0;
#pragma unroll 1
                for (int c = 1; c < 8; ++c) {
                    if (c < 7) lbar<16>(); else lbar<0>();
                    if (c + 2 < 8) { issue_h(c + 2, B); B = B == 2 * 16384 ? 0 : B + 16384; }
                }
            } else {
                unsigned B = 0;
#pragma unroll 1
                for (int c = 0; c < 8; ++c) {
                    if (c == 0) lbar<63>(); else if (c < 6) lbar<48>(); else if (c == 6) lbar<24>(); else lbar<0>();
                    if (c >= 1 && c + 3 < 8) { issue_w(c + 3, B); B = B == 3 * 24576 ? 0 : B + 24576; }
                }
            }
            bar();
        } else bar();
    }
    {
        const int rot = cb3;
        auto issue_w = [&](int c, unsigned B) {
            const int cc = c < 10 ? (c + rot) % 10 : 10;
            const char* src = wd + cb3 * 128 + (size_t)cc * 128 * 1024;
#pragma unroll
            for (int g = 0; g < 3; ++g) dma4(src + g * 32768, wvo[0], wvo[1], wvo[2], wvo[3], lds0 + B + g * 4096);
            dma4(src + (c < 10 ? 3 : 2) * 32768, wvo[0], wvo[1], wvo[2], wvo[3], lds0 + B + 3 * 4096);
        };
        bar();
        if (L == 1) {
#pragma unroll 1
            for (int c = 0; c < 6; ++c) issue_w(c, c * 16384);
        }
        if (L == 0) {
            if (p4_on && lane == 0) gst32(ws + WS_FLAG + 3 * 256 * 128, b * 128, tag);
            ST(4);
            const int ptb = tb3 >> 1, pcg = lane < 15 ? lane : 0;
            poll(flags + 3 * 256 * 128, (((pcg & 7) * 4 + (ptb & 3)) * 8 + (ptb >> 2) * 2 + (pcg >> 3)) * 128, true, tag);
            ST(5);
            const char* ab = ws + WS_AF + (size_t)tb3 * 2 * 86 * 1024;
            auto issue_a = [&](int c, unsigned B) {
                const int cc = c < 10 ? (c + rot) % 10 : 10;
                const char* src = ab + cc * 8192;
                const unsigned dst = lds0 + 98304 + B;
                const int o2 = c < 10 ? 4096 : 2048;
                dma4lin(src, lin, dst); dma4lin(src + o2, lin, dst + o2);
                dma4lin(src + 86 * 1024, lin, dst + 8192); dma4lin(src + 86 * 1024 + o2, lin, dst + 8192 + o2);
            };
            issue_a(0, 0); issue_a(1, 16384);
            unsigned B = 2 * 16384;
#pragma unroll 1
            for (int c = 0; c < 11; ++c) {
                if (c < 10) lbar<16>(); else lbar<0>();
                if (c + 2 < 11) { issue_a(c + 2, B); B = B == 2 * 16384 ? 0 : B + 16384; }
            }
        } else {
            unsigned B = 0;
#pragma unroll 1
            for (int c = 0; c < 11; ++c) {
                if (c < 7) lbar<63>(); else if (c == 7) lbar<48>(); else if (c == 8) lbar<32>(); else if (c == 9) lbar<16>(); else lbar<0>();
                if (c >= 1 && c + 5 < 11) { issue_w(c + 5, B); B = B == 5 * 16384 ? 0 : B + 16384; }
            }
        }
        bar();
    }
}

extern "C" void kernel_launch(void* const* d_in, const int* in_sizes, int n_in, void* d_out, int out_size, void* d_ws, size_t ws_size, hipStream_t stream) {
    (void)hipFuncSetAttribute((const void*)k_block, hipFuncAttributeMaxDynamicSharedMemorySize, LDS_BYTES);
    k_block<<<256, 640, LDS_BYTES, stream>>>((const char*)d_in[0], (const char*)d_in[1], (const char*)d_in[2], (const char*)d_in[4], (const char*)d_in[5], (const char*)d_in[6],
                                             (char*)d_ws, (char*)d_out);
}
```

```cpp
#include <hip/hip_runtime.h>
#include <hip/hip_bf16.h>
#include <stdint.h>

typedef __bf16 bf16x8 __attribute__((ext_vector_type(8)));
typedef __bf16 bf16x2 __attribute__((ext_vector_type(2)));
typedef short s16x4 __attribute__((ext_vector_type(4)));
typedef short s16x8 __attribute__((ext_vector_type(8)));
typedef float f32x16 __attribute__((ext_vector_type(16)));
typedef float f32x4 __attribute__((ext_vector_type(4)));
typedef float f32x2 __attribute__((ext_vector_type(2)));
typedef unsigned u32x4 __attribute__((ext_vector_type(4)));
typedef unsigned u32x2 __attribute__((ext_vector_type(2)));
#define LAS __attribute__((address_space(3)))
#define DI __device__ __forceinline__

constexpr int DM = 512, FF = 1376;
constexpr float LOG2E = 1.4426950408889634f;
constexpr float QSCALE = 0.25f * LOG2E;
constexpr float LN_EPS = 1e-5f;

constexpr size_t WS_QF = 0, WS_KF = 2u << 20, WS_VF = 4u << 20, WS_OF = 6u << 20, WS_HF = 8u << 20, WS_AF = 10u << 20,
                 WS_ST = 16u << 20, WS_CS = 17u << 20, WS_KM = (17u << 20) + 65536, WS_FLAG = 18u << 20, WS_EPOCH = WS_FLAG + 4 * 256 * 128,
                 WS_G1 = 19u << 20, WS_G2 = WS_G1 + 65536;
constexpr int LDS_BYTES = 163840;
constexpr unsigned SPIN_CAP = 1u << 17;
#define CODE_LINES_ 280
constexpr int CODE_LINES = CODE_LINES_;

#define MFMA(a, b, c) __builtin_amdgcn_mfma_f32_32x32x16_bf16(a, b, c, 0, 0, 0)
#define SB() __builtin_amdgcn_sched_barrier(0)
DI void cbar() { asm volatile("s_waitcnt lgkmcnt(0)\n\ts_barrier" ::: "memory"); }
DI void pbar() { asm volatile("s_waitcnt vmcnt(0) lgkmcnt(0)\n\ts_barrier" ::: "memory"); }
template <int N> DI void lbar() { asm volatile("s_waitcnt vmcnt(%0)\n\ts_barrier" ::"i"(N > 63 ? 63 : N) : "memory"); }
DI void bar() { asm volatile("s_barrier" ::: "memory"); }

DI void dma4(const void* sbase, unsigned v0, unsigned v1, unsigned v2, unsigned v3, unsigned dst) {
    asm volatile("s_mov_b32 m0, %5\n\ts_nop 0\n\tglobal_load_lds_dwordx4 %0, %4 sc1\n\tglobal_load_lds_dwordx4 %1, %4 offset:1024 sc1\n\t"
                 "global_load_lds_dwordx4 %2, %4 offset:2048 sc1\n\tglobal_load_lds_dwordx4 %3, %4 offset:3072 sc1"
                 :: "v"(v0), "v"(v1), "v"(v2), "v"(v3), "s"(sbase), "s"(dst) : "memory");
}
DI void dma4lin(const void* sbase, unsigned v, unsigned dst) { dma4(sbase, v, v, v, v, dst); }
DI void dma2lin(const void* sbase, unsigned v, unsigned dst) {
    asm volatile("s_mov_b32 m0, %2\n\ts_nop 0\n\tglobal_load_lds_dwordx4 %0, %1 sc1\n\tglobal_load_lds_dwordx4 %0, %1 offset:1024 sc1" :: "v"(v), "s"(sbase), "s"(dst) : "memory");
}
DI void dma1dw(const void* sbase, unsigned v, unsigned dst) {
    asm volatile("s_mov_b32 m0, %2\n\ts_nop 0\n\tglobal_load_lds_dword %0, %1 sc1" :: "v"(v), "s"(sbase), "s"(dst) : "memory");
}
DI void gtouch(const void* sbase, unsigned voff) {
    asm volatile("global_load_lds_dword %0, %1" :: "v"(voff), "s"(sbase) : "memory");
}
DI unsigned gld32_wait(const void* sbase, unsigned voff) {
    unsigned r; asm volatile("global_load_dword %0, %1, %2 sc1\n\ts_waitcnt vmcnt(0)" : "=&v"(r) : "v"(voff), "s"(sbase) : "memory"); return r;
}
DI void gst128(void* sbase, unsigned voff, u32x4 v) { asm volatile("global_store_dwordx4 %0, %1, %2 sc1\n\ts_nop 1" ::"v"(voff), "v"(v), "s"(sbase) : "memory"); }
DI void gst64(void* sbase, unsigned voff, u32x2 v) { asm volatile("global_store_dwordx2 %0, %1, %2 sc1\n\ts_nop 1" ::"v"(voff), "v"(v), "s"(sbase) : "memory"); }
DI void gst32(void* sbase, unsigned voff, unsigned v) { asm volatile("global_store_dword %0, %1, %2 sc1\n\ts_nop 1" ::"v"(voff), "v"(v), "s"(sbase) : "memory"); }
#define VWAIT_(N) asm volatile("s_waitcnt vmcnt(" #N ")" ::: "memory")
#define VWAIT(N) VWAIT_(N)
#define PIN(x) asm volatile("" : "+v"(x))
DI unsigned rfl(unsigned x) { return __builtin_amdgcn_readfirstlane(x); }
DI unsigned cvtpk(float lo, float hi) {
    f32x2 v = {lo, hi};
    bf16x2 b = __builtin_convertvector(v, bf16x2);
    return __builtin_bit_cast(unsigned, b);
}
DI float bflo(unsigned u) { return __uint_as_float(u << 16); }
DI float bfhi(unsigned u) { return __uint_as_float(u & 0xffff0000u); }
DI bf16x8 lds_b128(unsigned a) { return *(LAS const bf16x8*)(uintptr_t)a; }
typedef short v4i16_t __attribute__((ext_vector_type(4)));
DI s16x4 lds_tr(unsigned a) { return __builtin_bit_cast(s16x4, __builtin_amdgcn_ds_read_tr16_b64_v4i16((LAS v4i16_t*)(uintptr_t)a)); }
DI bf16x8 lds_trfrag(unsigned a, int off2) {
    s16x4 x = lds_tr(a), y = lds_tr(a + off2);
    s16x8 r = __builtin_shufflevector(x, y, 0, 1, 2, 3, 4, 5, 6, 7);
    return __builtin_bit_cast(bf16x8, r);
}
DI float dot2(unsigned a, unsigned b, float c) {
    return __builtin_amdgcn_fdot2_f32_bf16(__builtin_bit_cast(bf16x2, a), __builtin_bit_cast(bf16x2, b), c, false);
}
DI float other_half(float v) {
    auto r = __builtin_amdgcn_permlane32_swap(__float_as_uint(v), __float_as_uint(v), false, false);
    return __uint_as_float((threadIdx.x & 32) ? r[0] : r[1]);
}
DI float sum_halves(float v) {
    auto r = __builtin_amdgcn_permlane32_swap(__float_as_uint(v), __float_as_uint(v), false, false);
    return __uint_as_float(r[0]) + __uint_as_float(r[1]);
}
template <bool ALL> DI float dpp_max(float v) {
#define DPPMAX(ctrl, rm) v = fmaxf(v, __int_as_float(__builtin_amdgcn_update_dpp(0, __float_as_int(v), ctrl, rm, 0xf, true)))
    DPPMAX(0x111, 0xf); DPPMAX(0x112, 0xf); DPPMAX(0x114, 0xf); DPPMAX(0x118, 0xf); DPPMAX(0x142, 0xa);
    if (ALL) DPPMAX(0x143, 0xc);
    return v;
}
template <int CTRL> DI float dpp_add(float v) { return v + __int_as_float(__builtin_amdgcn_update_dpp(0, __float_as_int(v), CTRL, 0xf, 0xf, true)); }
DI u32x4 swap4(unsigned p0, unsigned p1, unsigned p2, unsigned p3) {
    auto s0 = __builtin_amdgcn_permlane32_swap(p0, p2, false, false);
    auto s1 = __builtin_amdgcn_permlane32_swap(p1, p3, false, false);
    u32x4 r = {s0[0], s1[0], s0[1], s1[1]};
    return r;
}
DI u32x4 pack_swap(float a0, float a1, float a2, float a3, float a4, float a5, float a6, float a7) {
    return swap4(cvtpk(a0, a1), cvtpk(a2, a3), cvtpk(a4, a5), cvtpk(a6, a7));
}
constexpr unsigned ONES = 0x3f803f80u;

#define ST(k)
#define ST2(k)

DI void poll(const char* fl, unsigned voff, bool active, unsigned tag) {
    for (unsigned spins = 0; spins < SPIN_CAP; ++spins) {
        unsigned f;
        asm volatile("global_load_dword %0, %1, %2 sc1\n\ts_waitcnt vmcnt(0)" : "=&v"(f) : "v"(voff), "s"(fl) : "memory");
        if (__all(!active || f == tag)) break;
        __builtin_amdgcn_s_sleep(1);
    }
}

struct Ptrs { const char *x, *wqkv, *wo, *wg, *wu, *wd; char* ws; char* out; };

#define NOTOUCH1
#define P1ROT 0
#define T1W 16
#define P1NB 4
constexpr int P1_BUF = 32768, P1_MISC = P1NB * P1_BUF;

DI void gst_granule(char* sbase, unsigned voff, float v, unsigned tag) { u32x2 g = {__float_as_uint(v), tag}; gst64(sbase, voff, g); }
DI float sum_x16(float v) {
    auto r = __builtin_amdgcn_permlane16_swap(__float_as_uint(v), __float_as_uint(v), false, false);
    return __uint_as_float(r[0]) + __uint_as_float(r[1]);
}
template <bool ISV>
DI void p1_epilogue(char* ws, int tb, int cb, char* smem, int tl, int cp, const f32x16& accA, const f32x16& accB, int lane) {
    const int r = lane & 31, hh = lane >> 5;
    const LAS f32x2* st = (const LAS f32x2*)(smem + P1_MISC);
    const LAS float* csl = (const LAS float*)(smem + P1_MISC + 1024);
    const int sec = cb >> 2, tile = tb * 4 + tl;
#pragma unroll
    for (int b = 0; b < 2; ++b) {
        const f32x16& acc = b ? accB : accA;
        const int ct = 2 * cp + b, head0 = (cb & 3) * 8 + ct * 2;
        float v[16];
        if constexpr (!ISV) {
            const f32x2 ms = st[tl * 32 + r];
            const float sc = ms[1] * (sec == 0 ? QSCALE : 1.f), msc = -ms[0] * sc;
#pragma unroll
            for (int g = 0; g < 4; ++g) {
                const f32x4 c4 = *(LAS const f32x4*)(csl + ct * 32 + 8 * g + 4 * hh);
#pragma unroll
                for (int e = 0; e < 4; ++e) v[4 * g + e] = acc[4 * g + e] * sc + msc * c4[e];
            }
            char* dst = ws + (sec == 0 ? WS_QF : WS_KF) + ((size_t)head0 * 64 + tile) * 1024;
#pragma unroll
            for (int hs = 0; hs < 2; ++hs) {
                const u32x4 o = pack_swap(v[8 * hs], v[8 * hs + 1], v[8 * hs + 2], v[8 * hs + 3], v[8 * hs + 4], v[8 * hs + 5], v[8 * hs + 6], v[8 * hs + 7]);
                gst128(dst + hs * 65536, lane * 16, o);
                if (sec == 1) {
                    float n2 = 0.f;
#pragma unroll
                    for (int e = 0; e < 8; ++e) n2 = fmaf(v[8 * hs + e], v[8 * hs + e], n2);
                    n2 = dpp_max<false>(sum_halves(n2));
                    if (lane == 31) gst32(ws + WS_KM + (head0 + hs) * 256 + tile * 4, 0, __float_as_uint(n2));
                }
            }
        } else {
            const float csn = csl[ct * 32 + r];
#pragma unroll
            for (int g = 0; g < 4; ++g) {
                const LAS f32x4* sp = (const LAS f32x4*)(st + tl * 32 + 8 * g + 4 * hh);
                const f32x4 pa = sp[0], pb = sp[1];
                v[4 * g] = (acc[4 * g] - pa[0] * csn) * pa[1]; v[4 * g + 1] = (acc[4 * g + 1] - pa[2] * csn) * pa[3];
                v[4 * g + 2] = (acc[4 * g + 2] - pb[0] * csn) * pb[1]; v[4 * g + 3] = (acc[4 * g + 3] - pb[2] * csn) * pb[3];
            }
            char* dst = ws + WS_VF + ((size_t)head0 * 64 + tile) * 1024;
            const unsigned vo = (r >> 4) * 65536 + hh * 256 + (r & 15) * 16;
#pragma unroll
            for (int s = 0; s < 2; ++s) {
                u32x4 o = {cvtpk(v[8 * s], v[8 * s + 1]), cvtpk(v[8 * s + 2], v[8 * s + 3]), cvtpk(v[8 * s + 4], v[8 * s + 5]), cvtpk(v[8 * s + 6], v[8 * s + 7])};
                gst128(dst + s * 512, vo, o);
            }
        }
    }
}
constexpr int P1_XCH = (5 % P1NB) * P1_BUF;

DI void p1_colsum(const char* wqkv, char* ws, int tb, int cb, char* smem, int j, int lane, f32x16& accA, f32x16& accB) {
    const unsigned tag = __hip_atomic_load((const __attribute__((address_space(1))) unsigned*)(ws + WS_EPOCH), __ATOMIC_RELAXED, __HIP_MEMORY_SCOPE_AGENT) + 1;
    const char* src = wqkv + (size_t)((tb & 3) * 128 + j * 32 + (lane >> 4)) * 3072 + cb * 256 + (lane & 15) * 16;
    u32x4 v[8];
#pragma unroll
    for (int i = 0; i < 8; ++i) v[i] = *(const u32x4*)(src + (size_t)i * 4 * 3072);
    cbar(); cbar();
    float s[8] = {};
#pragma unroll
    for (int i = 0; i < 8; ++i)
#pragma unroll
        for (int e = 0; e < 4; ++e) { s[2 * e] += bflo(v[i][e]); s[2 * e + 1] += bfhi(v[i][e]); }
#pragma unroll
    for (int e = 0; e < 8; ++e) s[e] = sum_halves(sum_x16(s[e]));
    LAS float* part = (LAS float*)(smem + P1_MISC + 2048);
    if (lane < 16) {
        f32x4 a = {s[0], s[1], s[2], s[3]}, c = {s[4], s[5], s[6], s[7]};
        *(LAS f32x4*)(part + j * 128 + lane * 8) = a; *(LAS f32x4*)(part + j * 128 + lane * 8 + 4) = c;
    }
    cbar();
    if (j == 0) {
#pragma unroll
        for (int k = 0; k < 2; ++k) {
            const int c = lane + 64 * k;
            gst_granule(ws + WS_G2, ((cb * 128 + c) * 4 + (tb & 3)) * 8, part[c] + part[128 + c] + part[256 + c] + part[384 + c], tag);
        }
    }
    cbar(); cbar(); cbar(); cbar(); cbar();
    cbar();
    f32x16 acc[2];
    {
        const LAS f32x4* xr = (const LAS f32x4*)(smem + P1_XCH + j * 8192 + lane * 16);
#pragma unroll
        for (int b = 0; b < 2; ++b)
#pragma unroll
            for (int g = 0; g < 4; ++g) { const f32x4 t = xr[(b * 4 + g) * 64]; acc[b][4 * g] = t[0]; acc[b][4 * g + 1] = t[1]; acc[b][4 * g + 2] = t[2]; acc[b][4 * g + 3] = t[3]; }
    }
    accA = acc[0]; accB = acc[1];
}

template <bool ISV>
DI void p1_compute(char* ws, int tb, int cb, char* smem, int w, int lane, f32x16& accA, f32x16& accB) {
    const int r = lane & 31, hh = lane >> 5;
    const unsigned lds0 = (unsigned)(uintptr_t)smem;
    const int tp = w & 1, cp = w >> 1;
    unsigned x0[4];
    {
        const int f = (r >> 1) & 7;
#pragma unroll
        for (int ks = 0; ks < 4; ++ks) x0[ks] = lds0 + (64 * tp + r) * 128 + (((2 * ks + hh) ^ f) << 4);
    }
    const unsigned wro = lds0 + 16384 + hh * 2048 + ((lane & 15) >> 2) * 64 + ((lane >> 4) & 1) * 32 + (lane & 3) * 8 + cp * 512;
    f32x16 acc[2][2] = {};
    bf16x8 X[4][2], W[4][2];
#define P1_LOAD(S, B, ks) { X[S][0] = lds_b128(x0[ks] + (B)); X[S][1] = lds_b128(x0[ks] + (B) + 4096); W[S][0] = lds_trfrag(wro + (B) + (ks) * 4096, 1024); W[S][1] = lds_trfrag(wro + (B) + (ks) * 4096 + 256, 1024); }
#define P1_MMA(S)                                                                                                                 \
    {                                                                                                                             \
        _Pragma("unroll") for (int a = 0; a < 2; ++a) _Pragma("unroll") for (int b = 0; b < 2; ++b)                                \
            acc[a][b] = ISV ? MFMA(X[S][a], W[S][b], acc[a][b]) : MFMA(W[S][b], X[S][a], acc[a][b]);                               \
    }
#define P1_ILV() { __builtin_amdgcn_sched_group_barrier(0x8, 1, 0); __builtin_amdgcn_sched_group_barrier(0x100, 2, 0); __builtin_amdgcn_sched_group_barrier(0x8, 1, 0); __builtin_amdgcn_sched_group_barrier(0x100, 2, 0); \
                   __builtin_amdgcn_sched_group_barrier(0x8, 1, 0); __builtin_amdgcn_sched_group_barrier(0x100, 2, 0); __builtin_amdgcn_sched_group_barrier(0x8, 1, 0); SB(); }
    cbar();
    ST(11);
    P1_LOAD(0, 0, 0); P1_LOAD(1, 0, 1);
    SB();
    unsigned B = 0;
#pragma unroll 1
    for (int ch = 0; ch < 7; ++ch) {
        P1_LOAD(2, B, 2); P1_MMA(0); P1_ILV();
        P1_LOAD(3, B, 3); P1_MMA(1); P1_ILV();
        cbar();
        B = B == (P1NB - 1) * P1_BUF ? 0 : B + P1_BUF;
        P1_LOAD(0, B, 0); P1_MMA(2); P1_ILV();
        P1_LOAD(1, B, 1); P1_MMA(3); P1_ILV();
    }
    P1_LOAD(2, B, 2); P1_MMA(0); P1_ILV();
    P1_LOAD(3, B, 3); P1_MMA(1); P1_ILV();
    P1_MMA(2); P1_MMA(3);
    ST(12);
    {
        LAS f32x4* xw = (LAS f32x4*)(smem + P1_XCH + w * 8192 + lane * 16);
#pragma unroll
        for (int b = 0; b < 2; ++b)
#pragma unroll
            for (int g = 0; g < 4; ++g) { f32x4 t = {acc[1][b][4 * g], acc[1][b][4 * g + 1], acc[1][b][4 * g + 2], acc[1][b][4 * g + 3]}; xw[(b * 4 + g) * 64] = t; }
    }
    cbar();
    accA = acc[0][0]; accB = acc[0][1];
}

typedef const __attribute__((address_space(1))) char* gcptr;
template <int NROW16>
DI void colsum32(gcptr W, unsigned rs, int lane, float (&s)[8]) {
    const unsigned vo = (lane & 15) * rs + (lane >> 4) * 16;
    u32x4 v[NROW16];
#pragma unroll
    for (int i = 0; i < NROW16; ++i) v[i] = *(const __attribute__((address_space(1))) u32x4*)(W + (size_t)i * 16 * rs + vo);
    __builtin_amdgcn_sched_barrier(0);
#pragma unroll
    for (int e = 0; e < 8; ++e) s[e] = 0.f;
#pragma unroll
    for (int i = 0; i < NROW16; ++i)
#pragma unroll
        for (int e = 0; e < 4; ++e) { s[2 * e] += bflo(v[i][e]); s[2 * e + 1] += bfhi(v[i][e]); }
#pragma unroll
    for (int e = 0; e < 8; ++e) s[e] = dpp_add<0x118>(dpp_add<0x114>(dpp_add<0x112>(dpp_add<0x111>(s[e]))));
}
DI void p1_helper(const char* px, const char* pqkv, const char* pg, const char* pu, char* ws, int h, int w, int lane) {
    if (w < 4) {
        const unsigned tag = __hip_atomic_load((const __attribute__((address_space(1))) unsigned*)(ws + WS_EPOCH), __ATOMIC_RELAXED, __HIP_MEMORY_SCOPE_AGENT) + 1;
        const int u = w * 64 + h;
        if (u < 256) {
            const int rl = lane >> 3, seg = lane & 7;
            const char* src = px + (size_t)(u * 8 + rl) * 1024 + seg * 16;
            u32x4 v[8];
#pragma unroll
            for (int i = 0; i < 8; ++i) v[i] = *(const u32x4*)(src + i * 128);
            float s1 = 0.f, s2 = 0.f;
#pragma unroll
            for (int i = 0; i < 8; ++i)
#pragma unroll
                for (int e = 0; e < 4; ++e) { s1 = dot2(v[i][e], ONES, s1); s2 = dot2(v[i][e], v[i][e], s2); }
            s1 = dpp_add<0x114>(dpp_add<0x112>(dpp_add<0x111>(s1))); s2 = dpp_add<0x114>(dpp_add<0x112>(dpp_add<0x111>(s2)));
            const float mean = s1 * (1.f / DM);
            const float rstd = __builtin_amdgcn_rsqf(fmaxf(s2 * (1.f / DM) - mean * mean, 0.f) + LN_EPS);
            if (seg == 7) { gst_granule(ws + WS_G1, (u * 8 + rl) * 16, mean, tag); gst_granule(ws + WS_G1, (u * 8 + rl) * 16 + 8, rstd, tag); }
        }
    }
    const int job = w == 4 ? h : (w == 5 && h < 22) ? 64 + h : -1;
    if (job >= 0) {
        const int blk = job < 43 ? job : job - 43;
        const uintptr_t wa = (uintptr_t)pg + (job < 43 ? 0 : (uintptr_t)pu - (uintptr_t)pg);
        float s[8];
        colsum32<32>((gcptr)wa + blk * 64, FF * 2, lane, s);
        if ((lane & 15) == 15) {
            char* dst = ws + WS_CS + (job < 43 ? 0 : FF) * 4;
            const int c0 = blk * 32 + (lane >> 4) * 8;
            u32x4 a = {__float_as_uint(s[0]), __float_as_uint(s[1]), __float_as_uint(s[2]), __float_as_uint(s[3])};
            u32x4 c = {__float_as_uint(s[4]), __float_as_uint(s[5]), __float_as_uint(s[6]), __float_as_uint(s[7])};
            gst128(dst, c0 * 4, a); gst128(dst, c0 * 4 + 16, c);
        }
    }
}

DI unsigned p1_load(const Ptrs& P, int L, int tb, int cb, int rot, int sidx, unsigned lds0, int lane) {
    unsigned vo[4];
    const char* base;
    unsigned cstep, gstep;
    if (L == 0) {
        const int rl = lane >> 3, cs = lane & 7;
#pragma unroll
        for (int i = 0; i < 4; ++i) vo[i] = (8 * i + rl) * 1024 + ((cs ^ ((4 * (i & 1) + (rl >> 1)) & 7)) << 4) - i * 1024;
        base = P.x + (size_t)tb * 128 * 1024; cstep = 128; gstep = 4 * 8192;
    } else {
        const int nb = lane >> 4, q = (lane >> 2) & 3, c = lane & 3;
#pragma unroll
        for (int i = 0; i < 4; ++i) vo[i] = (4 * i + q) * 3072 + nb * 64 + c * 16 - i * 1024;
        base = P.wqkv + cb * 256; cstep = 64 * 3072; gstep = 16 * 3072;
    }
    auto issue = [&](int c, unsigned B) {
        const char* src = base + (size_t)((c + rot) & 7) * cstep;
        const unsigned dst = lds0 + B + L * 16384;
#pragma unroll
        for (int g = 0; g < 4; ++g) dma4(src + (size_t)g * gstep, vo[0], vo[1], vo[2], vo[3], dst + g * 4096);
    };
    issue(0, 0);
#pragma unroll
    for (int c = 1; c < P1NB - 1; ++c) issue(c, c * P1_BUF);
    unsigned B = (P1NB - 1) * P1_BUF;
    constexpr int LA = P1NB - 1;
#pragma unroll 1
    for (int c = 0; c < 8; ++c) {
        const int y = 7 - c < LA - 1 ? 7 - c : LA - 1;
        if (c == 0) lbar<16 * (LA - 1) + T1W - 16>(); else if (y == 3) lbar<48>(); else if (y == 2) lbar<32>(); else if (y == 1) lbar<16>(); else lbar<0>();
        if (c + LA < 8) { issue(c + LA, B); B = B == (P1NB - 1) * P1_BUF ? 0 : B + P1_BUF; }
    }
    const unsigned tag = rfl(gld32_wait(P.ws + WS_EPOCH, 0)) + 1;
    if (L == 1) {
        u32x4 a, b, c0, c1, c2, c3;
        for (unsigned spins = 0; spins < SPIN_CAP; ++spins) {
            asm volatile("global_load_dwordx4 %0, %6, %7 sc1\n\tglobal_load_dwordx4 %1, %6, %7 offset:16 sc1\n\t"
                         "global_load_dwordx4 %2, %8, %9 sc1\n\tglobal_load_dwordx4 %3, %8, %9 offset:16 sc1\n\t"
                         "global_load_dwordx4 %4, %8, %9 offset:32 sc1\n\tglobal_load_dwordx4 %5, %8, %9 offset:48 sc1\n\ts_waitcnt vmcnt(0)"
                         : "=&v"(a), "=&v"(b), "=&v"(c0), "=&v"(c1), "=&v"(c2), "=&v"(c3)
                         : "v"(lane * 32), "s"(P.ws + WS_G1 + tb * 128 * 16), "v"(lane * 64), "s"(P.ws + WS_G2 + cb * 128 * 32) : "memory");
            if (__all(a[1] == tag && a[3] == tag && b[1] == tag && b[3] == tag && c0[1] == tag && c0[3] == tag && c1[1] == tag && c1[3] == tag &&
                      c2[1] == tag && c2[3] == tag && c3[1] == tag && c3[3] == tag)) break;
            __builtin_amdgcn_s_sleep(1);
        }
        { char* ws = P.ws; ST(13); }
        u32x4 ms = {a[0], a[2], b[0], b[2]};
        u32x2 cs = {__float_as_uint(__uint_as_float(c0[0]) + __uint_as_float(c0[2]) + __uint_as_float(c1[0]) + __uint_as_float(c1[2])),
                    __float_as_uint(__uint_as_float(c2[0]) + __uint_as_float(c2[2]) + __uint_as_float(c3[0]) + __uint_as_float(c3[2]))};
        *(LAS u32x4*)(uintptr_t)(lds0 + P1_MISC + lane * 16) = ms;
        *(LAS u32x2*)(uintptr_t)(lds0 + P1_MISC + 1024 + lane * 8) = cs;
        asm volatile("s_waitcnt lgkmcnt(0)" ::: "memory");
    }
    bar();
    return tag;
}

constexpr int P2_X = 131072 + 2048;
constexpr int P2_KM = P2_X + 8 * 2560;
constexpr float SAFE_RANGE = 80.f;

DI void p2_compute(char* ws, char* smem, int head, int qb, int w, int lane) {
    const int r = lane & 31, hh = lane >> 5;
    const unsigned lds0 = (unsigned)(uintptr_t)smem;
    const int qg = w & 3, kh = w >> 2;
    const int la = qg * 2 + kh, lb = qg * 2 + (1 - kh);
    cbar();
    ST(13);
    const u32x4 qa = *(LAS const u32x4*)(uintptr_t)(lds0 + P2_X + la * 1024 + lane * 16), qbv = *(LAS const u32x4*)(uintptr_t)(lds0 + P2_X + lb * 1024 + lane * 16);
    const float kmax2 = __int_as_float(__builtin_amdgcn_readlane(__float_as_int(dpp_max<true>(*(LAS const float*)(uintptr_t)(lds0 + P2_KM + lane * 4))), 63));
    float qa2 = 0.f, qb2 = 0.f;
#pragma unroll
    for (int e = 0; e < 4; ++e) { qa2 = dot2(qa[e], qa[e], qa2); qb2 = dot2(qbv[e], qbv[e], qb2); }
    const float bnda = __builtin_amdgcn_sqrtf(sum_halves(qa2) * kmax2) * 1.01f, bndb = __builtin_amdgcn_sqrtf(sum_halves(qb2) * kmax2) * 1.01f;
    const bool fast = __all(2.f * bnda <= SAFE_RANGE && 2.f * bndb <= SAFE_RANGE);
    const unsigned ka = lds0 + kh * 32768 + lane * 16, va = lds0 + 65536 + kh * 32768 + hh * 256 + (r & 15) * 16;
    const unsigned xw = lds0 + P2_X + w * 2560 + lane * 16, xr = lds0 + P2_X + (w ^ 4) * 2560 + lane * 16;
    float o[9], ma;
    ST(5);
    if (fast) {
        ma = bnda;
        unsigned kp = ka, vp = va, cnt;
#define EXP(r) "v_exp_f32 v" #r ", v" #r "\n\t"
#define CVT(d, a, b) "v_cvt_pk_bf16_f32 v" #d ", v" #a ", v" #b "\n\t"
#define MSQ(SN, KN, Q, C) "v_mfma_f32_32x32x16_bf16 " SN ", " KN ", " Q ", " C "\n\t"
#define MPO(O, V, P) "v_mfma_f32_32x32x16_bf16 " O ", " V ", " P ", " O "\n\t"
#define RDV(VN0, VN1, VOFF) "s_mov_b64 exec, %[msk]\n\t" "ds_read_b128 " VN0 ", %[vp] offset:" #VOFF "\n\t" "ds_read_b128 " VN1 ", %[vp] offset:" #VOFF "+512\n\t" "s_mov_b64 exec, -1\n\t"
#define RDK(KNN, KOFF) "ds_read_b128 " KNN ", %[kp] offset:" #KOFF "\n\t"
#define STEP(PRE, c0, c1, c2, c3, c4, c5, c6, c7, c8, c9, c10, c11, c12, c13, c14, c15, p14, p15, SN, KN, Q, CC, O, OP, VC0, VP1)  \
        EXP(c0) EXP(c1) CVT(127, p14, p15) EXP(c2) EXP(c3) MPO(OP, VP1, "v[124:127]") PRE                                    \
        CVT(120, c0, c1) EXP(c4) EXP(c5) CVT(121, c2, c3) EXP(c6)                                                            \
        "s_waitcnt lgkmcnt(3)\n\t" MSQ(SN, KN, Q, CC)                                                                        \
        EXP(c7) CVT(122, c4, c5) EXP(c8) EXP(c9) CVT(123, c6, c7) EXP(c10) EXP(c11)                                          \
        MPO(O, VC0, "v[120:123]")                                                                                            \
        CVT(124, c8, c9) EXP(c12) EXP(c13) CVT(125, c10, c11) EXP(c14) EXP(c15) CVT(126, c12, c13)
#define SA_ 32, 33, 34, 35, 36, 37, 38, 39, 40, 41, 42, 43, 44, 45, 46, 47
#define SB_ 48, 49, 50, 51, 52, 53, 54, 55, 56, 57, 58, 59, 60, 61, 62, 63
#define CALL(M, ...) M(__VA_ARGS__)
#define TE(KOFF, VOFF) CALL(STEP, RDV("v[112:115]", "v[116:119]", VOFF), SA_, 62, 63, "v[48:63]", "v[96:99]", "%[qb]", "v[144:159]", "v[64:79]", "v[80:95]", "v[104:107]", "v[116:119]") \
                       CALL(STEP, RDK("v[96:99]", KOFF), SB_, 46, 47, "v[32:47]", "v[100:103]", "%[qa]", "v[128:143]", "v[80:95]", "v[64:79]", "v[104:107]", "v[108:111]")
#define TO(KOFF, VOFF) CALL(STEP, RDV("v[104:107]", "v[108:111]", VOFF), SA_, 62, 63, "v[48:63]", "v[100:103]", "%[qb]", "v[144:159]", "v[64:79]", "v[80:95]", "v[112:115]", "v[108:111]") \
                       CALL(STEP, RDK("v[100:103]", KOFF), SB_, 46, 47, "v[32:47]", "v[96:99]", "%[qa]", "v[128:143]", "v[80:95]", "v[64:79]", "v[112:115]", "v[116:119]")
#define M64(a, b, c, d) "v_mov_b64 v[" #a ":" #b "], v[" #c ":" #d "]\n\t"
#define MOV4(a, b, c, d, src) "v_mov_b32 v" #a ", " src "\n\tv_mov_b32 v" #b ", " src "\n\tv_mov_b32 v" #c ", " src "\n\tv_mov_b32 v" #d ", " src "\n\t"
        asm volatile(
            "s_setprio 3\n\t"
            "ds_read_b128 v[96:99], %[kp]\n\t"
            "v_mov_b32 v128, %[na]\n\tv_mov_b32 v129, %[na]\n\tv_mov_b32 v144, %[nb]\n\tv_mov_b32 v145, %[nb]\n\t"
            "v_mov_b32 v104, %[one]\n\tv_mov_b32 v105, %[one]\n\tv_mov_b64 v[64:65], 0\n\t"
            M64(106, 107, 104, 105) M64(108, 109, 104, 105) M64(110, 111, 104, 105) M64(112, 113, 104, 105) M64(114, 115, 104, 105) M64(116, 117, 104, 105) M64(118, 119, 104, 105)
            "s_mov_b64 exec, %[msk]\n\t"
            "ds_read_b128 v[104:107], %[vp]\n\t"
            "ds_read_b128 v[108:111], %[vp] offset:512\n\t"
            "s_mov_b64 exec, -1\n\t"
            "ds_read_b128 v[100:103], %[kp] offset:1024\n\t"
            M64(130, 131, 128, 129) M64(132, 133, 128, 129) M64(134, 135, 128, 129) M64(136, 137, 128, 129) M64(138, 139, 128, 129) M64(140, 141, 128, 129) M64(142, 143, 128, 129)
            "s_waitcnt lgkmcnt(3)\n\t"
            "s_nop 1\n\t"
            "v_mfma_f32_32x32x16_bf16 v[32:47], v[96:99], %[qa], v[128:143]\n\t"
            M64(146, 147, 144, 145) M64(148, 149, 144, 145) M64(150, 151, 144, 145) M64(152, 153, 144, 145) M64(154, 155, 144, 145) M64(156, 157, 144, 145) M64(158, 159, 144, 145)
            M64(66, 67, 64, 65) M64(68, 69, 64, 65) M64(70, 71, 64, 65) M64(72, 73, 64, 65) M64(74, 75, 64, 65) M64(76, 77, 64, 65) M64(78, 79, 64, 65)
            M64(80, 81, 64, 65) M64(82, 83, 64, 65) M64(84, 85, 64, 65) M64(86, 87, 64, 65) M64(88, 89, 64, 65) M64(90, 91, 64, 65) M64(92, 93, 64, 65) M64(94, 95, 64, 65)
            M64(62, 63, 64, 65) M64(124, 125, 64, 65) M64(126, 127, 64, 65)
            "s_mov_b32 %[cnt], 0\n\t"
            "L_att_%=:\n\t"
            TE(2048, 1024) TO(3072, 2048) TE(4096, 3072) TO(5120, 4096)
            "v_add_u32 %[kp], 0x1000, %[kp]\n\t"
            "v_add_u32 %[vp], 0x1000, %[vp]\n\t"
            "s_cmp_lg_u32 %[cnt], 0\n\t"
            "s_cbranch_scc1 L_att_nb_%=\n\t"
            "s_barrier\n\t"
            "L_att_nb_%=:\n\t"
            "s_add_u32 %[cnt], %[cnt], 1\n\t"
            "s_cmp_eq_u32 %[cnt], 3\n\ts_cbranch_scc0 L_p1_%=\n\ts_setprio 2\n\tL_p1_%=:\n\t"
            "s_cmp_eq_u32 %[cnt], 6\n\ts_cbranch_scc0 L_p2_%=\n\ts_setprio 1\n\tL_p2_%=:\n\t"
            "s_cmp_eq_u32 %[cnt], 7\n\ts_cbranch_scc0 L_p3_%=\n\ts_setprio 0\n\tL_p3_%=:\n\t"
            "s_cmp_lt_u32 %[cnt], 8\n\t"
            "s_cbranch_scc1 L_att_%=\n\t"
            CVT(127, 62, 63)
            "s_nop 1\n\t"
            MPO("v[80:95]", "v[116:119]", "v[124:127]")
            "s_waitcnt lgkmcnt(0)\n\t"
            "s_nop 15\n\t"
            "v_mov_b32 v89, %[bb]\n\t"
            "v_mov_b32 %[o0], v64\n\tv_mov_b32 %[o1], v65\n\tv_mov_b32 %[o2], v66\n\tv_mov_b32 %[o3], v67\n\t"
            "v_mov_b32 %[o4], v68\n\tv_mov_b32 %[o5], v69\n\tv_mov_b32 %[o6], v70\n\tv_mov_b32 %[o7], v71\n\tv_mov_b32 %[o8], v72\n\t"
            "ds_write_b128 %[xw], v[80:83]\n\t"
            "ds_write_b128 %[xw], v[84:87] offset:1024\n\t"
            "ds_write_b64 %[xw2], v[88:89]\n\t"
            : [o0] "=&v"(o[0]), [o1] "=&v"(o[1]), [o2] "=&v"(o[2]), [o3] "=&v"(o[3]), [o4] "=&v"(o[4]), [o5] "=&v"(o[5]), [o6] "=&v"(o[6]), [o7] "=&v"(o[7]), [o8] "=&v"(o[8]),
              [kp] "+v"(kp), [vp] "+v"(vp), [cnt] "=&s"(cnt)
            : [qa] "v"(qa), [qb] "v"(qbv), [na] "v"(-bnda), [nb] "v"(-bndb), [bb] "v"(bndb), [msk] "s"(0x0000ffff0000ffffull), [xw] "v"(xw), [xw2] "v"(xw + 2048 - lane * 8), [one] "v"((r == 16 || r == 20) ? ONES : 0u)
            : "memory", "scc",
              "v32", "v33", "v34", "v35", "v36", "v37", "v38", "v39", "v40", "v41", "v42", "v43", "v44", "v45", "v46", "v47",
              "v48", "v49", "v50", "v51", "v52", "v53", "v54", "v55", "v56", "v57", "v58", "v59", "v60", "v61", "v62", "v63",
              "v64", "v65", "v66", "v67", "v68", "v69", "v70", "v71", "v72", "v73", "v74", "v75", "v76", "v77", "v78", "v79",
              "v80", "v81", "v82", "v83", "v84", "v85", "v86", "v87", "v88", "v89", "v90", "v91", "v92", "v93", "v94", "v95",
              "v96", "v97", "v98", "v99", "v100", "v101", "v102", "v103", "v104", "v105", "v106", "v107", "v108", "v109", "v110", "v111",
              "v112", "v113", "v114", "v115", "v116", "v117", "v118", "v119", "v120", "v121", "v122", "v123", "v124", "v125", "v126", "v127",
              "v128", "v129", "v130", "v131", "v132", "v133", "v134", "v135", "v136", "v137", "v138", "v139", "v140", "v141", "v142", "v143",
              "v144", "v145", "v146", "v147", "v148", "v149", "v150", "v151", "v152", "v153", "v154", "v155", "v156", "v157", "v158", "v159");
    } else {
        bar();
        const u32x4 ones = {ONES, ONES, ONES, ONES};
        const f32x16 zero = {};
#pragma unroll 1
        for (int sl = 1; sl >= 0; --sl) {
            const bf16x8 qf = __builtin_bit_cast(bf16x8, sl ? qbv : qa);
            bf16x8 v0 = __builtin_bit_cast(bf16x8, ones), v1 = v0, kf;
            f32x16 oo = {};
            float m = -INFINITY;
#pragma unroll 1
            for (int t = 0; t < 32; ++t) {
                asm volatile("ds_read_b128 %0, %3\n\ts_mov_b64 exec, %5\n\tds_read_b128 %1, %4\n\tds_read_b128 %2, %4 offset:512\n\ts_mov_b64 exec, -1\n\ts_waitcnt lgkmcnt(0)"
                             : "=&v"(kf), "+v"(v0), "+v"(v1) : "v"(ka + t * 1024), "v"(va + t * 1024), "s"(0x0000ffff0000ffffull) : "memory");
                const f32x16 s = MFMA(kf, qf, zero);
                float tm = s[0];
#pragma unroll
                for (int i = 1; i < 16; ++i) tm = fmaxf(tm, s[i]);
                tm = fmaxf(tm, other_half(tm));
                const float mn = fmaxf(m, tm);
                const float alpha = __builtin_amdgcn_exp2f(m - mn);
                m = mn;
#pragma unroll
                for (int i = 0; i < 16; ++i) oo[i] *= alpha;
                u32x4 p0, p1;
#pragma unroll
                for (int i = 0; i < 4; ++i) p0[i] = cvtpk(__builtin_amdgcn_exp2f(s[2 * i] - m), __builtin_amdgcn_exp2f(s[2 * i + 1] - m));
#pragma unroll
                for (int i = 0; i < 4; ++i) p1[i] = cvtpk(__builtin_amdgcn_exp2f(s[8 + 2 * i] - m), __builtin_amdgcn_exp2f(s[9 + 2 * i] - m));
                oo = MFMA(v0, __builtin_bit_cast(bf16x8, p0), oo);
                oo = MFMA(v1, __builtin_bit_cast(bf16x8, p1), oo);
            }
            if (sl == 1) {
                f32x4 t0 = {oo[0], oo[1], oo[2], oo[3]}, t1 = {oo[4], oo[5], oo[6], oo[7]};
                f32x2 t2 = {oo[8], m};
                *(LAS f32x4*)(uintptr_t)xw = t0; *(LAS f32x4*)(uintptr_t)(xw + 1024) = t1; *(LAS f32x2*)(uintptr_t)(xw + 2048 - lane * 8) = t2;
            } else {
#pragma unroll
                for (int i = 0; i < 9; ++i) o[i] = oo[i];
                ma = m;
            }
        }
    }
    ST(14);
    cbar();
    {
        const f32x4 t0 = *(LAS const f32x4*)(uintptr_t)xr, t1 = *(LAS const f32x4*)(uintptr_t)(xr + 1024);
        const f32x2 t2 = *(LAS const f32x2*)(uintptr_t)(xr + 2048 - lane * 8);
        const float mo = t2[1], mn = fmaxf(ma, mo);
        const float fa = __builtin_amdgcn_exp2f(ma - mn), fo = __builtin_amdgcn_exp2f(mo - mn);
        const float inv = __builtin_amdgcn_rcpf(o[8] * fa + t2[0] * fo);
        const float ia = fa * inv, io = fo * inv;
        const u32x4 ov = pack_swap(o[0] * ia + t0[0] * io, o[1] * ia + t0[1] * io, o[2] * ia + t0[2] * io, o[3] * ia + t0[3] * io,
                                   o[4] * ia + t1[0] * io, o[5] * ia + t1[1] * io, o[6] * ia + t1[2] * io, o[7] * ia + t1[3] * io);
        gst128(ws + WS_OF + ((size_t)(qb * 8 + la) * 32 + head) * 1024, lane * 16, ov);
    }
}

constexpr int P3_X = 153600;
template <int MODE>
DI void p35_compute(const Ptrs& P, char* smem, int tb, int cb, int w, int lane, u32x4& hkeep) {
    constexpr int A0 = MODE == 0 ? 65536 : 98304, XA = MODE == 0 ? 131072 : 147456;
    char* ws = P.ws;
    const int r = lane & 31, hh = lane >> 5;
    const unsigned lds0 = (unsigned)(uintptr_t)smem;
    const int ta = w & 1, cj = (w >> 1) & 1, kh = w >> 2;
    const int tile = tb * 2 + ta, token = tile * 32 + r, n0 = cb * 64 + cj * 32 + kh * 16;
    u32x2 res[2];
    const unsigned aro = lds0 + A0 + ta * 8192 + lane * 16 + kh * 4096;
    const unsigned wro = lds0 + hh * 1024 + ((lane & 15) >> 2) * 64 + ((lane >> 4) & 1) * 32 + (lane & 3) * 8 + cj * 256 + kh * 8192;
    f32x16 acc = {};
    bf16x8 A[4], Wf[4];
#define P35_LD(S, BA, BW, ks) { A[S] = lds_b128(aro + (BA) + (ks) * 1024); Wf[S] = lds_trfrag(wro + (BW) + (ks) * 2048, 512); }
    if constexpr (MODE == 0) {
        cbar();
        ST(7);
        P35_LD(0, 0, 0, 0); P35_LD(1, 0, 0, 1);
        unsigned BA = 0;
#pragma unroll 1
        for (int ch = 0; ch < 4; ++ch) {
            P35_LD(2, BA, BA, 2); SB(); acc = MFMA(Wf[0], A[0], acc); SB();
            P35_LD(3, BA, BA, 3); SB(); acc = MFMA(Wf[1], A[1], acc); SB();
            if (ch < 3) {
                cbar();
                BA += 16384;
                P35_LD(0, BA, BA, 0);
            }
            SB(); acc = MFMA(Wf[2], A[2], acc); SB();
            if (ch < 3) { P35_LD(1, BA, BA, 1); }
            SB(); acc = MFMA(Wf[3], A[3], acc); SB();
        }
    } else {
        bf16x8 WR[19];
        bar();
#pragma unroll
        for (int q = 0; q < 16; ++q) WR[q] = lds_trfrag(wro + (q >> 2) * 16384 + (q & 3) * 2048, 512);
#pragma unroll
        for (int j = 0; j < 3; ++j) WR[16 + j] = lds_trfrag(wro + 4 * 16384 - kh * 8192 + (kh * 3 + j) * 2048, 512);
        cbar();
        cbar();
        ST(8);
        P35_LD(0, 0, 0, 0); P35_LD(1, 0, 0, 1);
        unsigned BA = 0, BW = 0;
#define P5_ADV() { BA = BA == 2 * 16384 ? 0 : BA + 16384; }
#define P5_LDA(S, ks) { A[S] = lds_b128(aro + BA + (ks) * 1024); }
#pragma unroll 1
        for (int ch = 0; ch < 5; ++ch) {
            P35_LD(2, BA, BW, 2); SB(); acc = MFMA(Wf[0], A[0], acc); SB();
            P35_LD(3, BA, BW, 3); SB(); acc = MFMA(Wf[1], A[1], acc); SB();
            cbar();
            P5_ADV(); BW += 16384;
            P35_LD(0, BA, BW, 0); SB(); acc = MFMA(Wf[2], A[2], acc); SB();
            P35_LD(1, BA, BW, 1); SB(); acc = MFMA(Wf[3], A[3], acc); SB();
        }
        P35_LD(2, BA, BW, 2); SB(); acc = MFMA(Wf[0], A[0], acc); SB();
        P35_LD(3, BA, BW, 3); SB(); acc = MFMA(Wf[1], A[1], acc); SB();
        cbar();
        P5_ADV();
        P5_LDA(0, 0); SB(); acc = MFMA(Wf[2], A[2], acc); SB();
        P5_LDA(1, 1); SB(); acc = MFMA(Wf[3], A[3], acc); SB();
#pragma unroll
        for (int c = 0; c < 4; ++c) {
            P5_LDA(2, 2); SB(); acc = MFMA(WR[4 * c], A[0], acc); SB();
            P5_LDA(3, 3); SB(); acc = MFMA(WR[4 * c + 1], A[1], acc); SB();
            cbar();
            P5_ADV();
            if (c < 3) { P5_LDA(0, 0); }
            SB(); acc = MFMA(WR[4 * c + 2], A[2], acc); SB();
            if (c < 3) { P5_LDA(1, 1); }
            SB(); acc = MFMA(WR[4 * c + 3], A[3], acc); SB();
        }
#pragma unroll
        for (int j = 0; j < 3; ++j) acc = MFMA(WR[16 + j], lds_b128(aro + BA - kh * 4096 + (kh * 3 + j) * 1024), acc);
    }
    ST(MODE == 0 ? 9 : 0);
    {
        LAS f32x4* xw = (LAS f32x4*)(smem + XA + w * 2048 + lane * 16);
        f32x4 s0, s1;
#pragma unroll
        for (int e = 0; e < 4; ++e) { s0[e] = kh ? acc[e] : acc[8 + e]; s1[e] = kh ? acc[4 + e] : acc[12 + e]; }
        xw[0] = s0; xw[64] = s1;
    }
    cbar();
    float v[8];
    {
        LAS const f32x4* xr = (LAS const f32x4*)(smem + XA + (w ^ 4) * 2048 + lane * 16);
        const f32x4 s0 = xr[0], s1 = xr[64];
        if constexpr (MODE == 0) {
            const int row = ta * 32 + r;
#pragma unroll
            for (int g = 0; g < 2; ++g)
                res[g] = *(LAS const u32x2*)(smem + P3_X + row * 128 + (((cj * 4 + kh * 2 + g) ^ ((row >> 1) & 7)) << 4) + hh * 8);
        } else { res[0][0] = hkeep[0]; res[0][1] = hkeep[1]; res[1][0] = hkeep[2]; res[1][1] = hkeep[3]; }
#pragma unroll
        for (int e = 0; e < 4; ++e) { v[e] = (kh ? acc[8 + e] : acc[e]) + s0[e]; v[4 + e] = (kh ? acc[12 + e] : acc[4 + e]) + s1[e]; }
#pragma unroll
        for (int g = 0; g < 2; ++g) {
            v[4 * g] += bflo(res[g][0]); v[4 * g + 1] += bfhi(res[g][0]); v[4 * g + 2] += bflo(res[g][1]); v[4 * g + 3] += bfhi(res[g][1]);
        }
    }
    const unsigned p0 = cvtpk(v[0], v[1]), p1 = cvtpk(v[2], v[3]), p2 = cvtpk(v[4], v[5]), p3 = cvtpk(v[6], v[7]);
    const u32x4 o = swap4(p0, p1, p2, p3);
    if constexpr (MODE == 0) {
        hkeep[0] = p0; hkeep[1] = p1; hkeep[2] = p2; hkeep[3] = p3;
        gst128(ws + WS_HF + ((size_t)tile * 32 + (n0 >> 4)) * 1024, lane * 16, o);
        float s1 = 0.f, s2 = 0.f;
#pragma unroll
        for (int e = 0; e < 4; ++e) { s1 = dot2(o[e], ONES, s1); s2 = dot2(o[e], o[e], s2); }
        s1 = sum_halves(s1); s2 = sum_halves(s2);
        LAS f32x2* sl = (LAS f32x2*)(smem + XA + 8 * 2048);
        if (lane < 32) { f32x2 sv = {s1, s2}; sl[(ta * 32 + r) * 4 + cj * 2 + kh] = sv; }
        cbar();
        if (w == 0) {
            const f32x4 p = *(LAS const f32x4*)(sl + lane * 4), q = *(LAS const f32x4*)(sl + lane * 4 + 2);
            u32x2 sv = {__float_as_uint(p[0] + p[2] + q[0] + q[2]), __float_as_uint(p[1] + p[3] + q[1] + q[3])};
            gst64(ws + WS_ST + (size_t)tb * 64 * 64 + cb * 8, lane * 64, sv);
        }
    } else {
        *(u32x4*)(P.out + (size_t)token * 1024 + (n0 + 8 * hh) * 2) = o;
    }
}

constexpr int P4_ST = 147456, P4_CS = P4_ST + 8192;
template <int NB>
DI void p4_compute(char* ws, char* smem, int tb, int cg, int w, int lane) {
    const int r = lane & 31, hh = lane >> 5, i = w & 3, j0 = NB == 2 ? 0 : 2;
    const unsigned lds0 = (unsigned)(uintptr_t)smem;
    const int tile = tb * 4 + i;
    const unsigned hro = lds0 + 98304 + i * 4096 + lane * 16;
    const unsigned wro = lds0 + hh * 1536 + ((lane & 15) >> 2) * 64 + ((lane >> 4) & 1) * 32 + (lane & 3) * 8 + j0 * 256;
    f32x16 ag[NB], au[NB];
#pragma unroll
    for (int j = 0; j < NB; ++j) { ag[j] = f32x16{}; au[j] = f32x16{}; }
    bf16x8 H[4], G[4][NB], U[4][NB];
#define P4_LOAD(S, BH, BW, ks)                                                                                                    \
    {                                                                                                                             \
        H[S] = lds_b128(hro + (BH) + (ks) * 1024);                                                                                 \
        _Pragma("unroll") for (int j = 0; j < NB; ++j) { G[S][j] = lds_trfrag(wro + (BW) + (ks) * 3072 + j * 256, 768); U[S][j] = lds_trfrag(wro + (BW) + 12288 + (ks) * 3072 + j * 256, 768); } \
    }
#define P4_MMA(S) { _Pragma("unroll") for (int j = 0; j < NB; ++j) { ag[j] = MFMA(G[S][j], H[S], ag[j]); au[j] = MFMA(U[S][j], H[S], au[j]); } }
    cbar();
    ST(6);
    P4_LOAD(0, 0, 0, 0); P4_LOAD(1, 0, 0, 1);
    unsigned BH = 0, BW = 0;
#pragma unroll 1
    for (int ch = 0; ch < 8; ++ch) {
        P4_LOAD(2, BH, BW, 2); SB(); P4_MMA(0); SB();
        P4_LOAD(3, BH, BW, 3); SB(); P4_MMA(1); SB();
        cbar();
        if (ch < 7) {
            BH = BH == 2 * 16384 ? 0 : BH + 16384; BW = BW == 3 * 24576 ? 0 : BW + 24576;
            P4_LOAD(0, BH, BW, 0);
        }
        SB(); P4_MMA(2); SB();
        if (ch < 7) { P4_LOAD(1, BH, BW, 1); }
        SB(); P4_MMA(3); SB();
    }
    ST(15);
    float mean, rstd;
    {
        float s1 = 0.f, s2 = 0.f;
        const LAS f32x4* sp = (const LAS f32x4*)(smem + P4_ST + (i * 32 + r) * 64);
#pragma unroll
        for (int k = 0; k < 4; ++k) { const f32x4 v = sp[k]; s1 += v[0] + v[2]; s2 += v[1] + v[3]; }
        mean = s1 * (1.f / DM);
        rstd = __builtin_amdgcn_rsqf(fmaxf(s2 * (1.f / DM) - mean * mean, 0.f) + LN_EPS);
    }
    constexpr float C1 = -2.f * 0.7978845608028654f * LOG2E, C2 = C1 * 0.044715f;
    const float k1 = C1 * rstd, k2 = C2 * rstd * rstd * rstd, k3 = __builtin_amdgcn_rcpf(rstd * rstd);
    const f32x2 nm2 = {-mean, -mean}, k12 = {k1, k1}, k22 = {k2, k2}, k32 = {k3, k3};
#pragma unroll
    for (int j = 0; j < NB; ++j) {
        const int blk = cg * 3 + j0 + j;
        float v[16];
#pragma unroll
        for (int g = 0; g < 4; ++g) {
            const LAS f32x4* cp = (const LAS f32x4*)(smem + P4_CS + ((j0 + j) * 32 + 8 * g + 4 * hh) * 4);
            const f32x4 cgf = cp[0], cuf = cp[32];
#pragma unroll
            for (int e = 0; e < 4; e += 2) {
                const f32x2 ga = {ag[j][4 * g + e], ag[j][4 * g + e + 1]}, ua = {au[j][4 * g + e], au[j][4 * g + e + 1]};
                const f32x2 cg2 = {cgf[e], cgf[e + 1]}, cu2 = {cuf[e], cuf[e + 1]};
                const f32x2 a = __builtin_elementwise_fma(nm2, cg2, ga), bq = __builtin_elementwise_fma(nm2, cu2, ua);
                const f32x2 arg = a * __builtin_elementwise_fma(a * a, k22, k12);
                const f32x2 ex = {__builtin_amdgcn_exp2f(arg[0]), __builtin_amdgcn_exp2f(arg[1])};
                const f32x2 den = __builtin_elementwise_fma(ex, k32, k32);
                const f32x2 rc = {__builtin_amdgcn_rcpf(den[0]), __builtin_amdgcn_rcpf(den[1])};
                const f32x2 y = (a * rc) * bq;
                v[4 * g + e] = y[0]; v[4 * g + e + 1] = y[1];
            }
        }
        if (blk < 43) {
#pragma unroll
            for (int g = 0; g < 2; ++g) {
                const u32x4 o = pack_swap(v[8 * g], v[8 * g + 1], v[8 * g + 2], v[8 * g + 3], v[8 * g + 4], v[8 * g + 5], v[8 * g + 6], v[8 * g + 7]);
                gst128(ws + WS_AF + ((size_t)tile * 86 + blk * 2 + g) * 1024, lane * 16, o);
            }
        }
    }
}

__global__ __launch_bounds__(640, 3) void k_block(const char* __restrict__ x, const char* __restrict__ wqkv, const char* __restrict__ wo, const char* __restrict__ wg,
                                                  const char* __restrict__ wu, const char* __restrict__ wd, char* __restrict__ ws, char* __restrict__ out) {
    extern __shared__ __attribute__((aligned(16))) char smem[];
    const Ptrs P = {x, wqkv, wo, wg, wu, wd, ws, out};
    const int b = blockIdx.x, xcd = b & 7, idx = b >> 3;
    const int lane = threadIdx.x & 63, w = rfl(threadIdx.x >> 6);
    const unsigned lds0 = (unsigned)(uintptr_t)smem;
    if (w >= 8 && blockIdx.x < 64) {
        const char* pc = (const char*)(__builtin_amdgcn_s_getpc() & ~127ull);
        asm volatile("s_mov_b32 m0, %0\n\ts_nop 0" ::"s"(lds0 + LDS_BYTES - 256) : "memory");
#pragma unroll
        for (int i = 0; i < (CODE_LINES + 63) / 64; ++i) { const int ln = i * 64 + lane; gtouch(pc, (ln < CODE_LINES ? ln : CODE_LINES - 1) * 128); }
    }
    const bool p1_gemm = idx >= 8;
    const int gi = idx - 8;
    const int tb1 = (xcd >> 1) * 4 + (gi & 3), cb1 = (xcd & 1) * 6 + (gi >> 2);
    const int head = xcd * 4 + (idx >> 3), qb = idx & 7;
    const int tb3 = xcd * 4 + (idx & 3), cb3 = idx >> 2;
    const bool p4_on = !(xcd & 1) || idx < 28;
    const int tb4 = (xcd >> 1) * 4 + (idx & 3), cg4 = (xcd & 1) * 8 + (idx >> 2);
    ST(0);

    if (w < 8) {
        if (p1_gemm) {
            f32x16 accA, accB;
            if (w >= 4) p1_colsum(wqkv, ws, tb1, cb1, smem, w - 4, lane, accA, accB);
            else if (cb1 < 8) p1_compute<false>(ws, tb1, cb1, smem, w, lane, accA, accB); else p1_compute<true>(ws, tb1, cb1, smem, w, lane, accA, accB);
            const int tl = 2 * (w & 1) + (w >> 2), cp = (w >> 1) & 1;
            if (cb1 < 8) p1_epilogue<false>(ws, tb1, cb1, smem, tl, cp, accA, accB, lane); else p1_epilogue<true>(ws, tb1, cb1, smem, tl, cp, accA, accB, lane);
        } else p1_helper(x, wqkv, wg, wu, ws, idx * 8 + xcd, w, lane);
        ST(1);
        pbar();
        p2_compute(ws, smem, head, qb, w, lane);
        ST(2);
        pbar();
        u32x4 hkeep;
        p35_compute<0>(P, smem, tb3, cb3, w, lane, hkeep);
        ST(3);
        pbar();
        if (p4_on) {
            if (w < 4) p4_compute<2>(ws, smem, tb4, cg4, w, lane); else p4_compute<1>(ws, smem, tb4, cg4, w, lane);
        } else bar();
        ST(4);
        pbar();
        p35_compute<1>(P, smem, tb3, cb3, w, lane, hkeep);
        ST(10);
        return;
    }

    const int L = w - 8;
    if (L == 0) {
        unsigned o_ = (unsigned)WS_FLAG + (lane & 3) * 256 * 128 + b * 128;
        if (lane == 4) o_ = (unsigned)WS_G1 + b * 128;
        if (lane >= 5 && lane < 9) o_ = (unsigned)WS_ST + (b * 4 + lane - 5) * 128;
        if (lane == 9) o_ = (unsigned)WS_KM + (b & 63) * 128;
        if (lane == 10) o_ = (unsigned)WS_CS + (b < 86 ? b : 0) * 128;
        if (lane >= 11 && lane < 17) o_ = (unsigned)WS_G2 + (b % 64) * 768 + (lane - 11) * 128;
        asm volatile("s_mov_b32 m0, %0\n\ts_nop 0\n\tglobal_load_lds_dword %1, %2 sc1" ::"s"(lds0 + LDS_BYTES - 256), "v"(o_), "s"(ws) : "memory");
    }
    const char* flags = ws + WS_FLAG;
    const unsigned lin = lane * 16;
    const unsigned tag = p1_gemm ? p1_load(P, L, tb1, cb1, P1ROT, gi, lds0, lane) : rfl(gld32_wait(ws + WS_EPOCH, 0)) + 1;
    bar();
    if (L == 0 && lane == 0) gst32(ws + WS_FLAG, b * 128, tag);
    ST(1);
    {
        const int ptb = lane < 32 ? (lane & 15) : 2 * qb + (lane & 1), pcb = (lane < 16 ? 4 : lane < 32 ? 8 : 0) + (head >> 3);
        const int pb = ((pcb % 6) * 4 + (ptb & 3) + 8) * 8 + (ptb >> 2) * 2 + pcb / 6;
        poll(flags, pb * 128, lane < 34, tag);
        ST(6);
        const int rot = 0;
        const char* src = ws + (L == 0 ? WS_KF : WS_VF) + (size_t)head * 65536;
        const unsigned dst = lds0 + L * 65536;
        {
            const char* q = ws + WS_QF + ((size_t)head * 64 + qb * 8) * 1024 + L * 4096;
            dma4lin(q, lin, lds0 + P2_X + L * 4096);
            if (L == 0) dma1dw(ws + WS_KM + head * 256, lane * 4, lds0 + P2_KM);
        }
#pragma unroll 1
        for (int g = 0; g < 8; ++g) {
            const unsigned so = ((g * 4 + rot) & 31) * 1024;
            dma4lin(src + so, lin, dst + g * 4096); dma4lin(src + 32768 + so, lin, dst + 32768 + g * 4096);
            if (g == 1) lbar<0>();
        }
        ST(7);
        if (L == 1) {
            asm volatile("s_waitcnt vmcnt(0)" ::: "memory");
        }
        lbar<0>();
        if (L == 1) {
#pragma unroll 1
            for (int g = 0; g < 4; ++g) poll(flags, (g * 64 + lane) * 128, true, tag);
            if (b == 0 && lane == 0) gst32(ws + WS_EPOCH, 0, tag);
            asm volatile("s_mov_b32 m0, %0\n\ts_nop 0" ::"s"(lds0 + LDS_BYTES - 256) : "memory");
            gtouch(wo, (idx * 128 + lane) * 128); gtouch(wo, (idx * 128 + 64 + lane) * 128);
            {
                const int nl = (xcd & 1) ? 10 : 12, ln = lane & 15;
                const unsigned co = (xcd & 1) * 1536 + (ln < nl ? ln : 0) * 128;
#pragma unroll
                for (int i = 0; i < 4; ++i) {
                    unsigned o_ = (idx * 16 + i * 4 + (lane >> 4)) * (FF * 2) + co;
                    o_ = o_ > 512u * FF * 2 - 4 ? 512u * FF * 2 - 4 : o_;
                    gtouch(wg, o_); gtouch(wu, o_);
                }
            }
#pragma unroll
            for (int i = 0; i < 6; ++i) { int ln = i * 64 + lane; ln = ln > 343 ? 343 : ln; gtouch(wd, (idx * 344 + ln) * 128); }
        }
        bar();
    }
    unsigned wvo[4];
    {
        const unsigned wv = ((lane >> 5) * 4 + ((lane >> 2) & 3)) * 1024 + ((lane >> 4) & 1) * 64 + (lane & 3) * 16;
#pragma unroll
        for (int i = 0; i < 4; ++i) wvo[i] = wv + i * 8192 - i * 1024;
    }
    {
        const int rot = 0;
        bar();
        if (L == 1) {
            {
                const int rl = lane >> 3, cs = lane & 7;
                unsigned xv[4];
#pragma unroll
                for (int i = 0; i < 4; ++i) xv[i] = (8 * i + rl) * 1024 + ((cs ^ ((4 * (i & 1) + (rl >> 1)) & 7)) << 4) - i * 1024;
                const char* xs = x + (size_t)tb3 * 64 * 1024 + cb3 * 128;
                dma4(xs, xv[0], xv[1], xv[2], xv[3], lds0 + P3_X); dma4(xs + 32768, xv[0], xv[1], xv[2], xv[3], lds0 + P3_X + 4096);
            }
#pragma unroll 1
            for (int c = 0; c < 4; ++c) {
                const char* src = wo + cb3 * 128 + (size_t)((c + rot) & 3) * 128 * 1024;
#pragma unroll
                for (int g = 0; g < 4; ++g) dma4(src + g * 32768, wvo[0], wvo[1], wvo[2], wvo[3], lds0 + c * 16384 + g * 4096);
            }
        }
        if (L == 0) {
            if (lane == 0) gst32(ws + WS_FLAG + 256 * 128, b * 128, tag);
            ST(2);
            const int ph = lane & 31;
            poll(flags + 256 * 128, (((ph & 3) * 8 + (tb3 >> 2)) * 8 + (ph >> 2)) * 128, true, tag);
            ST(8);
            const char* ab = ws + WS_OF + (size_t)tb3 * 2 * 32 * 1024;
#pragma unroll 1
            for (int c = 0; c < 4; ++c) {
                const char* src = ab + ((c + rot) & 3) * 8192;
                const unsigned dst = lds0 + 65536 + c * 16384;
                dma4lin(src, lin, dst); dma4lin(src + 4096, lin, dst + 4096); dma4lin(src + 32768, lin, dst + 8192); dma4lin(src + 32768 + 4096, lin, dst + 12288);
                if (c == 1) lbar<16>();
            }
            lbar<32>(); lbar<16>(); lbar<0>();
        } else {
            lbar<0>(); bar(); bar(); bar();
        }
        bar();
    }
    {
        const int rot = 0;
        unsigned gvo[12];
        if (L == 1 && p4_on) {
#pragma unroll
            for (int j = 0; j < 12; ++j) {
                const int s = j * 64 + lane, st = s >> 4, kb = st / 3, nb = st % 3, q = (s >> 2) & 3, c = s & 3;
                int blk = cg4 * 3 + nb; blk = blk > 42 ? 42 : blk;
                gvo[j] = (4 * kb + q) * (FF * 2) + blk * 64 + c * 16 - (j & 3) * 1024;
            }
        }
        auto issue_w = [&](int c, unsigned B) {
            const size_t so = (size_t)((c + rot) & 7) * 64 * (FF * 2);
#pragma unroll
            for (int g = 0; g < 3; ++g) dma4(wg + so, gvo[4 * g], gvo[4 * g + 1], gvo[4 * g + 2], gvo[4 * g + 3], lds0 + B + g * 4096);
#pragma unroll
            for (int g = 0; g < 3; ++g) dma4(wu + so, gvo[4 * g], gvo[4 * g + 1], gvo[4 * g + 2], gvo[4 * g + 3], lds0 + B + 12288 + g * 4096);
        };
        bar();
        bar();
        if (L == 1 && p4_on) { issue_w(0, 0); issue_w(1, 24576); issue_w(2, 2 * 24576); issue_w(3, 3 * 24576); }
        if (L == 0) { if (lane == 0) gst32(ws + WS_FLAG + 2 * 256 * 128, b * 128, tag); ST(3); }
        if (p4_on) {
            if (L == 0) {
                const int ptb = tb4 * 2 + (lane & 1), pcb = (lane >> 1) & 7;
                poll(flags + 2 * 256 * 128, ((pcb * 4 + (ptb & 3)) * 8 + (ptb >> 2)) * 128, true, tag);
                ST(9);
                const char* hb = ws + WS_HF + (size_t)tb4 * 4 * 32 * 1024;
                auto issue_h = [&](int c, unsigned B) {
                    const char* src = hb + ((c + rot) & 7) * 4096;
#pragma unroll
                    for (int i = 0; i < 4; ++i) dma4lin(src + i * 32768, lin, lds0 + 98304 + B + i * 4096);
                };
                issue_h(0, 0); issue_h(1, 16384);
                lbar<16>();
                {
                    const char* sb = ws + WS_ST + (size_t)tb4 * 128 * 64;
                    dma4lin(sb, lin, lds0 + P4_ST); dma4lin(sb + 4096, lin, lds0 + P4_ST + 4096);
                    const char* cb_ = ws + WS_CS + cg4 * 384;
                    dma1dw(cb_, lane * 4, lds0 + P4_CS); dma1dw(cb_ + 256, lane * 4, lds0 + P4_CS + 256);
                    dma1dw(cb_ + FF * 4, lane * 4, lds0 + P4_CS + 512); dma1dw(cb_ + FF * 4 + 256, lane * 4, lds0 + P4_CS + 768);
                }
                issue_h(2, 2 * 16384);
                unsigned B = 0;
#pragma unroll 1
                for (int c = 1; c < 8; ++c) {
                    if (c < 7) lbar<16>(); else lbar<0>();
                    if (c + 2 < 8) { issue_h(c + 2, B); B = B == 2 * 16384 ? 0 : B + 16384; }
                }
            } else {
                unsigned B = 0;
#pragma unroll 1
                for (int c = 0; c < 8; ++c) {
                    if (c == 0) lbar<63>(); else if (c < 6) lbar<48>(); else if (c == 6) lbar<24>(); else lbar<0>();
                    if (c >= 1 && c + 3 < 8) { issue_w(c + 3, B); B = B == 3 * 24576 ? 0 : B + 24576; }
                }
            }
            bar();
        } else bar();
    }
    {
        const int rot = 0;
        auto issue_w = [&](int c, unsigned B) {
            const int cc = c < 10 ? (c + rot) % 10 : 10;
            const char* src = wd + cb3 * 128 + (size_t)cc * 128 * 1024;
#pragma unroll
            for (int g = 0; g < 3; ++g) dma4(src + g * 32768, wvo[0], wvo[1], wvo[2], wvo[3], lds0 + B + g * 4096);
            dma4(src + (c < 10 ? 3 : 2) * 32768, wvo[0], wvo[1], wvo[2], wvo[3], lds0 + B + 3 * 4096);
        };
        if (L == 1) {
#pragma unroll 1
            for (int c = 6; c < 11; ++c) issue_w(c, (c - 6) * 16384);
        }
        bar();
        if (L == 0 && p4_on && lane == 0) gst32(ws + WS_FLAG + 3 * 256 * 128, b * 128, tag);
        if (L == 1) lbar<0>(); else bar();
        bar();
        if (L == 1) {
#pragma unroll 1
            for (int c = 0; c < 6; ++c) issue_w(c, c * 16384);
        }
        if (L == 0) {
            ST(4);
            const int ptb = tb3 >> 1, pcg = lane < 15 ? lane : 0;
            poll(flags + 3 * 256 * 128, (((pcg & 7) * 4 + (ptb & 3)) * 8 + (ptb >> 2) * 2 + (pcg >> 3)) * 128, true, tag);
            ST(5);
            const char* ab = ws + WS_AF + (size_t)tb3 * 2 * 86 * 1024;
            auto issue_a = [&](int c, unsigned B) {
                const int cc = c < 10 ? (c + rot) % 10 : 10;
                const char* src = ab + cc * 8192;
                const unsigned dst = lds0 + 98304 + B;
                const int o2 = c < 10 ? 4096 : 2048;
                dma4lin(src, lin, dst); dma4lin(src + o2, lin, dst + o2);
                dma4lin(src + 86 * 1024, lin, dst + 8192); dma4lin(src + 86 * 1024 + o2, lin, dst + 8192 + o2);
            };
            issue_a(0, 0); issue_a(1, 16384);
            unsigned B = 2 * 16384;
#pragma unroll 1
            for (int c = 0; c < 11; ++c) {
                if (c < 10) lbar<16>(); else lbar<0>();
                if (c + 2 < 11) { issue_a(c + 2, B); B = B == 2 * 16384 ? 0 : B + 16384; }
            }
        } else {
            lbar<63>(); lbar<63>(); lbar<48>(); lbar<32>(); lbar<16>(); lbar<0>();
            bar(); bar(); bar(); bar(); bar();
        }
        bar();
    }
}

extern "C" void kernel_launch(void* const* d_in, const int* in_sizes, int n_in, void* d_out, int out_size, void* d_ws, size_t ws_size, hipStream_t stream) {
    (void)hipFuncSetAttribute((const void*)k_block, hipFuncAttributeMaxDynamicSharedMemorySize, LDS_BYTES);
    k_block<<<256, 640, LDS_BYTES, stream>>>((const char*)d_in[0], (const char*)d_in[1], (const char*)d_in[2], (const char*)d_in[4], (const char*)d_in[5], (const char*)d_in[6],
                                             (char*)d_ws, (char*)d_out);
}
```

```cpp
#include <hip/hip_runtime.h>
#include <hip/hip_bf16.h>
#include <stdint.h>

typedef __bf16 bf16x8 __attribute__((ext_vector_type(8)));
typedef __bf16 bf16x2 __attribute__((ext_vector_type(2)));
typedef short s16x4 __attribute__((ext_vector_type(4)));
typedef short s16x8 __attribute__((ext_vector_type(8)));
typedef float f32x16 __attribute__((ext_vector_type(16)));
typedef float f32x4 __attribute__((ext_vector_type(4)));
typedef float f32x2 __attribute__((ext_vector_type(2)));
typedef unsigned u32x4 __attribute__((ext_vector_type(4)));
typedef unsigned u32x2 __attribute__((ext_vector_type(2)));
#define LAS __attribute__((address_space(3)))
#define DI __device__ __forceinline__

constexpr int DM = 512, FF = 1376;
constexpr float LOG2E = 1.4426950408889634f;
constexpr float QSCALE = 0.25f * LOG2E;
constexpr float LN_EPS = 1e-5f;

constexpr size_t WS_QF = 0, WS_KF = 2u << 20, WS_VF = 4u << 20, WS_OF = 6u << 20, WS_HF = 8u << 20, WS_AF = 10u << 20,
                 WS_ST = 16u << 20, WS_CS = 17u << 20, WS_KM = (17u << 20) + 65536, WS_FLAG = 18u << 20, WS_EPOCH = WS_FLAG + 4 * 256 * 128,
                 WS_G1 = 19u << 20, WS_G2 = WS_G1 + 65536;
constexpr int LDS_BYTES = 163840;
constexpr unsigned SPIN_CAP = 1u << 17;
#define CODE_LINES_ 288
constexpr int CODE_LINES = CODE_LINES_;

#define MFMA(a, b, c) __builtin_amdgcn_mfma_f32_32x32x16_bf16(a, b, c, 0, 0, 0)
#define SB() __builtin_amdgcn_sched_barrier(0)
DI void cbar() { asm volatile("s_waitcnt lgkmcnt(0)\n\ts_barrier" ::: "memory"); }
DI void pbar() { asm volatile("s_waitcnt vmcnt(0) lgkmcnt(0)\n\ts_barrier" ::: "memory"); }
template <int N> DI void lbar() { asm volatile("s_waitcnt vmcnt(%0)\n\ts_barrier" ::"i"(N > 63 ? 63 : N) : "memory"); }
DI void bar() { asm volatile("s_barrier" ::: "memory"); }

DI void dma4(const void* sbase, unsigned v0, unsigned v1, unsigned v2, unsigned v3, unsigned dst) {
    asm volatile("s_mov_b32 m0, %5\n\ts_nop 0\n\tglobal_load_lds_dwordx4 %0, %4 sc1\n\tglobal_load_lds_dwordx4 %1, %4 offset:1024 sc1\n\t"
                 "global_load_lds_dwordx4 %2, %4 offset:2048 sc1\n\tglobal_load_lds_dwordx4 %3, %4 offset:3072 sc1"
                 :: "v"(v0), "v"(v1), "v"(v2), "v"(v3), "s"(sbase), "s"(dst) : "memory");
}
DI void dma4lin(const void* sbase, unsigned v, unsigned dst) { dma4(sbase, v, v, v, v, dst); }
DI void dma2lin(const void* sbase, unsigned v, unsigned dst) {
    asm volatile("s_mov_b32 m0, %2\n\ts_nop 0\n\tglobal_load_lds_dwordx4 %0, %1 sc1\n\tglobal_load_lds_dwordx4 %0, %1 offset:1024 sc1" :: "v"(v), "s"(sbase), "s"(dst) : "memory");
}
DI void dma1dw(const void* sbase, unsigned v, unsigned dst) {
    asm volatile("s_mov_b32 m0, %2\n\ts_nop 0\n\tglobal_load_lds_dword %0, %1 sc1" :: "v"(v), "s"(sbase), "s"(dst) : "memory");
}
DI void gtouch(const void* sbase, unsigned voff) {
    asm volatile("global_load_lds_dword %0, %1" :: "v"(voff), "s"(sbase) : "memory");
}
DI unsigned gld32_wait(const void* sbase, unsigned voff) {
    unsigned r; asm volatile("global_load_dword %0, %1, %2 sc1\n\ts_waitcnt vmcnt(0)" : "=&v"(r) : "v"(voff), "s"(sbase) : "memory"); return r;
}
DI void gst128(void* sbase, unsigned voff, u32x4 v) { asm volatile("global_store_dwordx4 %0, %1, %2 sc1\n\ts_nop 1" ::"v"(voff), "v"(v), "s"(sbase) : "memory"); }
DI void gst64(void* sbase, unsigned voff, u32x2 v) { asm volatile("global_store_dwordx2 %0, %1, %2 sc1\n\ts_nop 1" ::"v"(voff), "v"(v), "s"(sbase) : "memory"); }
DI void gst32(void* sbase, unsigned voff, unsigned v) { asm volatile("global_store_dword %0, %1, %2 sc1\n\ts_nop 1" ::"v"(voff), "v"(v), "s"(sbase) : "memory"); }
#define VWAIT_(N) asm volatile("s_waitcnt vmcnt(" #N ")" ::: "memory")
#define VWAIT(N) VWAIT_(N)
#define PIN(x) asm volatile("" : "+v"(x))
DI unsigned rfl(unsigned x) { return __builtin_amdgcn_readfirstlane(x); }
DI unsigned cvtpk(float lo, float hi) {
    f32x2 v = {lo, hi};
    bf16x2 b = __builtin_convertvector(v, bf16x2);
    return __builtin_bit_cast(unsigned, b);
}
DI float bflo(unsigned u) { return __uint_as_float(u << 16); }
DI float bfhi(unsigned u) { return __uint_as_float(u & 0xffff0000u); }
DI bf16x8 lds_b128(unsigned a) { return *(LAS const bf16x8*)(uintptr_t)a; }
typedef short v4i16_t __attribute__((ext_vector_type(4)));
DI s16x4 lds_tr(unsigned a) { return __builtin_bit_cast(s16x4, __builtin_amdgcn_ds_read_tr16_b64_v4i16((LAS v4i16_t*)(uintptr_t)a)); }
DI bf16x8 lds_trfrag(unsigned a, int off2) {
    s16x4 x = lds_tr(a), y = lds_tr(a + off2);
    s16x8 r = __builtin_shufflevector(x, y, 0, 1, 2, 3, 4, 5, 6, 7);
    return __builtin_bit_cast(bf16x8, r);
}
DI float dot2(unsigned a, unsigned b, float c) {
    return __builtin_amdgcn_fdot2_f32_bf16(__builtin_bit_cast(bf16x2, a), __builtin_bit_cast(bf16x2, b), c, false);
}
DI float other_half(float v) {
    auto r = __builtin_amdgcn_permlane32_swap(__float_as_uint(v), __float_as_uint(v), false, false);
    return __uint_as_float((threadIdx.x & 32) ? r[0] : r[1]);
}
DI float sum_halves(float v) {
    auto r = __builtin_amdgcn_permlane32_swap(__float_as_uint(v), __float_as_uint(v), false, false);
    return __uint_as_float(r[0]) + __uint_as_float(r[1]);
}
template <bool ALL> DI float dpp_max(float v) {
#define DPPMAX(ctrl, rm) v = fmaxf(v, __int_as_float(__builtin_amdgcn_update_dpp(0, __float_as_int(v), ctrl, rm, 0xf, true)))
    DPPMAX(0x111, 0xf); DPPMAX(0x112, 0xf); DPPMAX(0x114, 0xf); DPPMAX(0x118, 0xf); DPPMAX(0x142, 0xa);
    if (ALL) DPPMAX(0x143, 0xc);
    return v;
}
template <int CTRL> DI float dpp_add(float v) { return v + __int_as_float(__builtin_amdgcn_update_dpp(0, __float_as_int(v), CTRL, 0xf, 0xf, true)); }
DI u32x4 swap4(unsigned p0, unsigned p1, unsigned p2, unsigned p3) {
    auto s0 = __builtin_amdgcn_permlane32_swap(p0, p2, false, false);
    auto s1 = __builtin_amdgcn_permlane32_swap(p1, p3, false, false);
    u32x4 r = {s0[0], s1[0], s0[1], s1[1]};
    return r;
}
DI u32x4 pack_swap(float a0, float a1, float a2, float a3, float a4, float a5, float a6, float a7) {
    return swap4(cvtpk(a0, a1), cvtpk(a2, a3), cvtpk(a4, a5), cvtpk(a6, a7));
}
constexpr unsigned ONES = 0x3f803f80u;

#define ST(k)
#define ST2(k)

DI void poll(const char* fl, unsigned voff, bool active, unsigned tag) {
    for (unsigned spins = 0; spins < SPIN_CAP; ++spins) {
        unsigned f;
        asm volatile("global_load_dword %0, %1, %2 sc1\n\ts_waitcnt vmcnt(0)" : "=&v"(f) : "v"(voff), "s"(fl) : "memory");
        if (__all(!active || f == tag)) break;
        __builtin_amdgcn_s_sleep(3);
    }
}

struct Ptrs { const char *x, *wqkv, *wo, *wg, *wu, *wd; char* ws; char* out; };

#define NOTOUCH1
#define P1ROT 0
#define T1W 16
#define P1NB 4
constexpr int P1_BUF = 32768, P1_MISC = P1NB * P1_BUF;

DI void gst_granule(char* sbase, unsigned voff, float v, unsigned tag) { u32x2 g = {__float_as_uint(v), tag}; gst64(sbase, voff, g); }
DI float sum_x16(float v) {
    auto r = __builtin_amdgcn_permlane16_swap(__float_as_uint(v), __float_as_uint(v), false, false);
    return __uint_as_float(r[0]) + __uint_as_float(r[1]);
}
template <bool ISV>
DI void p1_epilogue(char* ws, int tb, int cb, char* smem, int tl, int cp, const f32x16& accA, const f32x16& accB, int lane) {
    const int r = lane & 31, hh = lane >> 5;
    const LAS f32x2* st = (const LAS f32x2*)(smem + P1_MISC);
    const LAS float* csl = (const LAS float*)(smem + P1_MISC + 1024);
    const int sec = cb >> 2, tile = tb * 4 + tl;
#pragma unroll
    for (int b = 0; b < 2; ++b) {
        const f32x16& acc = b ? accB : accA;
        const int ct = 2 * cp + b, head0 = (cb & 3) * 8 + ct * 2;
        float v[16];
        if constexpr (!ISV) {
            const f32x2 ms = st[tl * 32 + r];
            const float sc = ms[1] * (sec == 0 ? QSCALE : 1.f), msc = -ms[0] * sc;
#pragma unroll
            for (int g = 0; g < 4; ++g) {
                const f32x4 c4 = *(LAS const f32x4*)(csl + ct * 32 + 8 * g + 4 * hh);
#pragma unroll
                for (int e = 0; e < 4; ++e) v[4 * g + e] = acc[4 * g + e] * sc + msc * c4[e];
            }
            char* dst = ws + (sec == 0 ? WS_QF : WS_KF) + ((size_t)head0 * 64 + tile) * 1024;
#pragma unroll
            for (int hs = 0; hs < 2; ++hs) {
                const u32x4 o = pack_swap(v[8 * hs], v[8 * hs + 1], v[8 * hs + 2], v[8 * hs + 3], v[8 * hs + 4], v[8 * hs + 5], v[8 * hs + 6], v[8 * hs + 7]);
                gst128(dst + hs * 65536, lane * 16, o);
                if (sec == 1) {
                    float n2 = 0.f;
#pragma unroll
                    for (int e = 0; e < 8; ++e) n2 = fmaf(v[8 * hs + e], v[8 * hs + e], n2);
                    n2 = dpp_max<false>(sum_halves(n2));
                    if (lane == 31) gst32(ws + WS_KM + (head0 + hs) * 256 + tile * 4, 0, __float_as_uint(n2));
                }
            }
        } else {
            const float csn = csl[ct * 32 + r];
#pragma unroll
            for (int g = 0; g < 4; ++g) {
                const LAS f32x4* sp = (const LAS f32x4*)(st + tl * 32 + 8 * g + 4 * hh);
                const f32x4 pa = sp[0], pb = sp[1];
                v[4 * g] = (acc[4 * g] - pa[0] * csn) * pa[1]; v[4 * g + 1] = (acc[4 * g + 1] - pa[2] * csn) * pa[3];
                v[4 * g + 2] = (acc[4 * g + 2] - pb[0] * csn) * pb[1]; v[4 * g + 3] = (acc[4 * g + 3] - pb[2] * csn) * pb[3];
            }
            char* dst = ws + WS_VF + ((size_t)head0 * 64 + tile) * 1024;
            const unsigned vo = (r >> 4) * 65536 + hh * 256 + (r & 15) * 16;
#pragma unroll
            for (int s = 0; s < 2; ++s) {
                u32x4 o = {cvtpk(v[8 * s], v[8 * s + 1]), cvtpk(v[8 * s + 2], v[8 * s + 3]), cvtpk(v[8 * s + 4], v[8 * s + 5]), cvtpk(v[8 * s + 6], v[8 * s + 7])};
                gst128(dst + s * 512, vo, o);
            }
        }
    }
}
constexpr int P1_XCH = (5 % P1NB) * P1_BUF;

DI void p1_colsum(const char* wqkv, char* ws, int tb, int cb, char* smem, int j, int lane, f32x16& accA, f32x16& accB) {
    const unsigned tag = __hip_atomic_load((const __attribute__((address_space(1))) unsigned*)(ws + WS_EPOCH), __ATOMIC_RELAXED, __HIP_MEMORY_SCOPE_AGENT) + 1;
    const char* src = wqkv + (size_t)((tb & 3) * 128 + j * 32 + (lane >> 4)) * 3072 + cb * 256 + (lane & 15) * 16;
    u32x4 v[8];
#pragma unroll
    for (int i = 0; i < 8; ++i) v[i] = *(const u32x4*)(src + (size_t)i * 4 * 3072);
    cbar(); cbar();
    float s[8] = {};
#pragma unroll
    for (int i = 0; i < 8; ++i)
#pragma unroll
        for (int e = 0; e < 4; ++e) { s[2 * e] += bflo(v[i][e]); s[2 * e + 1] += bfhi(v[i][e]); }
#pragma unroll
    for (int e = 0; e < 8; ++e) s[e] = sum_halves(sum_x16(s[e]));
    LAS float* part = (LAS float*)(smem + P1_MISC + 2048);
    if (lane < 16) {
        f32x4 a = {s[0], s[1], s[2], s[3]}, c = {s[4], s[5], s[6], s[7]};
        *(LAS f32x4*)(part + j * 128 + lane * 8) = a; *(LAS f32x4*)(part + j * 128 + lane * 8 + 4) = c;
    }
    cbar();
    if (j == 0) {
#pragma unroll
        for (int k = 0; k < 2; ++k) {
            const int c = lane + 64 * k;
            gst_granule(ws + WS_G2, ((cb * 128 + c) * 4 + (tb & 3)) * 8, part[c] + part[128 + c] + part[256 + c] + part[384 + c], tag);
        }
    }
    cbar(); cbar(); cbar(); cbar(); cbar();
    cbar();
    f32x16 acc[2];
    {
        const LAS f32x4* xr = (const LAS f32x4*)(smem + P1_XCH + j * 8192 + lane * 16);
#pragma unroll
        for (int b = 0; b < 2; ++b)
#pragma unroll
            for (int g = 0; g < 4; ++g) { const f32x4 t = xr[(b * 4 + g) * 64]; acc[b][4 * g] = t[0]; acc[b][4 * g + 1] = t[1]; acc[b][4 * g + 2] = t[2]; acc[b][4 * g + 3] = t[3]; }
    }
    accA = acc[0]; accB = acc[1];
}

template <bool ISV>
DI void p1_compute(char* ws, int tb, int cb, char* smem, int w, int lane, f32x16& accA, f32x16& accB) {
    const int r = lane & 31, hh = lane >> 5;
    const unsigned lds0 = (unsigned)(uintptr_t)smem;
    const int tp = w & 1, cp = w >> 1;
    unsigned x0[4];
    {
        const int f = (r >> 1) & 7;
#pragma unroll
        for (int ks = 0; ks < 4; ++ks) x0[ks] = lds0 + (64 * tp + r) * 128 + (((2 * ks + hh) ^ f) << 4);
    }
    const unsigned wro = lds0 + 16384 + hh * 2048 + ((lane & 15) >> 2) * 64 + ((lane >> 4) & 1) * 32 + (lane & 3) * 8 + cp * 512;
    f32x16 acc[2][2] = {};
    bf16x8 X[4][2], W[4][2];
#define P1_LOAD(S, B, ks) { X[S][0] = lds_b128(x0[ks] + (B)); X[S][1] = lds_b128(x0[ks] + (B) + 4096); W[S][0] = lds_trfrag(wro + (B) + (ks) * 4096, 1024); W[S][1] = lds_trfrag(wro + (B) + (ks) * 4096 + 256, 1024); }
#define P1_MMA(S)                                                                                                                 \
    {                                                                                                                             \
        _Pragma("unroll") for (int a = 0; a < 2; ++a) _Pragma("unroll") for (int b = 0; b < 2; ++b)                                \
            acc[a][b] = ISV ? MFMA(X[S][a], W[S][b], acc[a][b]) : MFMA(W[S][b], X[S][a], acc[a][b]);                               \
    }
#define P1_ILV() { __builtin_amdgcn_sched_group_barrier(0x8, 1, 0); __builtin_amdgcn_sched_group_barrier(0x100, 2, 0); __builtin_amdgcn_sched_group_barrier(0x8, 1, 0); __builtin_amdgcn_sched_group_barrier(0x100, 2, 0); \
                   __builtin_amdgcn_sched_group_barrier(0x8, 1, 0); __builtin_amdgcn_sched_group_barrier(0x100, 2, 0); __builtin_amdgcn_sched_group_barrier(0x8, 1, 0); SB(); }
    cbar();
    ST(11);
    P1_LOAD(0, 0, 0); P1_LOAD(1, 0, 1);
    SB();
    unsigned B = 0;
#pragma unroll 1
    for (int ch = 0; ch < 7; ++ch) {
        P1_LOAD(2, B, 2); P1_MMA(0); P1_ILV();
        P1_LOAD(3, B, 3); P1_MMA(1); P1_ILV();
        cbar();
        B = B == (P1NB - 1) * P1_BUF ? 0 : B + P1_BUF;
        P1_LOAD(0, B, 0); P1_MMA(2); P1_ILV();
        P1_LOAD(1, B, 1); P1_MMA(3); P1_ILV();
    }
    P1_LOAD(2, B, 2); P1_MMA(0); P1_ILV();
    P1_LOAD(3, B, 3); P1_MMA(1); P1_ILV();
    P1_MMA(2); P1_MMA(3);
    ST(12);
    {
        LAS f32x4* xw = (LAS f32x4*)(smem + P1_XCH + w * 8192 + lane * 16);
#pragma unroll
        for (int b = 0; b < 2; ++b)
#pragma unroll
            for (int g = 0; g < 4; ++g) { f32x4 t = {acc[1][b][4 * g], acc[1][b][4 * g + 1], acc[1][b][4 * g + 2], acc[1][b][4 * g + 3]}; xw[(b * 4 + g) * 64] = t; }
    }
    cbar();
    accA = acc[0][0]; accB = acc[0][1];
}

typedef const __attribute__((address_space(1))) char* gcptr;
template <int NROW16>
DI void colsum32(gcptr W, unsigned rs, int lane, float (&s)[8]) {
    const unsigned vo = (lane & 15) * rs + (lane >> 4) * 16;
    u32x4 v[NROW16];
#pragma unroll
    for (int i = 0; i < NROW16; ++i) v[i] = *(const __attribute__((address_space(1))) u32x4*)(W + (size_t)i * 16 * rs + vo);
    __builtin_amdgcn_sched_barrier(0);
#pragma unroll
    for (int e = 0; e < 8; ++e) s[e] = 0.f;
#pragma unroll
    for (int i = 0; i < NROW16; ++i)
#pragma unroll
        for (int e = 0; e < 4; ++e) { s[2 * e] += bflo(v[i][e]); s[2 * e + 1] += bfhi(v[i][e]); }
#pragma unroll
    for (int e = 0; e < 8; ++e) s[e] = dpp_add<0x118>(dpp_add<0x114>(dpp_add<0x112>(dpp_add<0x111>(s[e]))));
}
DI void p1_helper(const char* px, const char* pqkv, const char* pg, const char* pu, char* ws, int h, int w, int lane) {
    if (w < 4) {
        const unsigned tag = __hip_atomic_load((const __attribute__((address_space(1))) unsigned*)(ws + WS_EPOCH), __ATOMIC_RELAXED, __HIP_MEMORY_SCOPE_AGENT) + 1;
        const int u = w * 64 + h;
        if (u < 256) {
            const int rl = lane >> 3, seg = lane & 7;
            const char* src = px + (size_t)(u * 8 + rl) * 1024 + seg * 16;
            u32x4 v[8];
#pragma unroll
            for (int i = 0; i < 8; ++i) v[i] = *(const u32x4*)(src + i * 128);
            float s1 = 0.f, s2 = 0.f;
#pragma unroll
            for (int i = 0; i < 8; ++i)
#pragma unroll
                for (int e = 0; e < 4; ++e) { s1 = dot2(v[i][e], ONES, s1); s2 = dot2(v[i][e], v[i][e], s2); }
            s1 = dpp_add<0x114>(dpp_add<0x112>(dpp_add<0x111>(s1))); s2 = dpp_add<0x114>(dpp_add<0x112>(dpp_add<0x111>(s2)));
            const float mean = s1 * (1.f / DM);
            const float rstd = __builtin_amdgcn_rsqf(fmaxf(s2 * (1.f / DM) - mean * mean, 0.f) + LN_EPS);
            if (seg == 7) { gst_granule(ws + WS_G1, (u * 8 + rl) * 16, mean, tag); gst_granule(ws + WS_G1, (u * 8 + rl) * 16 + 8, rstd, tag); }
        }
    }
    const int job = w == 4 ? h : (w == 5 && h < 22) ? 64 + h : -1;
    if (job >= 0) {
        const int blk = job < 43 ? job : job - 43;
        const uintptr_t wa = (uintptr_t)pg + (job < 43 ? 0 : (uintptr_t)pu - (uintptr_t)pg);
        float s[8];
        colsum32<32>((gcptr)wa + blk * 64, FF * 2, lane, s);
        if ((lane & 15) == 15) {
            char* dst = ws + WS_CS + (job < 43 ? 0 : FF) * 4;
            const int c0 = blk * 32 + (lane >> 4) * 8;
            u32x4 a = {__float_as_uint(s[0]), __float_as_uint(s[1]), __float_as_uint(s[2]), __float_as_uint(s[3])};
            u32x4 c = {__float_as_uint(s[4]), __float_as_uint(s[5]), __float_as_uint(s[6]), __float_as_uint(s[7])};
            gst128(dst, c0 * 4, a); gst128(dst, c0 * 4 + 16, c);
        }
    }
}

DI void small_touch(char* ws, int b, int lane, unsigned lds0) {
    unsigned o_ = (unsigned)WS_FLAG + (lane & 3) * 256 * 128 + b * 128;
    if (lane == 4) o_ = (unsigned)WS_G1 + b * 128;
    if (lane >= 5 && lane < 9) o_ = (unsigned)WS_ST + (b * 4 + lane - 5) * 128;
    if (lane == 9) o_ = (unsigned)WS_KM + (b & 63) * 128;
    if (lane == 10) o_ = (unsigned)WS_CS + (b < 86 ? b : 0) * 128;
    if (lane >= 11 && lane < 17) o_ = (unsigned)WS_G2 + (b % 64) * 768 + (lane - 11) * 128;
    asm volatile("s_mov_b32 m0, %0\n\ts_nop 0\n\tglobal_load_lds_dword %1, %2 sc1" ::"s"(lds0 + LDS_BYTES - 256), "v"(o_), "s"(ws) : "memory");
}

DI unsigned p1_load(const Ptrs& P, int L, int tb, int cb, int rot, int sidx, unsigned lds0, int lane) {
    unsigned vo[4];
    const char* base;
    unsigned cstep, gstep;
    if (L == 0) {
        const int rl = lane >> 3, cs = lane & 7;
#pragma unroll
        for (int i = 0; i < 4; ++i) vo[i] = (8 * i + rl) * 1024 + ((cs ^ ((4 * (i & 1) + (rl >> 1)) & 7)) << 4) - i * 1024;
        base = P.x + (size_t)tb * 128 * 1024; cstep = 128; gstep = 4 * 8192;
    } else {
        const int nb = lane >> 4, q = (lane >> 2) & 3, c = lane & 3;
#pragma unroll
        for (int i = 0; i < 4; ++i) vo[i] = (4 * i + q) * 3072 + nb * 64 + c * 16 - i * 1024;
        base = P.wqkv + cb * 256; cstep = 64 * 3072; gstep = 16 * 3072;
    }
    auto issue = [&](int c, unsigned B) {
        const char* src = base + (size_t)((c + rot) & 7) * cstep;
        const unsigned dst = lds0 + B + L * 16384;
#pragma unroll
        for (int g = 0; g < 4; ++g) dma4(src + (size_t)g * gstep, vo[0], vo[1], vo[2], vo[3], dst + g * 4096);
    };
    issue(0, 0);
#pragma unroll
    for (int c = 1; c < P1NB - 1; ++c) issue(c, c * P1_BUF);
    if (L == 0) small_touch(P.ws, blockIdx.x, lane, lds0);
    unsigned B = (P1NB - 1) * P1_BUF;
    constexpr int LA = P1NB - 1;
#pragma unroll 1
    for (int c = 0; c < 8; ++c) {
        const int y = 7 - c < LA - 1 ? 7 - c : LA - 1;
        if (c == 0) lbar<16 * (LA - 1) + T1W - 16>(); else if (y == 3) lbar<48>(); else if (y == 2) lbar<32>(); else if (y == 1) lbar<16>(); else lbar<0>();
        if (c + LA < 8) { issue(c + LA, B); B = B == (P1NB - 1) * P1_BUF ? 0 : B + P1_BUF; }
    }
    const unsigned tag = rfl(gld32_wait(P.ws + WS_EPOCH, 0)) + 1;
    if (L == 1) {
        u32x4 a, b, c0, c1, c2, c3;
        for (unsigned spins = 0; spins < SPIN_CAP; ++spins) {
            asm volatile("global_load_dwordx4 %0, %6, %7 sc1\n\tglobal_load_dwordx4 %1, %6, %7 offset:16 sc1\n\t"
                         "global_load_dwordx4 %2, %8, %9 sc1\n\tglobal_load_dwordx4 %3, %8, %9 offset:16 sc1\n\t"
                         "global_load_dwordx4 %4, %8, %9 offset:32 sc1\n\tglobal_load_dwordx4 %5, %8, %9 offset:48 sc1\n\ts_waitcnt vmcnt(0)"
                         : "=&v"(a), "=&v"(b), "=&v"(c0), "=&v"(c1), "=&v"(c2), "=&v"(c3)
                         : "v"(lane * 32), "s"(P.ws + WS_G1 + tb * 128 * 16), "v"(lane * 64), "s"(P.ws + WS_G2 + cb * 128 * 32) : "memory");
            if (__all(a[1] == tag && a[3] == tag && b[1] == tag && b[3] == tag && c0[1] == tag && c0[3] == tag && c1[1] == tag && c1[3] == tag &&
                      c2[1] == tag && c2[3] == tag && c3[1] == tag && c3[3] == tag)) break;
            __builtin_amdgcn_s_sleep(1);
        }
        { char* ws = P.ws; ST(13); }
        u32x4 ms = {a[0], a[2], b[0], b[2]};
        u32x2 cs = {__float_as_uint(__uint_as_float(c0[0]) + __uint_as_float(c0[2]) + __uint_as_float(c1[0]) + __uint_as_float(c1[2])),
                    __float_as_uint(__uint_as_float(c2[0]) + __uint_as_float(c2[2]) + __uint_as_float(c3[0]) + __uint_as_float(c3[2]))};
        *(LAS u32x4*)(uintptr_t)(lds0 + P1_MISC + lane * 16) = ms;
        *(LAS u32x2*)(uintptr_t)(lds0 + P1_MISC + 1024 + lane * 8) = cs;
        asm volatile("s_waitcnt lgkmcnt(0)" ::: "memory");
    }
    bar();
    return tag;
}

constexpr int P2_X = 131072 + 2048;
constexpr int P2_KM = P2_X + 8 * 2560;
constexpr float SAFE_RANGE = 80.f;

DI void p2_compute(char* ws, char* smem, int head, int qb, int w, int lane) {
    const int r = lane & 31, hh = lane >> 5;
    const unsigned lds0 = (unsigned)(uintptr_t)smem;
    const int qg = w & 3, kh = w >> 2;
    const int la = qg * 2 + kh, lb = qg * 2 + (1 - kh);
    cbar();
    ST(13);
    const u32x4 qa = *(LAS const u32x4*)(uintptr_t)(lds0 + P2_X + la * 1024 + lane * 16), qbv = *(LAS const u32x4*)(uintptr_t)(lds0 + P2_X + lb * 1024 + lane * 16);
    const float kmax2 = __int_as_float(__builtin_amdgcn_readlane(__float_as_int(dpp_max<true>(*(LAS const float*)(uintptr_t)(lds0 + P2_KM + lane * 4))), 63));
    float qa2 = 0.f, qb2 = 0.f;
#pragma unroll
    for (int e = 0; e < 4; ++e) { qa2 = dot2(qa[e], qa[e], qa2); qb2 = dot2(qbv[e], qbv[e], qb2); }
    const float bnda = __builtin_amdgcn_sqrtf(sum_halves(qa2) * kmax2) * 1.01f, bndb = __builtin_amdgcn_sqrtf(sum_halves(qb2) * kmax2) * 1.01f;
    const bool fast = __all(2.f * bnda <= SAFE_RANGE && 2.f * bndb <= SAFE_RANGE);
    const unsigned ka = lds0 + kh * 32768 + lane * 16, va = lds0 + 65536 + kh * 32768 + hh * 256 + (r & 15) * 16;
    const unsigned xw = lds0 + P2_X + w * 2560 + lane * 16, xr = lds0 + P2_X + (w ^ 4) * 2560 + lane * 16;
    float o[9], ma;
    ST(5);
    if (fast) {
        ma = bnda;
        unsigned kp = ka, vp = va, cnt;
#define EXP(r) "v_exp_f32 v" #r ", v" #r "\n\t"
#define CVT(d, a, b) "v_cvt_pk_bf16_f32 v" #d ", v" #a ", v" #b "\n\t"
#define MSQ(SN, KN, Q, C) "v_mfma_f32_32x32x16_bf16 " SN ", " KN ", " Q ", " C "\n\t"
#define MPO(O, V, P) "v_mfma_f32_32x32x16_bf16 " O ", " V ", " P ", " O "\n\t"
#define RDV(VN0, VN1, VOFF) "s_mov_b64 exec, %[msk]\n\t" "ds_read_b128 " VN0 ", %[vp] offset:" #VOFF "\n\t" "ds_read_b128 " VN1 ", %[vp] offset:" #VOFF "+512\n\t" "s_mov_b64 exec, -1\n\t"
#define RDK(KNN, KOFF) "ds_read_b128 " KNN ", %[kp] offset:" #KOFF "\n\t"
#define STEP(PRE, c0, c1, c2, c3, c4, c5, c6, c7, c8, c9, c10, c11, c12, c13, c14, c15, p14, p15, SN, KN, Q, CC, O, OP, VC0, VP1)  \
        EXP(c0) EXP(c1) CVT(127, p14, p15) EXP(c2) EXP(c3) MPO(OP, VP1, "v[124:127]") PRE                                    \
        CVT(120, c0, c1) EXP(c4) EXP(c5) CVT(121, c2, c3) EXP(c6)                                                            \
        "s_waitcnt lgkmcnt(3)\n\t" MSQ(SN, KN, Q, CC)                                                                        \
        EXP(c7) CVT(122, c4, c5) EXP(c8) EXP(c9) CVT(123, c6, c7) EXP(c10) EXP(c11)                                          \
        MPO(O, VC0, "v[120:123]")                                                                                            \
        CVT(124, c8, c9) EXP(c12) EXP(c13) CVT(125, c10, c11) EXP(c14) EXP(c15) CVT(126, c12, c13)
#define SA_ 32, 33, 34, 35, 36, 37, 38, 39, 40, 41, 42, 43, 44, 45, 46, 47
#define SB_ 48, 49, 50, 51, 52, 53, 54, 55, 56, 57, 58, 59, 60, 61, 62, 63
#define CALL(M, ...) M(__VA_ARGS__)
#define TE(KOFF, VOFF) CALL(STEP, RDV("v[112:115]", "v[116:119]", VOFF), SA_, 62, 63, "v[48:63]", "v[96:99]", "%[qb]", "v[144:159]", "v[64:79]", "v[80:95]", "v[104:107]", "v[116:119]") \
                       CALL(STEP, RDK("v[96:99]", KOFF), SB_, 46, 47, "v[32:47]", "v[100:103]", "%[qa]", "v[128:143]", "v[80:95]", "v[64:79]", "v[104:107]", "v[108:111]")
#define TO(KOFF, VOFF) CALL(STEP, RDV("v[104:107]", "v[108:111]", VOFF), SA_, 62, 63, "v[48:63]", "v[100:103]", "%[qb]", "v[144:159]", "v[64:79]", "v[80:95]", "v[112:115]", "v[108:111]") \
                       CALL(STEP, RDK("v[100:103]", KOFF), SB_, 46, 47, "v[32:47]", "v[96:99]", "%[qa]", "v[128:143]", "v[80:95]", "v[64:79]", "v[112:115]", "v[116:119]")
#define M64(a, b, c, d) "v_mov_b64 v[" #a ":" #b "], v[" #c ":" #d "]\n\t"
#define MOV4(a, b, c, d, src) "v_mov_b32 v" #a ", " src "\n\tv_mov_b32 v" #b ", " src "\n\tv_mov_b32 v" #c ", " src "\n\tv_mov_b32 v" #d ", " src "\n\t"
        asm volatile(
            "s_setprio 3\n\t"
            "ds_read_b128 v[96:99], %[kp]\n\t"
            "v_mov_b32 v128, %[na]\n\tv_mov_b32 v129, %[na]\n\tv_mov_b32 v144, %[nb]\n\tv_mov_b32 v145, %[nb]\n\t"
            "v_mov_b32 v104, %[one]\n\tv_mov_b32 v105, %[one]\n\tv_mov_b64 v[64:65], 0\n\t"
            M64(106, 107, 104, 105) M64(108, 109, 104, 105) M64(110, 111, 104, 105) M64(112, 113, 104, 105) M64(114, 115, 104, 105) M64(116, 117, 104, 105) M64(118, 119, 104, 105)
            "s_mov_b64 exec, %[msk]\n\t"
            "ds_read_b128 v[104:107], %[vp]\n\t"
            "ds_read_b128 v[108:111], %[vp] offset:512\n\t"
            "s_mov_b64 exec, -1\n\t"
            "ds_read_b128 v[100:103], %[kp] offset:1024\n\t"
            M64(130, 131, 128, 129) M64(132, 133, 128, 129) M64(134, 135, 128, 129) M64(136, 137, 128, 129) M64(138, 139, 128, 129) M64(140, 141, 128, 129) M64(142, 143, 128, 129)
            "s_waitcnt lgkmcnt(3)\n\t"
            "s_nop 1\n\t"
            "v_mfma_f32_32x32x16_bf16 v[32:47], v[96:99], %[qa], v[128:143]\n\t"
            M64(146, 147, 144, 145) M64(148, 149, 144, 145) M64(150, 151, 144, 145) M64(152, 153, 144, 145) M64(154, 155, 144, 145) M64(156, 157, 144, 145) M64(158, 159, 144, 145)
            M64(66, 67, 64, 65) M64(68, 69, 64, 65) M64(70, 71, 64, 65) M64(72, 73, 64, 65) M64(74, 75, 64, 65) M64(76, 77, 64, 65) M64(78, 79, 64, 65)
            M64(80, 81, 64, 65) M64(82, 83, 64, 65) M64(84, 85, 64, 65) M64(86, 87, 64, 65) M64(88, 89, 64, 65) M64(90, 91, 64, 65) M64(92, 93, 64, 65) M64(94, 95, 64, 65)
            M64(62, 63, 64, 65) M64(124, 125, 64, 65) M64(126, 127, 64, 65)
            "s_mov_b32 %[cnt], 0\n\t"
            "L_att_%=:\n\t"
            TE(2048, 1024) TO(3072, 2048) TE(4096, 3072) TO(5120, 4096)
            "v_add_u32 %[kp], 0x1000, %[kp]\n\t"
            "v_add_u32 %[vp], 0x1000, %[vp]\n\t"
            "s_cmp_lg_u32 %[cnt], 0\n\t"
            "s_cbranch_scc1 L_att_nb_%=\n\t"
            "s_barrier\n\t"
            "L_att_nb_%=:\n\t"
            "s_add_u32 %[cnt], %[cnt], 1\n\t"
            "s_cmp_eq_u32 %[cnt], 3\n\ts_cbranch_scc0 L_p1_%=\n\ts_setprio 2\n\tL_p1_%=:\n\t"
            "s_cmp_eq_u32 %[cnt], 6\n\ts_cbranch_scc0 L_p2_%=\n\ts_setprio 1\n\tL_p2_%=:\n\t"
            "s_cmp_eq_u32 %[cnt], 7\n\ts_cbranch_scc0 L_p3_%=\n\ts_setprio 0\n\tL_p3_%=:\n\t"
            "s_cmp_lt_u32 %[cnt], 8\n\t"
            "s_cbranch_scc1 L_att_%=\n\t"
            CVT(127, 62, 63)
            "s_nop 1\n\t"
            MPO("v[80:95]", "v[116:119]", "v[124:127]")
            "s_waitcnt lgkmcnt(0)\n\t"
            "s_nop 15\n\t"
            "v_mov_b32 v89, %[bb]\n\t"
            "v_mov_b32 %[o0], v64\n\tv_mov_b32 %[o1], v65\n\tv_mov_b32 %[o2], v66\n\tv_mov_b32 %[o3], v67\n\t"
            "v_mov_b32 %[o4], v68\n\tv_mov_b32 %[o5], v69\n\tv_mov_b32 %[o6], v70\n\tv_mov_b32 %[o7], v71\n\tv_mov_b32 %[o8], v72\n\t"
            "ds_write_b128 %[xw], v[80:83]\n\t"
            "ds_write_b128 %[xw], v[84:87] offset:1024\n\t"
            "ds_write_b64 %[xw2], v[88:89]\n\t"
            : [o0] "=&v"(o[0]), [o1] "=&v"(o[1]), [o2] "=&v"(o[2]), [o3] "=&v"(o[3]), [o4] "=&v"(o[4]), [o5] "=&v"(o[5]), [o6] "=&v"(o[6]), [o7] "=&v"(o[7]), [o8] "=&v"(o[8]),
              [kp] "+v"(kp), [vp] "+v"(vp), [cnt] "=&s"(cnt)
            : [qa] "v"(qa), [qb] "v"(qbv), [na] "v"(-bnda), [nb] "v"(-bndb), [bb] "v"(bndb), [msk] "s"(0x0000ffff0000ffffull), [xw] "v"(xw), [xw2] "v"(xw + 2048 - lane * 8), [one] "v"((r == 16 || r == 20) ? ONES : 0u)
            : "memory", "scc",
              "v32", "v33", "v34", "v35", "v36", "v37", "v38", "v39", "v40", "v41", "v42", "v43", "v44", "v45", "v46", "v47",
              "v48", "v49", "v50", "v51", "v52", "v53", "v54", "v55", "v56", "v57", "v58", "v59", "v60", "v61", "v62", "v63",
              "v64", "v65", "v66", "v67", "v68", "v69", "v70", "v71", "v72", "v73", "v74", "v75", "v76", "v77", "v78", "v79",
              "v80", "v81", "v82", "v83", "v84", "v85", "v86", "v87", "v88", "v89", "v90", "v91", "v92", "v93", "v94", "v95",
              "v96", "v97", "v98", "v99", "v100", "v101", "v102", "v103", "v104", "v105", "v106", "v107", "v108", "v109", "v110", "v111",
              "v112", "v113", "v114", "v115", "v116", "v117", "v118", "v119", "v120", "v121", "v122", "v123", "v124", "v125", "v126", "v127",
              "v128", "v129", "v130", "v131", "v132", "v133", "v134", "v135", "v136", "v137", "v138", "v139", "v140", "v141", "v142", "v143",
              "v144", "v145", "v146", "v147", "v148", "v149", "v150", "v151", "v152", "v153", "v154", "v155", "v156", "v157", "v158", "v159");
    } else {
        bar();
        const u32x4 ones = {ONES, ONES, ONES, ONES};
        const f32x16 zero = {};
#pragma unroll 1
        for (int sl = 1; sl >= 0; --sl) {
            const bf16x8 qf = __builtin_bit_cast(bf16x8, sl ? qbv : qa);
            bf16x8 v0 = __builtin_bit_cast(bf16x8, ones), v1 = v0, kf;
            f32x16 oo = {};
            float m = -INFINITY;
#pragma unroll 1
            for (int t = 0; t < 32; ++t) {
                asm volatile("ds_read_b128 %0, %3\n\ts_mov_b64 exec, %5\n\tds_read_b128 %1, %4\n\tds_read_b128 %2, %4 offset:512\n\ts_mov_b64 exec, -1\n\ts_waitcnt lgkmcnt(0)"
                             : "=&v"(kf), "+v"(v0), "+v"(v1) : "v"(ka + t * 1024), "v"(va + t * 1024), "s"(0x0000ffff0000ffffull) : "memory");
                const f32x16 s = MFMA(kf, qf, zero);
                float tm = s[0];
#pragma unroll
                for (int i = 1; i < 16; ++i) tm = fmaxf(tm, s[i]);
                tm = fmaxf(tm, other_half(tm));
                const float mn = fmaxf(m, tm);
                const float alpha = __builtin_amdgcn_exp2f(m - mn);
                m = mn;
#pragma unroll
                for (int i = 0; i < 16; ++i) oo[i] *= alpha;
                u32x4 p0, p1;
#pragma unroll
                for (int i = 0; i < 4; ++i) p0[i] = cvtpk(__builtin_amdgcn_exp2f(s[2 * i] - m), __builtin_amdgcn_exp2f(s[2 * i + 1] - m));
#pragma unroll
                for (int i = 0; i < 4; ++i) p1[i] = cvtpk(__builtin_amdgcn_exp2f(s[8 + 2 * i] - m), __builtin_amdgcn_exp2f(s[9 + 2 * i] - m));
                oo = MFMA(v0, __builtin_bit_cast(bf16x8, p0), oo);
                oo = MFMA(v1, __builtin_bit_cast(bf16x8, p1), oo);
            }
            if (sl == 1) {
                f32x4 t0 = {oo[0], oo[1], oo[2], oo[3]}, t1 = {oo[4], oo[5], oo[6], oo[7]};
                f32x2 t2 = {oo[8], m};
                *(LAS f32x4*)(uintptr_t)xw = t0; *(LAS f32x4*)(uintptr_t)(xw + 1024) = t1; *(LAS f32x2*)(uintptr_t)(xw + 2048 - lane * 8) = t2;
            } else {
#pragma unroll
                for (int i = 0; i < 9; ++i) o[i] = oo[i];
                ma = m;
            }
        }
    }
    ST(14);
    cbar();
    {
        const f32x4 t0 = *(LAS const f32x4*)(uintptr_t)xr, t1 = *(LAS const f32x4*)(uintptr_t)(xr + 1024);
        const f32x2 t2 = *(LAS const f32x2*)(uintptr_t)(xr + 2048 - lane * 8);
        const float mo = t2[1], mn = fmaxf(ma, mo);
        const float fa = __builtin_amdgcn_exp2f(ma - mn), fo = __builtin_amdgcn_exp2f(mo - mn);
        const float inv = __builtin_amdgcn_rcpf(o[8] * fa + t2[0] * fo);
        const float ia = fa * inv, io = fo * inv;
        const u32x4 ov = pack_swap(o[0] * ia + t0[0] * io, o[1] * ia + t0[1] * io, o[2] * ia + t0[2] * io, o[3] * ia + t0[3] * io,
                                   o[4] * ia + t1[0] * io, o[5] * ia + t1[1] * io, o[6] * ia + t1[2] * io, o[7] * ia + t1[3] * io);
        gst128(ws + WS_OF + ((size_t)(qb * 8 + la) * 32 + head) * 1024, lane * 16, ov);
    }
}

constexpr int P3_X = 153600;
template <int MODE>
DI void p35_compute(const Ptrs& P, char* smem, int tb, int cb, int w, int lane, u32x4& hkeep) {
    constexpr int A0 = MODE == 0 ? 65536 : 98304, XA = MODE == 0 ? 131072 : 147456;
    char* ws = P.ws;
    const int r = lane & 31, hh = lane >> 5;
    const unsigned lds0 = (unsigned)(uintptr_t)smem;
    const int ta = w & 1, cj = (w >> 1) & 1, kh = w >> 2;
    const int tile = tb * 2 + ta, token = tile * 32 + r, n0 = cb * 64 + cj * 32 + kh * 16;
    u32x2 res[2];
    const unsigned aro = lds0 + A0 + ta * 8192 + lane * 16 + kh * 4096;
    const unsigned wro = lds0 + hh * 1024 + ((lane & 15) >> 2) * 64 + ((lane >> 4) & 1) * 32 + (lane & 3) * 8 + cj * 256 + kh * 8192;
    f32x16 acc = {};
    bf16x8 A[4], Wf[4];
#define P35_LD(S, BA, BW, ks) { A[S] = lds_b128(aro + (BA) + (ks) * 1024); Wf[S] = lds_trfrag(wro + (BW) + (ks) * 2048, 512); }
    if constexpr (MODE == 0) {
        cbar();
        ST(7);
        P35_LD(0, 0, 0, 0); P35_LD(1, 0, 0, 1);
        unsigned BA = 0;
#pragma unroll 1
        for (int ch = 0; ch < 4; ++ch) {
            P35_LD(2, BA, BA, 2); SB(); acc = MFMA(Wf[0], A[0], acc); SB();
            P35_LD(3, BA, BA, 3); SB(); acc = MFMA(Wf[1], A[1], acc); SB();
            if (ch < 3) {
                cbar();
                BA += 16384;
                P35_LD(0, BA, BA, 0);
            }
            SB(); acc = MFMA(Wf[2], A[2], acc); SB();
            if (ch < 3) { P35_LD(1, BA, BA, 1); }
            SB(); acc = MFMA(Wf[3], A[3], acc); SB();
        }
    } else {
        bf16x8 WR[19];
        bar();
#pragma unroll
        for (int q = 0; q < 16; ++q) WR[q] = lds_trfrag(wro + (q >> 2) * 16384 + (q & 3) * 2048, 512);
#pragma unroll
        for (int j = 0; j < 3; ++j) WR[16 + j] = lds_trfrag(wro + 4 * 16384 - kh * 8192 + (kh * 3 + j) * 2048, 512);
        cbar();
        cbar();
        ST(8);
        P35_LD(0, 0, 0, 0); P35_LD(1, 0, 0, 1);
        unsigned BA = 0, BW = 0;
#define P5_ADV() { BA = BA == 2 * 16384 ? 0 : BA + 16384; }
#define P5_LDA(S, ks) { A[S] = lds_b128(aro + BA + (ks) * 1024); }
#pragma unroll 1
        for (int ch = 0; ch < 5; ++ch) {
            P35_LD(2, BA, BW, 2); SB(); acc = MFMA(Wf[0], A[0], acc); SB();
            P35_LD(3, BA, BW, 3); SB(); acc = MFMA(Wf[1], A[1], acc); SB();
            cbar();
            P5_ADV(); BW += 16384;
            P35_LD(0, BA, BW, 0); SB(); acc = MFMA(Wf[2], A[2], acc); SB();
            P35_LD(1, BA, BW, 1); SB(); acc = MFMA(Wf[3], A[3], acc); SB();
        }
        P35_LD(2, BA, BW, 2); SB(); acc = MFMA(Wf[0], A[0], acc); SB();
        P35_LD(3, BA, BW, 3); SB(); acc = MFMA(Wf[1], A[1], acc); SB();
        cbar();
        P5_ADV();
        P5_LDA(0, 0); SB(); acc = MFMA(Wf[2], A[2], acc); SB();
        P5_LDA(1, 1); SB(); acc = MFMA(Wf[3], A[3], acc); SB();
#pragma unroll
        for (int c = 0; c < 4; ++c) {
            P5_LDA(2, 2); SB(); acc = MFMA(WR[4 * c], A[0], acc); SB();
            P5_LDA(3, 3); SB(); acc = MFMA(WR[4 * c + 1], A[1], acc); SB();
            cbar();
            P5_ADV();
            if (c < 3) { P5_LDA(0, 0); }
            SB(); acc = MFMA(WR[4 * c + 2], A[2], acc); SB();
            if (c < 3) { P5_LDA(1, 1); }
            SB(); acc = MFMA(WR[4 * c + 3], A[3], acc); SB();
        }
#pragma unroll
        for (int j = 0; j < 3; ++j) acc = MFMA(WR[16 + j], lds_b128(aro + BA - kh * 4096 + (kh * 3 + j) * 1024), acc);
    }
    ST(MODE == 0 ? 9 : 0);
    {
        LAS f32x4* xw = (LAS f32x4*)(smem + XA + w * 2048 + lane * 16);
        f32x4 s0, s1;
#pragma unroll
        for (int e = 0; e < 4; ++e) { s0[e] = kh ? acc[e] : acc[8 + e]; s1[e] = kh ? acc[4 + e] : acc[12 + e]; }
        xw[0] = s0; xw[64] = s1;
    }
    cbar();
    float v[8];
    {
        LAS const f32x4* xr = (LAS const f32x4*)(smem + XA + (w ^ 4) * 2048 + lane * 16);
        const f32x4 s0 = xr[0], s1 = xr[64];
        if constexpr (MODE == 0) {
            const int row = ta * 32 + r;
#pragma unroll
            for (int g = 0; g < 2; ++g)
                res[g] = *(LAS const u32x2*)(smem + P3_X + row * 128 + (((cj * 4 + kh * 2 + g) ^ ((row >> 1) & 7)) << 4) + hh * 8);
        } else { res[0][0] = hkeep[0]; res[0][1] = hkeep[1]; res[1][0] = hkeep[2]; res[1][1] = hkeep[3]; }
#pragma unroll
        for (int e = 0; e < 4; ++e) { v[e] = (kh ? acc[8 + e] : acc[e]) + s0[e]; v[4 + e] = (kh ? acc[12 + e] : acc[4 + e]) + s1[e]; }
#pragma unroll
        for (int g = 0; g < 2; ++g) {
            v[4 * g] += bflo(res[g][0]); v[4 * g + 1] += bfhi(res[g][0]); v[4 * g + 2] += bflo(res[g][1]); v[4 * g + 3] += bfhi(res[g][1]);
        }
    }
    const unsigned p0 = cvtpk(v[0], v[1]), p1 = cvtpk(v[2], v[3]), p2 = cvtpk(v[4], v[5]), p3 = cvtpk(v[6], v[7]);
    const u32x4 o = swap4(p0, p1, p2, p3);
    if constexpr (MODE == 0) {
        hkeep[0] = p0; hkeep[1] = p1; hkeep[2] = p2; hkeep[3] = p3;
        gst128(ws + WS_HF + ((size_t)tile * 32 + (n0 >> 4)) * 1024, lane * 16, o);
        float s1 = 0.f, s2 = 0.f;
#pragma unroll
        for (int e = 0; e < 4; ++e) { s1 = dot2(o[e], ONES, s1); s2 = dot2(o[e], o[e], s2); }
        s1 = sum_halves(s1); s2 = sum_halves(s2);
        LAS f32x2* sl = (LAS f32x2*)(smem + XA + 8 * 2048);
        if (lane < 32) { f32x2 sv = {s1, s2}; sl[(ta * 32 + r) * 4 + cj * 2 + kh] = sv; }
        cbar();
        if (w == 0) {
            const f32x4 p = *(LAS const f32x4*)(sl + lane * 4), q = *(LAS const f32x4*)(sl + lane * 4 + 2);
            u32x2 sv = {__float_as_uint(p[0] + p[2] + q[0] + q[2]), __float_as_uint(p[1] + p[3] + q[1] + q[3])};
            gst64(ws + WS_ST + (size_t)tb * 64 * 64 + cb * 8, lane * 64, sv);
        }
    } else {
        *(u32x4*)(P.out + (size_t)token * 1024 + (n0 + 8 * hh) * 2) = o;
    }
}

constexpr int P4_ST = 147456, P4_CS = P4_ST + 8192;
template <int NB>
DI void p4_compute(char* ws, char* smem, int tb, int cg, int w, int lane) {
    const int r = lane & 31, hh = lane >> 5, i = w & 3, j0 = NB == 2 ? 0 : 2;
    const unsigned lds0 = (unsigned)(uintptr_t)smem;
    const int tile = tb * 4 + i;
    const unsigned hro = lds0 + 98304 + i * 4096 + lane * 16;
    const unsigned wro = lds0 + hh * 1536 + ((lane & 15) >> 2) * 64 + ((lane >> 4) & 1) * 32 + (lane & 3) * 8 + j0 * 256;
    f32x16 ag[NB], au[NB];
#pragma unroll
    for (int j = 0; j < NB; ++j) { ag[j] = f32x16{}; au[j] = f32x16{}; }
    bf16x8 H[4], G[4][NB], U[4][NB];
#define P4_LOAD(S, BH, BW, ks)                                                                                                    \
    {                                                                                                                             \
        H[S] = lds_b128(hro + (BH) + (ks) * 1024);                                                                                 \
        _Pragma("unroll") for (int j = 0; j < NB; ++j) { G[S][j] = lds_trfrag(wro + (BW) + (ks) * 3072 + j * 256, 768); U[S][j] = lds_trfrag(wro + (BW) + 12288 + (ks) * 3072 + j * 256, 768); } \
    }
#define P4_MMA(S) { _Pragma("unroll") for (int j = 0; j < NB; ++j) { ag[j] = MFMA(G[S][j], H[S], ag[j]); au[j] = MFMA(U[S][j], H[S], au[j]); } }
    cbar();
    ST(6);
    P4_LOAD(0, 0, 0, 0); P4_LOAD(1, 0, 0, 1);
    unsigned BH = 0, BW = 0;
#pragma unroll 1
    for (int ch = 0; ch < 8; ++ch) {
        P4_LOAD(2, BH, BW, 2); SB(); P4_MMA(0); SB();
        P4_LOAD(3, BH, BW, 3); SB(); P4_MMA(1); SB();
        cbar();
        if (ch < 7) {
            BH = BH == 2 * 16384 ? 0 : BH + 16384; BW = BW == 3 * 24576 ? 0 : BW + 24576;
            P4_LOAD(0, BH, BW, 0);
        }
        SB(); P4_MMA(2); SB();
        if (ch < 7) { P4_LOAD(1, BH, BW, 1); }
        SB(); P4_MMA(3); SB();
    }
    ST(15);
    float mean, rstd;
    {
        float s1 = 0.f, s2 = 0.f;
        const LAS f32x4* sp = (const LAS f32x4*)(smem + P4_ST + (i * 32 + r) * 64);
#pragma unroll
        for (int k = 0; k < 4; ++k) { const f32x4 v = sp[k]; s1 += v[0] + v[2]; s2 += v[1] + v[3]; }
        mean = s1 * (1.f / DM);
        rstd = __builtin_amdgcn_rsqf(fmaxf(s2 * (1.f / DM) - mean * mean, 0.f) + LN_EPS);
    }
    constexpr float C1 = -2.f * 0.7978845608028654f * LOG2E, C2 = C1 * 0.044715f;
    const float k1 = C1 * rstd, k2 = C2 * rstd * rstd * rstd, k3 = __builtin_amdgcn_rcpf(rstd * rstd);
    const f32x2 nm2 = {-mean, -mean}, k12 = {k1, k1}, k22 = {k2, k2}, k32 = {k3, k3};
#pragma unroll
    for (int j = 0; j < NB; ++j) {
        const int blk = cg * 3 + j0 + j;
        float v[16];
#pragma unroll
        for (int g = 0; g < 4; ++g) {
            const LAS f32x4* cp = (const LAS f32x4*)(smem + P4_CS + ((j0 + j) * 32 + 8 * g + 4 * hh) * 4);
            const f32x4 cgf = cp[0], cuf = cp[32];
#pragma unroll
            for (int e = 0; e < 4; e += 2) {
                const f32x2 ga = {ag[j][4 * g + e], ag[j][4 * g + e + 1]}, ua = {au[j][4 * g + e], au[j][4 * g + e + 1]};
                const f32x2 cg2 = {cgf[e], cgf[e + 1]}, cu2 = {cuf[e], cuf[e + 1]};
                const f32x2 a = __builtin_elementwise_fma(nm2, cg2, ga), bq = __builtin_elementwise_fma(nm2, cu2, ua);
                const f32x2 arg = a * __builtin_elementwise_fma(a * a, k22, k12);
                const f32x2 ex = {__builtin_amdgcn_exp2f(arg[0]), __builtin_amdgcn_exp2f(arg[1])};
                const f32x2 den = __builtin_elementwise_fma(ex, k32, k32);
                const f32x2 rc = {__builtin_amdgcn_rcpf(den[0]), __builtin_amdgcn_rcpf(den[1])};
                const f32x2 y = (a * rc) * bq;
                v[4 * g + e] = y[0]; v[4 * g + e + 1] = y[1];
            }
        }
        if (blk < 43) {
#pragma unroll
            for (int g = 0; g < 2; ++g) {
                const u32x4 o = pack_swap(v[8 * g], v[8 * g + 1], v[8 * g + 2], v[8 * g + 3], v[8 * g + 4], v[8 * g + 5], v[8 * g + 6], v[8 * g + 7]);
                gst128(ws + WS_AF + ((size_t)tile * 86 + blk * 2 + g) * 1024, lane * 16, o);
            }
        }
    }
}

__global__ __launch_bounds__(640, 3) void k_block(const char* __restrict__ x, const char* __restrict__ wqkv, const char* __restrict__ wo, const char* __restrict__ wg,
                                                  const char* __restrict__ wu, const char* __restrict__ wd, char* __restrict__ ws, char* __restrict__ out) {
    extern __shared__ __attribute__((aligned(16))) char smem[];
    const Ptrs P = {x, wqkv, wo, wg, wu, wd, ws, out};
    const int b = blockIdx.x, xcd = b & 7, idx = b >> 3;
    const int lane = threadIdx.x & 63, w = rfl(threadIdx.x >> 6);
    const unsigned lds0 = (unsigned)(uintptr_t)smem;
    if (w >= 8 && blockIdx.x < 64) {
        const char* pc = (const char*)(__builtin_amdgcn_s_getpc() & ~127ull);
        asm volatile("s_mov_b32 m0, %0\n\ts_nop 0" ::"s"(lds0 + LDS_BYTES - 256) : "memory");
#pragma unroll
        for (int i = 0; i < (CODE_LINES + 63) / 64; ++i) { const int ln = i * 64 + lane; gtouch(pc, (ln < CODE_LINES ? ln : CODE_LINES - 1) * 128); }
    }
    const bool p1_gemm = idx >= 8;
    const int gi = idx - 8;
    const int tb1 = (xcd >> 1) * 4 + (gi & 3), cb1 = (xcd & 1) * 6 + (gi >> 2);
    const int head = xcd * 4 + (idx >> 3), qb = idx & 7;
    const int tb3 = xcd * 4 + (idx & 3), cb3 = idx >> 2;
    const bool p4_on = !(xcd & 1) || idx < 28;
    const int tb4 = (xcd >> 1) * 4 + (idx & 3), cg4 = (xcd & 1) * 8 + (idx >> 2);
    ST(0);

    if (w < 8) {
        if (p1_gemm) {
            f32x16 accA, accB;
            if (w >= 4) p1_colsum(wqkv, ws, tb1, cb1, smem, w - 4, lane, accA, accB);
            else if (cb1 < 8) p1_compute<false>(ws, tb1, cb1, smem, w, lane, accA, accB); else p1_compute<true>(ws, tb1, cb1, smem, w, lane, accA, accB);
            const int tl = 2 * (w & 1) + (w >> 2), cp = (w >> 1) & 1;
            if (cb1 < 8) p1_epilogue<false>(ws, tb1, cb1, smem, tl, cp, accA, accB, lane); else p1_epilogue<true>(ws, tb1, cb1, smem, tl, cp, accA, accB, lane);
        } else p1_helper(x, wqkv, wg, wu, ws, idx * 8 + xcd, w, lane);
        ST(1);
        pbar();
        p2_compute(ws, smem, head, qb, w, lane);
        ST(2);
        pbar();
        u32x4 hkeep;
        p35_compute<0>(P, smem, tb3, cb3, w, lane, hkeep);
        ST(3);
        pbar();
        if (p4_on) {
            if (w < 4) p4_compute<2>(ws, smem, tb4, cg4, w, lane); else p4_compute<1>(ws, smem, tb4, cg4, w, lane);
        } else bar();
        ST(4);
        pbar();
        p35_compute<1>(P, smem, tb3, cb3, w, lane, hkeep);
        ST(10);
        return;
    }

    const int L = w - 8;
    const char* flags = ws + WS_FLAG;
    const unsigned lin = lane * 16;
    if (!p1_gemm && L == 0) small_touch(ws, b, lane, lds0);
    const unsigned tag = p1_gemm ? p1_load(P, L, tb1, cb1, P1ROT, gi, lds0, lane) : rfl(gld32_wait(ws + WS_EPOCH, 0)) + 1;
    bar();
    if (L == 0 && lane == 0) gst32(ws + WS_FLAG, b * 128, tag);
    ST(1);
    {
        const int ptb = lane < 32 ? (lane & 15) : 2 * qb + (lane & 1), pcb = (lane < 16 ? 4 : lane < 32 ? 8 : 0) + (head >> 3);
        const int pb = ((pcb % 6) * 4 + (ptb & 3) + 8) * 8 + (ptb >> 2) * 2 + pcb / 6;
        poll(flags, pb * 128, lane < 34, tag);
        ST(6);
        const int rot = 0;
        const char* src = ws + (L == 0 ? WS_KF : WS_VF) + (size_t)head * 65536;
        const unsigned dst = lds0 + L * 65536;
        {
            const char* q = ws + WS_QF + ((size_t)head * 64 + qb * 8) * 1024 + L * 4096;
            dma4lin(q, lin, lds0 + P2_X + L * 4096);
            if (L == 0) dma1dw(ws + WS_KM + head * 256, lane * 4, lds0 + P2_KM);
        }
        dma4lin(src, lin, dst); dma4lin(src + 32768, lin, dst + 32768);
        dma2lin(src + 4096, lin, dst + 4096); dma2lin(src + 32768 + 4096, lin, dst + 32768 + 4096);
        lbar<0>();
        dma2lin(src + 6144, lin, dst + 6144); dma2lin(src + 32768 + 6144, lin, dst + 32768 + 6144);
#pragma unroll 1
        for (int g = 2; g < 8; ++g) {
            dma4lin(src + g * 4096, lin, dst + g * 4096); dma4lin(src + 32768 + g * 4096, lin, dst + 32768 + g * 4096);
        }
        ST(7);
        if (L == 1) {
            asm volatile("s_waitcnt vmcnt(0)" ::: "memory");
        }
        lbar<0>();
        if (L == 1) {
#pragma unroll 1
            for (int g = 0; g < 4; ++g) poll(flags, (g * 64 + lane) * 128, true, tag);
            if (b == 0 && lane == 0) gst32(ws + WS_EPOCH, 0, tag);
            asm volatile("s_mov_b32 m0, %0\n\ts_nop 0" ::"s"(lds0 + LDS_BYTES - 256) : "memory");
            gtouch(wo, (idx * 128 + lane) * 128); gtouch(wo, (idx * 128 + 64 + lane) * 128);
            {
                const int nl = (xcd & 1) ? 10 : 12, ln = lane & 15;
                const unsigned co = (xcd & 1) * 1536 + (ln < nl ? ln : 0) * 128;
#pragma unroll
                for (int i = 0; i < 4; ++i) {
                    unsigned o_ = (idx * 16 + i * 4 + (lane >> 4)) * (FF * 2) + co;
                    o_ = o_ > 512u * FF * 2 - 4 ? 512u * FF * 2 - 4 : o_;
                    gtouch(wg, o_); gtouch(wu, o_);
                }
            }
            gtouch(out + cb3 * 128, (tb3 * 64 + lane) * 1024);
#pragma unroll
            for (int i = 0; i < 6; ++i) { int ln = i * 64 + lane; ln = ln > 343 ? 343 : ln; gtouch(wd, (idx * 344 + ln) * 128); }
        }
        bar();
    }
    unsigned wvo[4];
    {
        const unsigned wv = ((lane >> 5) * 4 + ((lane >> 2) & 3)) * 1024 + ((lane >> 4) & 1) * 64 + (lane & 3) * 16;
#pragma unroll
        for (int i = 0; i < 4; ++i) wvo[i] = wv + i * 8192 - i * 1024;
    }
    {
        const int rot = 0;
        bar();
        if (L == 1) {
            {
                const int rl = lane >> 3, cs = lane & 7;
                unsigned xv[4];
#pragma unroll
                for (int i = 0; i < 4; ++i) xv[i] = (8 * i + rl) * 1024 + ((cs ^ ((4 * (i & 1) + (rl >> 1)) & 7)) << 4) - i * 1024;
                const char* xs = x + (size_t)tb3 * 64 * 1024 + cb3 * 128;
                dma4(xs, xv[0], xv[1], xv[2], xv[3], lds0 + P3_X); dma4(xs + 32768, xv[0], xv[1], xv[2], xv[3], lds0 + P3_X + 4096);
            }
#pragma unroll 1
            for (int c = 0; c < 4; ++c) {
                const char* src = wo + cb3 * 128 + (size_t)((c + rot) & 3) * 128 * 1024;
#pragma unroll
                for (int g = 0; g < 4; ++g) dma4(src + g * 32768, wvo[0], wvo[1], wvo[2], wvo[3], lds0 + c * 16384 + g * 4096);
            }
        }
        if (L == 0) {
            if (lane == 0) gst32(ws + WS_FLAG + 256 * 128, b * 128, tag);
            ST(2);
            const int ph = lane & 31;
            poll(flags + 256 * 128, (((ph & 3) * 8 + (tb3 >> 2)) * 8 + (ph >> 2)) * 128, true, tag);
            ST(8);
            const char* ab = ws + WS_OF + (size_t)tb3 * 2 * 32 * 1024;
#pragma unroll 1
            for (int c = 0; c < 4; ++c) {
                const char* src = ab + ((c + rot) & 3) * 8192;
                const unsigned dst = lds0 + 65536 + c * 16384;
                dma4lin(src, lin, dst); dma4lin(src + 4096, lin, dst + 4096); dma4lin(src + 32768, lin, dst + 8192); dma4lin(src + 32768 + 4096, lin, dst + 12288);
                if (c == 1) lbar<16>();
            }
            lbar<32>(); lbar<16>(); lbar<0>();
        } else {
            lbar<0>(); bar(); bar(); bar();
        }
        bar();
    }
    {
        const int rot = 0;
        unsigned gvo[12];
        if (L == 1 && p4_on) {
#pragma unroll
            for (int j = 0; j < 12; ++j) {
                const int s = j * 64 + lane, st = s >> 4, kb = st / 3, nb = st % 3, q = (s >> 2) & 3, c = s & 3;
                int blk = cg4 * 3 + nb; blk = blk > 42 ? 42 : blk;
                gvo[j] = (4 * kb + q) * (FF * 2) + blk * 64 + c * 16 - (j & 3) * 1024;
            }
        }
        auto issue_w = [&](int c, unsigned B) {
            const size_t so = (size_t)((c + rot) & 7) * 64 * (FF * 2);
#pragma unroll
            for (int g = 0; g < 3; ++g) dma4(wg + so, gvo[4 * g], gvo[4 * g + 1], gvo[4 * g + 2], gvo[4 * g + 3], lds0 + B + g * 4096);
#pragma unroll
            for (int g = 0; g < 3; ++g) dma4(wu + so, gvo[4 * g], gvo[4 * g + 1], gvo[4 * g + 2], gvo[4 * g + 3], lds0 + B + 12288 + g * 4096);
        };
        bar();
        bar();
        if (L == 1 && p4_on) { issue_w(0, 0); issue_w(1, 24576); issue_w(2, 2 * 24576); issue_w(3, 3 * 24576); }
        if (L == 0) { if (lane == 0) gst32(ws + WS_FLAG + 2 * 256 * 128, b * 128, tag); ST(3); }
        if (p4_on) {
            if (L == 0) {
                const int ptb = tb4 * 2 + (lane & 1), pcb = (lane >> 1) & 7;
                poll(flags + 2 * 256 * 128, ((pcb * 4 + (ptb & 3)) * 8 + (ptb >> 2)) * 128, true, tag);
                ST(9);
                const char* hb = ws + WS_HF + (size_t)tb4 * 4 * 32 * 1024;
                auto issue_h = [&](int c, unsigned B) {
                    const char* src = hb + ((c + rot) & 7) * 4096;
#pragma unroll
                    for (int i = 0; i < 4; ++i) dma4lin(src + i * 32768, lin, lds0 + 98304 + B + i * 4096);
                };
                issue_h(0, 0); issue_h(1, 16384);
                lbar<16>();
                {
                    const char* sb = ws + WS_ST + (size_t)tb4 * 128 * 64;
                    dma4lin(sb, lin, lds0 + P4_ST); dma4lin(sb + 4096, lin, lds0 + P4_ST + 4096);
                    const char* cb_ = ws + WS_CS + cg4 * 384;
                    dma1dw(cb_, lane * 4, lds0 + P4_CS); dma1dw(cb_ + 256, lane * 4, lds0 + P4_CS + 256);
                    dma1dw(cb_ + FF * 4, lane * 4, lds0 + P4_CS + 512); dma1dw(cb_ + FF * 4 + 256, lane * 4, lds0 + P4_CS + 768);
                }
                issue_h(2, 2 * 16384);
                unsigned B = 0;
#pragma unroll 1
                for (int c = 1; c < 8; ++c) {
                    if (c < 7) lbar<16>(); else lbar<0>();
                    if (c + 2 < 8) { issue_h(c + 2, B); B = B == 2 * 16384 ? 0 : B + 16384; }
                }
            } else {
                unsigned B = 0;
#pragma unroll 1
                for (int c = 0; c < 8; ++c) {
                    if (c == 0) lbar<63>(); else if (c < 6) lbar<48>(); else if (c == 6) lbar<24>(); else lbar<0>();
                    if (c >= 1 && c + 3 < 8) { issue_w(c + 3, B); B = B == 3 * 24576 ? 0 : B + 24576; }
                }
            }
            bar();
        } else bar();
    }
    {
        const int rot = 0;
        auto issue_w = [&](int c, unsigned B) {
            const int cc = c < 10 ? (c + rot) % 10 : 10;
            const char* src = wd + cb3 * 128 + (size_t)cc * 128 * 1024;
#pragma unroll
            for (int g = 0; g < 3; ++g) dma4(src + g * 32768, wvo[0], wvo[1], wvo[2], wvo[3], lds0 + B + g * 4096);
            dma4(src + (c < 10 ? 3 : 2) * 32768, wvo[0], wvo[1], wvo[2], wvo[3], lds0 + B + 3 * 4096);
        };
        if (L == 1) {
#pragma unroll 1
            for (int c = 6; c < 11; ++c) issue_w(c, (c - 6) * 16384);
        }
        bar();
        if (L == 0 && p4_on && lane == 0) gst32(ws + WS_FLAG + 3 * 256 * 128, b * 128, tag);
        if (L == 1) lbar<0>(); else bar();
        bar();
        if (L == 1) {
#pragma unroll 1
            for (int c = 0; c < 6; ++c) issue_w(c, c * 16384);
        }
        if (L == 0) {
            ST(4);
            const int ptb = tb3 >> 1, pcg = lane < 15 ? lane : 0;
            poll(flags + 3 * 256 * 128, (((pcg & 7) * 4 + (ptb & 3)) * 8 + (ptb >> 2) * 2 + (pcg >> 3)) * 128, true, tag);
            ST(5);
            const char* ab = ws + WS_AF + (size_t)tb3 * 2 * 86 * 1024;
            auto issue_a = [&](int c, unsigned B) {
                const int cc = c < 10 ? (c + rot) % 10 : 10;
                const char* src = ab + cc * 8192;
                const unsigned dst = lds0 + 98304 + B;
                const int o2 = c < 10 ? 4096 : 2048;
                dma4lin(src, lin, dst); dma4lin(src + o2, lin, dst + o2);
                dma4lin(src + 86 * 1024, lin, dst + 8192); dma4lin(src + 86 * 1024 + o2, lin, dst + 8192 + o2);
            };
            issue_a(0, 0); issue_a(1, 16384);
            unsigned B = 2 * 16384;
#pragma unroll 1
            for (int c = 0; c < 11; ++c) {
                if (c < 10) lbar<16>(); else lbar<0>();
                if (c + 2 < 11) { issue_a(c + 2, B); B = B == 2 * 16384 ? 0 : B + 16384; }
            }
        } else {
            lbar<63>(); lbar<63>(); lbar<48>(); lbar<32>(); lbar<16>(); lbar<0>();
            bar(); bar(); bar(); bar(); bar();
        }
        bar();
    }
}

extern "C" void kernel_launch(void* const* d_in, const int* in_sizes, int n_in, void* d_out, int out_size, void* d_ws, size_t ws_size, hipStream_t stream) {
    (void)hipFuncSetAttribute((const void*)k_block, hipFuncAttributeMaxDynamicSharedMemorySize, LDS_BYTES);
    k_block<<<256, 640, LDS_BYTES, stream>>>((const char*)d_in[0], (const char*)d_in[1], (const char*)d_in[2], (const char*)d_in[4], (const char*)d_in[5], (const char*)d_in[6],
                                             (char*)d_ws, (char*)d_out);
}
```

```cpp
#include <hip/hip_runtime.h>
#include <hip/hip_bf16.h>
#include <stdint.h>

typedef __bf16 bf16x8 __attribute__((ext_vector_type(8)));
typedef __bf16 bf16x2 __attribute__((ext_vector_type(2)));
typedef short s16x4 __attribute__((ext_vector_type(4)));
typedef short s16x8 __attribute__((ext_vector_type(8)));
typedef float f32x16 __attribute__((ext_vector_type(16)));
typedef float f32x4 __attribute__((ext_vector_type(4)));
typedef float f32x2 __attribute__((ext_vector_type(2)));
typedef unsigned u32x4 __attribute__((ext_vector_type(4)));
typedef unsigned u32x2 __attribute__((ext_vector_type(2)));
#define LAS __attribute__((address_space(3)))
#define DI __device__ __forceinline__

constexpr int DM = 512, FF = 1376;
constexpr float LOG2E = 1.4426950408889634f;
constexpr float QSCALE = 0.25f * LOG2E;
constexpr float LN_EPS = 1e-5f;

constexpr size_t WS_QF = 0, WS_KF = 2u << 20, WS_VF = 4u << 20, WS_OF = 6u << 20, WS_HF = 8u << 20, WS_AF = 10u << 20,
                 WS_ST = 16u << 20, WS_CS = 17u << 20, WS_KM = (17u << 20) + 65536, WS_FLAG = 18u << 20, WS_EPOCH = WS_FLAG + 4 * 256 * 128,
                 WS_G1 = 19u << 20, WS_G2 = WS_G1 + 65536;
constexpr int LDS_BYTES = 163840;
constexpr unsigned SPIN_CAP = 1u << 17;
constexpr int CODE_LINES = 288;

#define MFMA(a, b, c) __builtin_amdgcn_mfma_f32_32x32x16_bf16(a, b, c, 0, 0, 0)
#define SB() __builtin_amdgcn_sched_barrier(0)
DI void cbar() { asm volatile("s_waitcnt lgkmcnt(0)\n\ts_barrier" ::: "memory"); }
DI void pbar() { asm volatile("s_waitcnt vmcnt(0) lgkmcnt(0)\n\ts_barrier" ::: "memory"); }
template <int N> DI void lbar() { asm volatile("s_waitcnt vmcnt(%0)\n\ts_barrier" ::"i"(N > 63 ? 63 : N) : "memory"); }
DI void bar() { asm volatile("s_barrier" ::: "memory"); }

DI void dma4(const void* sbase, unsigned v0, unsigned v1, unsigned v2, unsigned v3, unsigned dst) {
    asm volatile("s_mov_b32 m0, %5\n\ts_nop 0\n\tglobal_load_lds_dwordx4 %0, %4 sc1\n\tglobal_load_lds_dwordx4 %1, %4 offset:1024 sc1\n\t"
                 "global_load_lds_dwordx4 %2, %4 offset:2048 sc1\n\tglobal_load_lds_dwordx4 %3, %4 offset:3072 sc1"
                 :: "v"(v0), "v"(v1), "v"(v2), "v"(v3), "s"(sbase), "s"(dst) : "memory");
}
DI void dma4lin(const void* sbase, unsigned v, unsigned dst) { dma4(sbase, v, v, v, v, dst); }
DI void dma2lin(const void* sbase, unsigned v, unsigned dst) {
    asm volatile("s_mov_b32 m0, %2\n\ts_nop 0\n\tglobal_load_lds_dwordx4 %0, %1 sc1\n\tglobal_load_lds_dwordx4 %0, %1 offset:1024 sc1" :: "v"(v), "s"(sbase), "s"(dst) : "memory");
}
DI void dma1dw(const void* sbase, unsigned v, unsigned dst) {
    asm volatile("s_mov_b32 m0, %2\n\ts_nop 0\n\tglobal_load_lds_dword %0, %1 sc1" :: "v"(v), "s"(sbase), "s"(dst) : "memory");
}
DI void gtouch(const void* sbase, unsigned voff) {
    asm volatile("global_load_lds_dword %0, %1" :: "v"(voff), "s"(sbase) : "memory");
}
DI unsigned gld32_wait(const void* sbase, unsigned voff) {
    unsigned r; asm volatile("global_load_dword %0, %1, %2 sc1\n\ts_waitcnt vmcnt(0)" : "=&v"(r) : "v"(voff), "s"(sbase) : "memory"); return r;
}
DI void gst128(void* sbase, unsigned voff, u32x4 v) { asm volatile("global_store_dwordx4 %0, %1, %2 sc1\n\ts_nop 1" ::"v"(voff), "v"(v), "s"(sbase) : "memory"); }
DI void gst64(void* sbase, unsigned voff, u32x2 v) { asm volatile("global_store_dwordx2 %0, %1, %2 sc1\n\ts_nop 1" ::"v"(voff), "v"(v), "s"(sbase) : "memory"); }
DI void gst32(void* sbase, unsigned voff, unsigned v) { asm volatile("global_store_dword %0, %1, %2 sc1\n\ts_nop 1" ::"v"(voff), "v"(v), "s"(sbase) : "memory"); }
DI unsigned rfl(unsigned x) { return __builtin_amdgcn_readfirstlane(x); }
DI unsigned cvtpk(float lo, float hi) {
    f32x2 v = {lo, hi};
    bf16x2 b = __builtin_convertvector(v, bf16x2);
    return __builtin_bit_cast(unsigned, b);
}
DI float bflo(unsigned u) { return __uint_as_float(u << 16); }
DI float bfhi(unsigned u) { return __uint_as_float(u & 0xffff0000u); }
DI bf16x8 lds_b128(unsigned a) { return *(LAS const bf16x8*)(uintptr_t)a; }
typedef short v4i16_t __attribute__((ext_vector_type(4)));
DI s16x4 lds_tr(unsigned a) { return __builtin_bit_cast(s16x4, __builtin_amdgcn_ds_read_tr16_b64_v4i16((LAS v4i16_t*)(uintptr_t)a)); }
DI bf16x8 lds_trfrag(unsigned a, int off2) {
    s16x4 x = lds_tr(a), y = lds_tr(a + off2);
    s16x8 r = __builtin_shufflevector(x, y, 0, 1, 2, 3, 4, 5, 6, 7);
    return __builtin_bit_cast(bf16x8, r);
}
DI float dot2(unsigned a, unsigned b, float c) {
    return __builtin_amdgcn_fdot2_f32_bf16(__builtin_bit_cast(bf16x2, a), __builtin_bit_cast(bf16x2, b), c, false);
}
DI float other_half(float v) {
    auto r = __builtin_amdgcn_permlane32_swap(__float_as_uint(v), __float_as_uint(v), false, false);
    return __uint_as_float((threadIdx.x & 32) ? r[0] : r[1]);
}
DI float sum_halves(float v) {
    auto r = __builtin_amdgcn_permlane32_swap(__float_as_uint(v), __float_as_uint(v), false, false);
    return __uint_as_float(r[0]) + __uint_as_float(r[1]);
}
template <bool ALL> DI float dpp_max(float v) {
#define DPPMAX(ctrl, rm) v = fmaxf(v, __int_as_float(__builtin_amdgcn_update_dpp(0, __float_as_int(v), ctrl, rm, 0xf, true)))
    DPPMAX(0x111, 0xf); DPPMAX(0x112, 0xf); DPPMAX(0x114, 0xf); DPPMAX(0x118, 0xf); DPPMAX(0x142, 0xa);
    if (ALL) DPPMAX(0x143, 0xc);
    return v;
}
template <int CTRL> DI float dpp_add(float v) { return v + __int_as_float(__builtin_amdgcn_update_dpp(0, __float_as_int(v), CTRL, 0xf, 0xf, true)); }
DI u32x4 swap4(unsigned p0, unsigned p1, unsigned p2, unsigned p3) {
    auto s0 = __builtin_amdgcn_permlane32_swap(p0, p2, false, false);
    auto s1 = __builtin_amdgcn_permlane32_swap(p1, p3, false, false);
    u32x4 r = {s0[0], s1[0], s0[1], s1[1]};
    return r;
}
DI u32x4 pack_swap(float a0, float a1, float a2, float a3, float a4, float a5, float a6, float a7) {
    return swap4(cvtpk(a0, a1), cvtpk(a2, a3), cvtpk(a4, a5), cvtpk(a6, a7));
}
constexpr unsigned ONES = 0x3f803f80u;

DI void poll(const char* fl, unsigned voff, bool active, unsigned tag) {
    for (unsigned spins = 0; spins < SPIN_CAP; ++spins) {
        unsigned f;
        asm volatile("global_load_dword %0, %1, %2 sc1\n\ts_waitcnt vmcnt(0)" : "=&v"(f) : "v"(voff), "s"(fl) : "memory");
        if (__all(!active || f == tag)) break;
        __builtin_amdgcn_s_sleep(3);
    }
}

struct Ptrs { const char *x, *wqkv, *wo, *wg, *wu, *wd; char* ws; char* out; };

constexpr int P1NB = 4;
constexpr int P1_BUF = 32768, P1_MISC = P1NB * P1_BUF;

DI void gst_granule(char* sbase, unsigned voff, float v, unsigned tag) { u32x2 g = {__float_as_uint(v), tag}; gst64(sbase, voff, g); }
DI float sum_x16(float v) {
    auto r = __builtin_amdgcn_permlane16_swap(__float_as_uint(v), __float_as_uint(v), false, false);
    return __uint_as_float(r[0]) + __uint_as_float(r[1]);
}
template <bool ISV>
DI void p1_epilogue(char* ws, int tb, int cb, char* smem, int tl, int cp, const f32x16& accA, const f32x16& accB, int lane) {
    const int r = lane & 31, hh = lane >> 5;
    const LAS f32x2* st = (const LAS f32x2*)(smem + P1_MISC);
    const LAS float* csl = (const LAS float*)(smem + P1_MISC + 1024);
    const int sec = cb >> 2, tile = tb * 4 + tl;
#pragma unroll
    for (int b = 0; b < 2; ++b) {
        const f32x16& acc = b ? accB : accA;
        const int ct = 2 * cp + b, head0 = (cb & 3) * 8 + ct * 2;
        float v[16];
        if constexpr (!ISV) {
            const f32x2 ms = st[tl * 32 + r];
            const float sc = ms[1] * (sec == 0 ? QSCALE : 1.f), msc = -ms[0] * sc;
#pragma unroll
            for (int g = 0; g < 4; ++g) {
                const f32x4 c4 = *(LAS const f32x4*)(csl + ct * 32 + 8 * g + 4 * hh);
#pragma unroll
                for (int e = 0; e < 4; ++e) v[4 * g + e] = acc[4 * g + e] * sc + msc * c4[e];
            }
            char* dst = ws + (sec == 0 ? WS_QF : WS_KF) + ((size_t)head0 * 64 + tile) * 1024;
#pragma unroll
            for (int hs = 0; hs < 2; ++hs) {
                const u32x4 o = pack_swap(v[8 * hs], v[8 * hs + 1], v[8 * hs + 2], v[8 * hs + 3], v[8 * hs + 4], v[8 * hs + 5], v[8 * hs + 6], v[8 * hs + 7]);
                gst128(dst + hs * 65536, lane * 16, o);
                if (sec == 1) {
                    float n2 = 0.f;
#pragma unroll
                    for (int e = 0; e < 8; ++e) n2 = fmaf(v[8 * hs + e], v[8 * hs + e], n2);
                    n2 = dpp_max<false>(sum_halves(n2));
                    if (lane == 31) gst32(ws + WS_KM + (head0 + hs) * 256 + tile * 4, 0, __float_as_uint(n2));
                }
            }
        } else {
            const float csn = csl[ct * 32 + r];
#pragma unroll
            for (int g = 0; g < 4; ++g) {
                const LAS f32x4* sp = (const LAS f32x4*)(st + tl * 32 + 8 * g + 4 * hh);
                const f32x4 pa = sp[0], pb = sp[1];
                v[4 * g] = (acc[4 * g] - pa[0] * csn) * pa[1]; v[4 * g + 1] = (acc[4 * g + 1] - pa[2] * csn) * pa[3];
                v[4 * g + 2] = (acc[4 * g + 2] - pb[0] * csn) * pb[1]; v[4 * g + 3] = (acc[4 * g + 3] - pb[2] * csn) * pb[3];
            }
            char* dst = ws + WS_VF + ((size_t)head0 * 64 + tile) * 1024;
            const unsigned vo = (r >> 4) * 65536 + hh * 256 + (r & 15) * 16;
#pragma unroll
            for (int s = 0; s < 2; ++s) {
                u32x4 o = {cvtpk(v[8 * s], v[8 * s + 1]), cvtpk(v[8 * s + 2], v[8 * s + 3]), cvtpk(v[8 * s + 4], v[8 * s + 5]), cvtpk(v[8 * s + 6], v[8 * s + 7])};
                gst128(dst + s * 512, vo, o);
            }
        }
    }
}
constexpr int P1_XCH = (5 % P1NB) * P1_BUF;

DI void p1_colsum(const char* wqkv, char* ws, int tb, int cb, char* smem, int j, int lane, f32x16& accA, f32x16& accB) {
    const unsigned tag = __hip_atomic_load((const __attribute__((address_space(1))) unsigned*)(ws + WS_EPOCH), __ATOMIC_RELAXED, __HIP_MEMORY_SCOPE_AGENT) + 1;
    const char* src = wqkv + (size_t)((tb & 3) * 128 + j * 32 + (lane >> 4)) * 3072 + cb * 256 + (lane & 15) * 16;
    u32x4 v[8];
#pragma unroll
    for (int i = 0; i < 8; ++i) v[i] = *(const u32x4*)(src + (size_t)i * 4 * 3072);
    cbar(); cbar();
    float s[8] = {};
#pragma unroll
    for (int i = 0; i < 8; ++i)
#pragma unroll
        for (int e = 0; e < 4; ++e) { s[2 * e] += bflo(v[i][e]); s[2 * e + 1] += bfhi(v[i][e]); }
#pragma unroll
    for (int e = 0; e < 8; ++e) s[e] = sum_halves(sum_x16(s[e]));
    LAS float* part = (LAS float*)(smem + P1_MISC + 2048);
    if (lane < 16) {
        f32x4 a = {s[0], s[1], s[2], s[3]}, c = {s[4], s[5], s[6], s[7]};
        *(LAS f32x4*)(part + j * 128 + lane * 8) = a; *(LAS f32x4*)(part + j * 128 + lane * 8 + 4) = c;
    }
    cbar();
    if (j == 0) {
#pragma unroll
        for (int k = 0; k < 2; ++k) {
            const int c = lane + 64 * k;
            gst_granule(ws + WS_G2, ((cb * 128 + c) * 4 + (tb & 3)) * 8, part[c] + part[128 + c] + part[256 + c] + part[384 + c], tag);
        }
    }
    cbar(); cbar(); cbar(); cbar(); cbar();
    cbar();
    f32x16 acc[2];
    {
        const LAS f32x4* xr = (const LAS f32x4*)(smem + P1_XCH + j * 8192 + lane * 16);
#pragma unroll
        for (int b = 0; b < 2; ++b)
#pragma unroll
            for (int g = 0; g < 4; ++g) { const f32x4 t = xr[(b * 4 + g) * 64]; acc[b][4 * g] = t[0]; acc[b][4 * g + 1] = t[1]; acc[b][4 * g + 2] = t[2]; acc[b][4 * g + 3] = t[3]; }
    }
    accA = acc[0]; accB = acc[1];
}

template <bool ISV>
DI void p1_compute(char* ws, int tb, int cb, char* smem, int w, int lane, f32x16& accA, f32x16& accB) {
    const int r = lane & 31, hh = lane >> 5;
    const unsigned lds0 = (unsigned)(uintptr_t)smem;
    const int tp = w & 1, cp = w >> 1;
    unsigned x0[4];
    {
        const int f = (r >> 1) & 7;
#pragma unroll
        for (int ks = 0; ks < 4; ++ks) x0[ks] = lds0 + (64 * tp + r) * 128 + (((2 * ks + hh) ^ f) << 4);
    }
    const unsigned wro = lds0 + 16384 + hh * 2048 + ((lane & 15) >> 2) * 64 + ((lane >> 4) & 1) * 32 + (lane & 3) * 8 + cp * 512;
    f32x16 acc[2][2] = {};
    bf16x8 X[4][2], W[4][2];
#define P1_LOAD(S, B, ks) { X[S][0] = lds_b128(x0[ks] + (B)); X[S][1] = lds_b128(x0[ks] + (B) + 4096); W[S][0] = lds_trfrag(wro + (B) + (ks) * 4096, 1024); W[S][1] = lds_trfrag(wro + (B) + (ks) * 4096 + 256, 1024); }
#define P1_MMA(S)                                                                                                                 \
    {                                                                                                                             \
        _Pragma("unroll") for (int a = 0; a < 2; ++a) _Pragma("unroll") for (int b = 0; b < 2; ++b)                                \
            acc[a][b] = ISV ? MFMA(X[S][a], W[S][b], acc[a][b]) : MFMA(W[S][b], X[S][a], acc[a][b]);                               \
    }
#define P1_ILV() { __builtin_amdgcn_sched_group_barrier(0x8, 1, 0); __builtin_amdgcn_sched_group_barrier(0x100, 2, 0); __builtin_amdgcn_sched_group_barrier(0x8, 1, 0); __builtin_amdgcn_sched_group_barrier(0x100, 2, 0); \
                   __builtin_amdgcn_sched_group_barrier(0x8, 1, 0); __builtin_amdgcn_sched_group_barrier(0x100, 2, 0); __builtin_amdgcn_sched_group_barrier(0x8, 1, 0); SB(); }
    cbar();
    P1_LOAD(0, 0, 0); P1_LOAD(1, 0, 1);
    SB();
    unsigned B = 0;
#pragma unroll 1
    for (int ch = 0; ch < 7; ++ch) {
        P1_LOAD(2, B, 2); P1_MMA(0); P1_ILV();
        P1_LOAD(3, B, 3); P1_MMA(1); P1_ILV();
        cbar();
        B = B == (P1NB - 1) * P1_BUF ? 0 : B + P1_BUF;
        P1_LOAD(0, B, 0); P1_MMA(2); P1_ILV();
        P1_LOAD(1, B, 1); P1_MMA(3); P1_ILV();
    }
    P1_LOAD(2, B, 2); P1_MMA(0); P1_ILV();
    P1_LOAD(3, B, 3); P1_MMA(1); P1_ILV();
    P1_MMA(2); P1_MMA(3);
    {
        LAS f32x4* xw = (LAS f32x4*)(smem + P1_XCH + w * 8192 + lane * 16);
#pragma unroll
        for (int b = 0; b < 2; ++b)
#pragma unroll
            for (int g = 0; g < 4; ++g) { f32x4 t = {acc[1][b][4 * g], acc[1][b][4 * g + 1], acc[1][b][4 * g + 2], acc[1][b][4 * g + 3]}; xw[(b * 4 + g) * 64] = t; }
    }
    cbar();
    accA = acc[0][0]; accB = acc[0][1];
}

typedef const __attribute__((address_space(1))) char* gcptr;
template <int NROW16>
DI void colsum32(gcptr W, unsigned rs, int lane, float (&s)[8]) {
    const unsigned vo = (lane & 15) * rs + (lane >> 4) * 16;
    u32x4 v[NROW16];
#pragma unroll
    for (int i = 0; i < NROW16; ++i) v[i] = *(const __attribute__((address_space(1))) u32x4*)(W + (size_t)i * 16 * rs + vo);
    __builtin_amdgcn_sched_barrier(0);
#pragma unroll
    for (int e = 0; e < 8; ++e) s[e] = 0.f;
#pragma unroll
    for (int i = 0; i < NROW16; ++i)
#pragma unroll
        for (int e = 0; e < 4; ++e) { s[2 * e] += bflo(v[i][e]); s[2 * e + 1] += bfhi(v[i][e]); }
#pragma unroll
    for (int e = 0; e < 8; ++e) s[e] = dpp_add<0x118>(dpp_add<0x114>(dpp_add<0x112>(dpp_add<0x111>(s[e]))));
}
DI void p1_helper(const char* px, const char* pqkv, const char* pg, const char* pu, char* ws, int h, int w, int lane) {
    if (w < 4) {
        const unsigned tag = __hip_atomic_load((const __attribute__((address_space(1))) unsigned*)(ws + WS_EPOCH), __ATOMIC_RELAXED, __HIP_MEMORY_SCOPE_AGENT) + 1;
        const int u = w * 64 + h;
        if (u < 256) {
            const int rl = lane >> 3, seg = lane & 7;
            const char* src = px + (size_t)(u * 8 + rl) * 1024 + seg * 16;
            u32x4 v[8];
#pragma unroll
            for (int i = 0; i < 8; ++i) v[i] = *(const u32x4*)(src + i * 128);
            float s1 = 0.f, s2 = 0.f;
#pragma unroll
            for (int i = 0; i < 8; ++i)
#pragma unroll
                for (int e = 0; e < 4; ++e) { s1 = dot2(v[i][e], ONES, s1); s2 = dot2(v[i][e], v[i][e], s2); }
            s1 = dpp_add<0x114>(dpp_add<0x112>(dpp_add<0x111>(s1))); s2 = dpp_add<0x114>(dpp_add<0x112>(dpp_add<0x111>(s2)));
            const float mean = s1 * (1.f / DM);
            const float rstd = __builtin_amdgcn_rsqf(fmaxf(s2 * (1.f / DM) - mean * mean, 0.f) + LN_EPS);
            if (seg == 7) { gst_granule(ws + WS_G1, (u * 8 + rl) * 16, mean, tag); gst_granule(ws + WS_G1, (u * 8 + rl) * 16 + 8, rstd, tag); }
        }
    }
    const int job = w == 4 ? h : (w == 5 && h < 22) ? 64 + h : -1;
    if (job >= 0) {
        const int blk = job < 43 ? job : job - 43;
        const uintptr_t wa = (uintptr_t)pg + (job < 43 ? 0 : (uintptr_t)pu - (uintptr_t)pg);
        float s[8];
        colsum32<32>((gcptr)wa + blk * 64, FF * 2, lane, s);
        if ((lane & 15) == 15) {
            char* dst = ws + WS_CS + (job < 43 ? 0 : FF) * 4;
            const int c0 = blk * 32 + (lane >> 4) * 8;
            u32x4 a = {__float_as_uint(s[0]), __float_as_uint(s[1]), __float_as_uint(s[2]), __float_as_uint(s[3])};
            u32x4 c = {__float_as_uint(s[4]), __float_as_uint(s[5]), __float_as_uint(s[6]), __float_as_uint(s[7])};
            gst128(dst, c0 * 4, a); gst128(dst, c0 * 4 + 16, c);
        }
    }
}

DI void small_touch(char* ws, int b, int lane, unsigned lds0) {
    unsigned o_ = (unsigned)WS_FLAG + (lane & 3) * 256 * 128 + b * 128;
    if (lane == 4) o_ = (unsigned)WS_G1 + b * 128;
    if (lane >= 5 && lane < 9) o_ = (unsigned)WS_ST + (b * 4 + lane - 5) * 128;
    if (lane == 9) o_ = (unsigned)WS_KM + (b & 63) * 128;
    if (lane == 10) o_ = (unsigned)WS_CS + (b < 86 ? b : 0) * 128;
    if (lane >= 11 && lane < 17) o_ = (unsigned)WS_G2 + (b % 64) * 768 + (lane - 11) * 128;
    asm volatile("s_mov_b32 m0, %0\n\ts_nop 0\n\tglobal_load_lds_dword %1, %2 sc1" ::"s"(lds0 + LDS_BYTES - 256), "v"(o_), "s"(ws) : "memory");
}

DI unsigned p1_load(const Ptrs& P, int L, int tb, int cb, unsigned lds0, int lane) {
    unsigned vo[4];
    const char* base;
    unsigned cstep, gstep;
    if (L == 0) {
        const int rl = lane >> 3, cs = lane & 7;
#pragma unroll
        for (int i = 0; i < 4; ++i) vo[i] = (8 * i + rl) * 1024 + ((cs ^ ((4 * (i & 1) + (rl >> 1)) & 7)) << 4) - i * 1024;
        base = P.x + (size_t)tb * 128 * 1024; cstep = 128; gstep = 4 * 8192;
    } else {
        const int nb = lane >> 4, q = (lane >> 2) & 3, c = lane & 3;
#pragma unroll
        for (int i = 0; i < 4; ++i) vo[i] = (4 * i + q) * 3072 + nb * 64 + c * 16 - i * 1024;
        base = P.wqkv + cb * 256; cstep = 64 * 3072; gstep = 16 * 3072;
    }
    auto issue = [&](int c, unsigned B) {
        const char* src = base + (size_t)c * cstep;
        const unsigned dst = lds0 + B + L * 16384;
#pragma unroll
        for (int g = 0; g < 4; ++g) dma4(src + (size_t)g * gstep, vo[0], vo[1], vo[2], vo[3], dst + g * 4096);
    };
    issue(0, 0);
#pragma unroll
    for (int c = 1; c < P1NB - 1; ++c) issue(c, c * P1_BUF);
    if (L == 0) small_touch(P.ws, blockIdx.x, lane, lds0);
    unsigned B = (P1NB - 1) * P1_BUF;
    constexpr int LA = P1NB - 1;
#pragma unroll 1
    for (int c = 0; c < 8; ++c) {
        const int y = 7 - c < LA - 1 ? 7 - c : LA - 1;
        if (y == 3) lbar<48>(); else if (y == 2) lbar<32>(); else if (y == 1) lbar<16>(); else lbar<0>();
        if (c + LA < 8) { issue(c + LA, B); B = B == (P1NB - 1) * P1_BUF ? 0 : B + P1_BUF; }
    }
    const unsigned tag = rfl(gld32_wait(P.ws + WS_EPOCH, 0)) + 1;
    if (L == 1) {
        u32x4 a, b, c0, c1, c2, c3;
        for (unsigned spins = 0; spins < SPIN_CAP; ++spins) {
            asm volatile("global_load_dwordx4 %0, %6, %7 sc1\n\tglobal_load_dwordx4 %1, %6, %7 offset:16 sc1\n\t"
                         "global_load_dwordx4 %2, %8, %9 sc1\n\tglobal_load_dwordx4 %3, %8, %9 offset:16 sc1\n\t"
                         "global_load_dwordx4 %4, %8, %9 offset:32 sc1\n\tglobal_load_dwordx4 %5, %8, %9 offset:48 sc1\n\ts_waitcnt vmcnt(0)"
                         : "=&v"(a), "=&v"(b), "=&v"(c0), "=&v"(c1), "=&v"(c2), "=&v"(c3)
                         : "v"(lane * 32), "s"(P.ws + WS_G1 + tb * 128 * 16), "v"(lane * 64), "s"(P.ws + WS_G2 + cb * 128 * 32) : "memory");
            if (__all(a[1] == tag && a[3] == tag && b[1] == tag && b[3] == tag && c0[1] == tag && c0[3] == tag && c1[1] == tag && c1[3] == tag &&
                      c2[1] == tag && c2[3] == tag && c3[1] == tag && c3[3] == tag)) break;
            __builtin_amdgcn_s_sleep(1);
        }
        u32x4 ms = {a[0], a[2], b[0], b[2]};
        u32x2 cs = {__float_as_uint(__uint_as_float(c0[0]) + __uint_as_float(c0[2]) + __uint_as_float(c1[0]) + __uint_as_float(c1[2])),
                    __float_as_uint(__uint_as_float(c2[0]) + __uint_as_float(c2[2]) + __uint_as_float(c3[0]) + __uint_as_float(c3[2]))};
        *(LAS u32x4*)(uintptr_t)(lds0 + P1_MISC + lane * 16) = ms;
        *(LAS u32x2*)(uintptr_t)(lds0 + P1_MISC + 1024 + lane * 8) = cs;
        asm volatile("s_waitcnt lgkmcnt(0)" ::: "memory");
    }
    bar();
    return tag;
}

constexpr int P2_X = 131072 + 2048;
constexpr int P2_KM = P2_X + 8 * 2560;
constexpr float SAFE_RANGE = 80.f;

DI void p2_compute(char* ws, char* smem, int head, int qb, int w, int lane) {
    const int r = lane & 31, hh = lane >> 5;
    const unsigned lds0 = (unsigned)(uintptr_t)smem;
    const int qg = w & 3, kh = w >> 2;
    const int la = qg * 2 + kh, lb = qg * 2 + (1 - kh);
    cbar();
    const u32x4 qa = *(LAS const u32x4*)(uintptr_t)(lds0 + P2_X + la * 1024 + lane * 16), qbv = *(LAS const u32x4*)(uintptr_t)(lds0 + P2_X + lb * 1024 + lane * 16);
    const float kmax2 = __int_as_float(__builtin_amdgcn_readlane(__float_as_int(dpp_max<true>(*(LAS const float*)(uintptr_t)(lds0 + P2_KM + lane * 4))), 63));
    float qa2 = 0.f, qb2 = 0.f;
#pragma unroll
    for (int e = 0; e < 4; ++e) { qa2 = dot2(qa[e], qa[e], qa2); qb2 = dot2(qbv[e], qbv[e], qb2); }
    const float bnda = __builtin_amdgcn_sqrtf(sum_halves(qa2) * kmax2) * 1.01f, bndb = __builtin_amdgcn_sqrtf(sum_halves(qb2) * kmax2) * 1.01f;
    const bool fast = __all(2.f * bnda <= SAFE_RANGE && 2.f * bndb <= SAFE_RANGE);
    const unsigned ka = lds0 + kh * 32768 + lane * 16, va = lds0 + 65536 + kh * 32768 + hh * 256 + (r & 15) * 16;
    const unsigned xw = lds0 + P2_X + w * 2560 + lane * 16, xr = lds0 + P2_X + (w ^ 4) * 2560 + lane * 16;
    float o[9], ma;
    if (fast) {
        ma = bnda;
        unsigned kp = ka, vp = va, cnt;
#define EXP(r) "v_exp_f32 v" #r ", v" #r "\n\t"
#define CVT(d, a, b) "v_cvt_pk_bf16_f32 v" #d ", v" #a ", v" #b "\n\t"
#define MSQ(SN, KN, Q, C) "v_mfma_f32_32x32x16_bf16 " SN ", " KN ", " Q ", " C "\n\t"
#define MPO(O, V, P) "v_mfma_f32_32x32x16_bf16 " O ", " V ", " P ", " O "\n\t"
#define RDV(VN0, VN1, VOFF) "s_mov_b64 exec, %[msk]\n\t" "ds_read_b128 " VN0 ", %[vp] offset:" #VOFF "\n\t" "ds_read_b128 " VN1 ", %[vp] offset:" #VOFF "+512\n\t" "s_mov_b64 exec, -1\n\t"
#define RDK(KNN, KOFF) "ds_read_b128 " KNN ", %[kp] offset:" #KOFF "\n\t"
#define STEP(PRE, c0, c1, c2, c3, c4, c5, c6, c7, c8, c9, c10, c11, c12, c13, c14, c15, p14, p15, SN, KN, Q, CC, O, OP, VC0, VP1)  \
        EXP(c0) EXP(c1) CVT(127, p14, p15) EXP(c2) EXP(c3) MPO(OP, VP1, "v[124:127]") PRE                                    \
        CVT(120, c0, c1) EXP(c4) EXP(c5) CVT(121, c2, c3) EXP(c6)                                                            \
        "s_waitcnt lgkmcnt(3)\n\t" MSQ(SN, KN, Q, CC)                                                                        \
        EXP(c7) CVT(122, c4, c5) EXP(c8) EXP(c9) CVT(123, c6, c7) EXP(c10) EXP(c11)                                          \
        MPO(O, VC0, "v[120:123]")                                                                                            \
        CVT(124, c8, c9) EXP(c12) EXP(c13) CVT(125, c10, c11) EXP(c14) EXP(c15) CVT(126, c12, c13)
#define SA_ 32, 33, 34, 35, 36, 37, 38, 39, 40, 41, 42, 43, 44, 45, 46, 47
#define SB_ 48, 49, 50, 51, 52, 53, 54, 55, 56, 57, 58, 59, 60, 61, 62, 63
#define CALL(M, ...) M(__VA_ARGS__)
#define TE(KOFF, VOFF) CALL(STEP, RDV("v[112:115]", "v[116:119]", VOFF), SA_, 62, 63, "v[48:63]", "v[96:99]", "%[qb]", "v[144:159]", "v[64:79]", "v[80:95]", "v[104:107]", "v[116:119]") \
                       CALL(STEP, RDK("v[96:99]", KOFF), SB_, 46, 47, "v[32:47]", "v[100:103]", "%[qa]", "v[128:143]", "v[80:95]", "v[64:79]", "v[104:107]", "v[108:111]")
#define TO(KOFF, VOFF) CALL(STEP, RDV("v[104:107]", "v[108:111]", VOFF), SA_, 62, 63, "v[48:63]", "v[100:103]", "%[qb]", "v[144:159]", "v[64:79]", "v[80:95]", "v[112:115]", "v[108:111]") \
                       CALL(STEP, RDK("v[100:103]", KOFF), SB_, 46, 47, "v[32:47]", "v[96:99]", "%[qa]", "v[128:143]", "v[80:95]", "v[64:79]", "v[112:115]", "v[116:119]")
#define M64(a, b, c, d) "v_mov_b64 v[" #a ":" #b "], v[" #c ":" #d "]\n\t"
#define MOV4(a, b, c, d, src) "v_mov_b32 v" #a ", " src "\n\tv_mov_b32 v" #b ", " src "\n\tv_mov_b32 v" #c ", " src "\n\tv_mov_b32 v" #d ", " src "\n\t"
        asm volatile(
            "s_setprio 3\n\t"
            "ds_read_b128 v[96:99], %[kp]\n\t"
            "v_mov_b32 v128, %[na]\n\tv_mov_b32 v129, %[na]\n\tv_mov_b32 v144, %[nb]\n\tv_mov_b32 v145, %[nb]\n\t"
            "v_mov_b32 v104, %[one]\n\tv_mov_b32 v105, %[one]\n\tv_mov_b64 v[64:65], 0\n\t"
            M64(106, 107, 104, 105) M64(108, 109, 104, 105) M64(110, 111, 104, 105) M64(112, 113, 104, 105) M64(114, 115, 104, 105) M64(116, 117, 104, 105) M64(118, 119, 104, 105)
            "s_mov_b64 exec, %[msk]\n\t"
            "ds_read_b128 v[104:107], %[vp]\n\t"
            "ds_read_b128 v[108:111], %[vp] offset:512\n\t"
            "s_mov_b64 exec, -1\n\t"
            "ds_read_b128 v[100:103], %[kp] offset:1024\n\t"
            M64(130, 131, 128, 129) M64(132, 133, 128, 129) M64(134, 135, 128, 129) M64(136, 137, 128, 129) M64(138, 139, 128, 129) M64(140, 141, 128, 129) M64(142, 143, 128, 129)
            "s_waitcnt lgkmcnt(3)\n\t"
            "s_nop 1\n\t"
            "v_mfma_f32_32x32x16_bf16 v[32:47], v[96:99], %[qa], v[128:143]\n\t"
            M64(146, 147, 144, 145) M64(148, 149, 144, 145) M64(150, 151, 144, 145) M64(152, 153, 144, 145) M64(154, 155, 144, 145) M64(156, 157, 144, 145) M64(158, 159, 144, 145)
            M64(66, 67, 64, 65) M64(68, 69, 64, 65) M64(70, 71, 64, 65) M64(72, 73, 64, 65) M64(74, 75, 64, 65) M64(76, 77, 64, 65) M64(78, 79, 64, 65)
            M64(80, 81, 64, 65) M64(82, 83, 64, 65) M64(84, 85, 64, 65) M64(86, 87, 64, 65) M64(88, 89, 64, 65) M64(90, 91, 64, 65) M64(92, 93, 64, 65) M64(94, 95, 64, 65)
            M64(62, 63, 64, 65) M64(124, 125, 64, 65) M64(126, 127, 64, 65)
            "s_mov_b32 %[cnt], 0\n\t"
            "L_att_%=:\n\t"
            TE(2048, 1024) TO(3072, 2048) TE(4096, 3072) TO(5120, 4096)
            "v_add_u32 %[kp], 0x1000, %[kp]\n\t"
            "v_add_u32 %[vp], 0x1000, %[vp]\n\t"
            "s_cmp_lg_u32 %[cnt], 0\n\t"
            "s_cbranch_scc1 L_att_nb_%=\n\t"
            "s_barrier\n\t"
            "L_att_nb_%=:\n\t"
            "s_add_u32 %[cnt], %[cnt], 1\n\t"
            "s_cmp_eq_u32 %[cnt], 3\n\ts_cbranch_scc0 L_p1_%=\n\ts_setprio 2\n\tL_p1_%=:\n\t"
            "s_cmp_eq_u32 %[cnt], 6\n\ts_cbranch_scc0 L_p2_%=\n\ts_setprio 1\n\tL_p2_%=:\n\t"
            "s_cmp_eq_u32 %[cnt], 7\n\ts_cbranch_scc0 L_p3_%=\n\ts_setprio 0\n\tL_p3_%=:\n\t"
            "s_cmp_lt_u32 %[cnt], 8\n\t"
            "s_cbranch_scc1 L_att_%=\n\t"
            CVT(127, 62, 63)
            "s_nop 1\n\t"
            MPO("v[80:95]", "v[116:119]", "v[124:127]")
            "s_waitcnt lgkmcnt(0)\n\t"
            "s_nop 15\n\t"
            "v_mov_b32 v89, %[bb]\n\t"
            "v_mov_b32 %[o0], v64\n\tv_mov_b32 %[o1], v65\n\tv_mov_b32 %[o2], v66\n\tv_mov_b32 %[o3], v67\n\t"
            "v_mov_b32 %[o4], v68\n\tv_mov_b32 %[o5], v69\n\tv_mov_b32 %[o6], v70\n\tv_mov_b32 %[o7], v71\n\tv_mov_b32 %[o8], v72\n\t"
            "ds_write_b128 %[xw], v[80:83]\n\t"
            "ds_write_b128 %[xw], v[84:87] offset:1024\n\t"
            "ds_write_b64 %[xw2], v[88:89]\n\t"
            : [o0] "=&v"(o[0]), [o1] "=&v"(o[1]), [o2] "=&v"(o[2]), [o3] "=&v"(o[3]), [o4] "=&v"(o[4]), [o5] "=&v"(o[5]), [o6] "=&v"(o[6]), [o7] "=&v"(o[7]), [o8] "=&v"(o[8]),
              [kp] "+v"(kp), [vp] "+v"(vp), [cnt] "=&s"(cnt)
            : [qa] "v"(qa), [qb] "v"(qbv), [na] "v"(-bnda), [nb] "v"(-bndb), [bb] "v"(bndb), [msk] "s"(0x0000ffff0000ffffull), [xw] "v"(xw), [xw2] "v"(xw + 2048 - lane * 8), [one] "v"((r == 16 || r == 20) ? ONES : 0u)
            : "memory", "scc",
              "v32", "v33", "v34", "v35", "v36", "v37", "v38", "v39", "v40", "v41", "v42", "v43", "v44", "v45", "v46", "v47",
              "v48", "v49", "v50", "v51", "v52", "v53", "v54", "v55", "v56", "v57", "v58", "v59", "v60", "v61", "v62", "v63",
              "v64", "v65", "v66", "v67", "v68", "v69", "v70", "v71", "v72", "v73", "v74", "v75", "v76", "v77", "v78", "v79",
              "v80", "v81", "v82", "v83", "v84", "v85", "v86", "v87", "v88", "v89", "v90", "v91", "v92", "v93", "v94", "v95",
              "v96", "v97", "v98", "v99", "v100", "v101", "v102", "v103", "v104", "v105", "v106", "v107", "v108", "v109", "v110", "v111",
              "v112", "v113", "v114", "v115", "v116", "v117", "v118", "v119", "v120", "v121", "v122", "v123", "v124", "v125", "v126", "v127",
              "v128", "v129", "v130", "v131", "v132", "v133", "v134", "v135", "v136", "v137", "v138", "v139", "v140", "v141", "v142", "v143",
              "v144", "v145", "v146", "v147", "v148", "v149", "v150", "v151", "v152", "v153", "v154", "v155", "v156", "v157", "v158", "v159");
    } else {
        bar();
        const u32x4 ones = {ONES, ONES, ONES, ONES};
        const f32x16 zero = {};
#pragma unroll 1
        for (int sl = 1; sl >= 0; --sl) {
            const bf16x8 qf = __builtin_bit_cast(bf16x8, sl ? qbv : qa);
            bf16x8 v0 = __builtin_bit_cast(bf16x8, ones), v1 = v0, kf;
            f32x16 oo = {};
            float m = -INFINITY;
#pragma unroll 1
            for (int t = 0; t < 32; ++t) {
                asm volatile("ds_read_b128 %0, %3\n\ts_mov_b64 exec, %5\n\tds_read_b128 %1, %4\n\tds_read_b128 %2, %4 offset:512\n\ts_mov_b64 exec, -1\n\ts_waitcnt lgkmcnt(0)"
                             : "=&v"(kf), "+v"(v0), "+v"(v1) : "v"(ka + t * 1024), "v"(va + t * 1024), "s"(0x0000ffff0000ffffull) : "memory");
                const f32x16 s = MFMA(kf, qf, zero);
                float tm = s[0];
#pragma unroll
                for (int i = 1; i < 16; ++i) tm = fmaxf(tm, s[i]);
                tm = fmaxf(tm, other_half(tm));
                const float mn = fmaxf(m, tm);
                const float alpha = __builtin_amdgcn_exp2f(m - mn);
                m = mn;
#pragma unroll
                for (int i = 0; i < 16; ++i) oo[i] *= alpha;
                u32x4 p0, p1;
#pragma unroll
                for (int i = 0; i < 4; ++i) p0[i] = cvtpk(__builtin_amdgcn_exp2f(s[2 * i] - m), __builtin_amdgcn_exp2f(s[2 * i + 1] - m));
#pragma unroll
                for (int i = 0; i < 4; ++i) p1[i] = cvtpk(__builtin_amdgcn_exp2f(s[8 + 2 * i] - m), __builtin_amdgcn_exp2f(s[9 + 2 * i] - m));
                oo = MFMA(v0, __builtin_bit_cast(bf16x8, p0), oo);
                oo = MFMA(v1, __builtin_bit_cast(bf16x8, p1), oo);
            }
            if (sl == 1) {
                f32x4 t0 = {oo[0], oo[1], oo[2], oo[3]}, t1 = {oo[4], oo[5], oo[6], oo[7]};
                f32x2 t2 = {oo[8], m};
                *(LAS f32x4*)(uintptr_t)xw = t0; *(LAS f32x4*)(uintptr_t)(xw + 1024) = t1; *(LAS f32x2*)(uintptr_t)(xw + 2048 - lane * 8) = t2;
            } else {
#pragma unroll
                for (int i = 0; i < 9; ++i) o[i] = oo[i];
                ma = m;
            }
        }
    }
    cbar();
    {
        const f32x4 t0 = *(LAS const f32x4*)(uintptr_t)xr, t1 = *(LAS const f32x4*)(uintptr_t)(xr + 1024);
        const f32x2 t2 = *(LAS const f32x2*)(uintptr_t)(xr + 2048 - lane * 8);
        const float mo = t2[1], mn = fmaxf(ma, mo);
        const float fa = __builtin_amdgcn_exp2f(ma - mn), fo = __builtin_amdgcn_exp2f(mo - mn);
        const float inv = __builtin_amdgcn_rcpf(o[8] * fa + t2[0] * fo);
        const float ia = fa * inv, io = fo * inv;
        const u32x4 ov = pack_swap(o[0] * ia + t0[0] * io, o[1] * ia + t0[1] * io, o[2] * ia + t0[2] * io, o[3] * ia + t0[3] * io,
                                   o[4] * ia + t1[0] * io, o[5] * ia + t1[1] * io, o[6] * ia + t1[2] * io, o[7] * ia + t1[3] * io);
        gst128(ws + WS_OF + ((size_t)(qb * 8 + la) * 32 + head) * 1024, lane * 16, ov);
    }
}

constexpr int P3_X = 153600;
template <int MODE>
DI void p35_compute(const Ptrs& P, char* smem, int tb, int cb, int w, int lane, u32x4& hkeep) {
    constexpr int A0 = MODE == 0 ? 65536 : 98304, XA = MODE == 0 ? 131072 : 147456;
    char* ws = P.ws;
    const int r = lane & 31, hh = lane >> 5;
    const unsigned lds0 = (unsigned)(uintptr_t)smem;
    const int ta = w & 1, cj = (w >> 1) & 1, kh = w >> 2;
    const int tile = tb * 2 + ta, token = tile * 32 + r, n0 = cb * 64 + cj * 32 + kh * 16;
    u32x2 res[2];
    const unsigned aro = lds0 + A0 + ta * 8192 + lane * 16 + kh * 4096;
    const unsigned wro = lds0 + hh * 1024 + ((lane & 15) >> 2) * 64 + ((lane >> 4) & 1) * 32 + (lane & 3) * 8 + cj * 256 + kh * 8192;
    f32x16 acc = {};
    bf16x8 A[4], Wf[4];
#define P35_LD(S, BA, BW, ks) { A[S] = lds_b128(aro + (BA) + (ks) * 1024); Wf[S] = lds_trfrag(wro + (BW) + (ks) * 2048, 512); }
    if constexpr (MODE == 0) {
        cbar();
        P35_LD(0, 0, 0, 0); P35_LD(1, 0, 0, 1);
        unsigned BA = 0;
#pragma unroll 1
        for (int ch = 0; ch < 4; ++ch) {
            P35_LD(2, BA, BA, 2); SB(); acc = MFMA(Wf[0], A[0], acc); SB();
            P35_LD(3, BA, BA, 3); SB(); acc = MFMA(Wf[1], A[1], acc); SB();
            if (ch < 3) {
                cbar();
                BA += 16384;
                P35_LD(0, BA, BA, 0);
            }
            SB(); acc = MFMA(Wf[2], A[2], acc); SB();
            if (ch < 3) { P35_LD(1, BA, BA, 1); }
            SB(); acc = MFMA(Wf[3], A[3], acc); SB();
        }
    } else {
        bf16x8 WR[19];
        bar();
#pragma unroll
        for (int q = 0; q < 16; ++q) WR[q] = lds_trfrag(wro + (q >> 2) * 16384 + (q & 3) * 2048, 512);
#pragma unroll
        for (int j = 0; j < 3; ++j) WR[16 + j] = lds_trfrag(wro + 4 * 16384 - kh * 8192 + (kh * 3 + j) * 2048, 512);
        cbar();
        cbar();
        P35_LD(0, 0, 0, 0); P35_LD(1, 0, 0, 1);
        unsigned BA = 0, BW = 0;
#define P5_ADV() { BA = BA == 2 * 16384 ? 0 : BA + 16384; }
#define P5_LDA(S, ks) { A[S] = lds_b128(aro + BA + (ks) * 1024); }
#pragma unroll 1
        for (int ch = 0; ch < 5; ++ch) {
            P35_LD(2, BA, BW, 2); SB(); acc = MFMA(Wf[0], A[0], acc); SB();
            P35_LD(3, BA, BW, 3); SB(); acc = MFMA(Wf[1], A[1], acc); SB();
            cbar();
            P5_ADV(); BW += 16384;
            P35_LD(0, BA, BW, 0); SB(); acc = MFMA(Wf[2], A[2], acc); SB();
            P35_LD(1, BA, BW, 1); SB(); acc = MFMA(Wf[3], A[3], acc); SB();
        }
        P35_LD(2, BA, BW, 2); SB(); acc = MFMA(Wf[0], A[0], acc); SB();
        P35_LD(3, BA, BW, 3); SB(); acc = MFMA(Wf[1], A[1], acc); SB();
        cbar();
        P5_ADV();
        P5_LDA(0, 0); SB(); acc = MFMA(Wf[2], A[2], acc); SB();
        P5_LDA(1, 1); SB(); acc = MFMA(Wf[3], A[3], acc); SB();
#pragma unroll
        for (int c = 0; c < 4; ++c) {
            P5_LDA(2, 2); SB(); acc = MFMA(WR[4 * c], A[0], acc); SB();
            P5_LDA(3, 3); SB(); acc = MFMA(WR[4 * c + 1], A[1], acc); SB();
            cbar();
            P5_ADV();
            if (c < 3) { P5_LDA(0, 0); }
            SB(); acc = MFMA(WR[4 * c + 2], A[2], acc); SB();
            if (c < 3) { P5_LDA(1, 1); }
            SB(); acc = MFMA(WR[4 * c + 3], A[3], acc); SB();
        }
#pragma unroll
        for (int j = 0; j < 3; ++j) acc = MFMA(WR[16 + j], lds_b128(aro + BA - kh * 4096 + (kh * 3 + j) * 1024), acc);
    }
    {
        LAS f32x4* xw = (LAS f32x4*)(smem + XA + w * 2048 + lane * 16);
        f32x4 s0, s1;
#pragma unroll
        for (int e = 0; e < 4; ++e) { s0[e] = kh ? acc[e] : acc[8 + e]; s1[e] = kh ? acc[4 + e] : acc[12 + e]; }
        xw[0] = s0; xw[64] = s1;
    }
    cbar();
    float v[8];
    {
        LAS const f32x4* xr = (LAS const f32x4*)(smem + XA + (w ^ 4) * 2048 + lane * 16);
        const f32x4 s0 = xr[0], s1 = xr[64];
        if constexpr (MODE == 0) {
            const int row = ta * 32 + r;
#pragma unroll
            for (int g = 0; g < 2; ++g)
                res[g] = *(LAS const u32x2*)(smem + P3_X + row * 128 + (((cj * 4 + kh * 2 + g) ^ ((row >> 1) & 7)) << 4) + hh * 8);
        } else { res[0][0] = hkeep[0]; res[0][1] = hkeep[1]; res[1][0] = hkeep[2]; res[1][1] = hkeep[3]; }
#pragma unroll
        for (int e = 0; e < 4; ++e) { v[e] = (kh ? acc[8 + e] : acc[e]) + s0[e]; v[4 + e] = (kh ? acc[12 + e] : acc[4 + e]) + s1[e]; }
#pragma unroll
        for (int g = 0; g < 2; ++g) {
            v[4 * g] += bflo(res[g][0]); v[4 * g + 1] += bfhi(res[g][0]); v[4 * g + 2] += bflo(res[g][1]); v[4 * g + 3] += bfhi(res[g][1]);
        }
    }
    const unsigned p0 = cvtpk(v[0], v[1]), p1 = cvtpk(v[2], v[3]), p2 = cvtpk(v[4], v[5]), p3 = cvtpk(v[6], v[7]);
    const u32x4 o = swap4(p0, p1, p2, p3);
    if constexpr (MODE == 0) {
        hkeep[0] = p0; hkeep[1] = p1; hkeep[2] = p2; hkeep[3] = p3;
        gst128(ws + WS_HF + ((size_t)tile * 32 + (n0 >> 4)) * 1024, lane * 16, o);
        float s1 = 0.f, s2 = 0.f;
#pragma unroll
        for (int e = 0; e < 4; ++e) { s1 = dot2(o[e], ONES, s1); s2 = dot2(o[e], o[e], s2); }
        s1 = sum_halves(s1); s2 = sum_halves(s2);
        LAS f32x2* sl = (LAS f32x2*)(smem + XA + 8 * 2048);
        if (lane < 32) { f32x2 sv = {s1, s2}; sl[(ta * 32 + r) * 4 + cj * 2 + kh] = sv; }
        cbar();
        if (w == 0) {
            const f32x4 p = *(LAS const f32x4*)(sl + lane * 4), q = *(LAS const f32x4*)(sl + lane * 4 + 2);
            u32x2 sv = {__float_as_uint(p[0] + p[2] + q[0] + q[2]), __float_as_uint(p[1] + p[3] + q[1] + q[3])};
            gst64(ws + WS_ST + (size_t)tb * 64 * 64 + cb * 8, lane * 64, sv);
        }
    } else {
        *(u32x4*)(P.out + (size_t)token * 1024 + (n0 + 8 * hh) * 2) = o;
    }
}

constexpr int P4_ST = 147456, P4_CS = P4_ST + 8192;
template <int NB>
DI void p4_compute(char* ws, char* smem, int tb, int cg, int w, int lane) {
    const int r = lane & 31, hh = lane >> 5, i = w & 3, j0 = NB == 2 ? 0 : 2;
    const unsigned lds0 = (unsigned)(uintptr_t)smem;
    const int tile = tb * 4 + i;
    const unsigned hro = lds0 + 98304 + i * 4096 + lane * 16;
    const unsigned wro = lds0 + hh * 1536 + ((lane & 15) >> 2) * 64 + ((lane >> 4) & 1) * 32 + (lane & 3) * 8 + j0 * 256;
    f32x16 ag[NB], au[NB];
#pragma unroll
    for (int j = 0; j < NB; ++j) { ag[j] = f32x16{}; au[j] = f32x16{}; }
    bf16x8 H[4], G[4][NB], U[4][NB];
#define P4_LOAD(S, BH, BW, ks)                                                                                                    \
    {                                                                                                                             \
        H[S] = lds_b128(hro + (BH) + (ks) * 1024);                                                                                 \
        _Pragma("unroll") for (int j = 0; j < NB; ++j) { G[S][j] = lds_trfrag(wro + (BW) + (ks) * 3072 + j * 256, 768); U[S][j] = lds_trfrag(wro + (BW) + 12288 + (ks) * 3072 + j * 256, 768); } \
    }
#define P4_MMA(S) { _Pragma("unroll") for (int j = 0; j < NB; ++j) { ag[j] = MFMA(G[S][j], H[S], ag[j]); au[j] = MFMA(U[S][j], H[S], au[j]); } }
    cbar();
    P4_LOAD(0, 0, 0, 0); P4_LOAD(1, 0, 0, 1);
    unsigned BH = 0, BW = 0;
#pragma unroll 1
    for (int ch = 0; ch < 8; ++ch) {
        P4_LOAD(2, BH, BW, 2); SB(); P4_MMA(0); SB();
        P4_LOAD(3, BH, BW, 3); SB(); P4_MMA(1); SB();
        cbar();
        if (ch < 7) {
            BH = BH == 2 * 16384 ? 0 : BH + 16384; BW = BW == 3 * 24576 ? 0 : BW + 24576;
            P4_LOAD(0, BH, BW, 0);
        }
        SB(); P4_MMA(2); SB();
        if (ch < 7) { P4_LOAD(1, BH, BW, 1); }
        SB(); P4_MMA(3); SB();
    }
    float mean, rstd;
    {
        float s1 = 0.f, s2 = 0.f;
        const LAS f32x4* sp = (const LAS f32x4*)(smem + P4_ST + (i * 32 + r) * 64);
#pragma unroll
        for (int k = 0; k < 4; ++k) { const f32x4 v = sp[k]; s1 += v[0] + v[2]; s2 += v[1] + v[3]; }
        mean = s1 * (1.f / DM);
        rstd = __builtin_amdgcn_rsqf(fmaxf(s2 * (1.f / DM) - mean * mean, 0.f) + LN_EPS);
    }
    constexpr float C1 = -2.f * 0.7978845608028654f * LOG2E, C2 = C1 * 0.044715f;
    const float k1 = C1 * rstd, k2 = C2 * rstd * rstd * rstd, k3 = __builtin_amdgcn_rcpf(rstd * rstd);
    const f32x2 nm2 = {-mean, -mean}, k12 = {k1, k1}, k22 = {k2, k2}, k32 = {k3, k3};
#pragma unroll
    for (int j = 0; j < NB; ++j) {
        const int blk = cg * 3 + j0 + j;
        float v[16];
#pragma unroll
        for (int g = 0; g < 4; ++g) {
            const LAS f32x4* cp = (const LAS f32x4*)(smem + P4_CS + ((j0 + j) * 32 + 8 * g + 4 * hh) * 4);
            const f32x4 cgf = cp[0], cuf = cp[32];
#pragma unroll
            for (int e = 0; e < 4; e += 2) {
                const f32x2 ga = {ag[j][4 * g + e], ag[j][4 * g + e + 1]}, ua = {au[j][4 * g + e], au[j][4 * g + e + 1]};
                const f32x2 cg2 = {cgf[e], cgf[e + 1]}, cu2 = {cuf[e], cuf[e + 1]};
                const f32x2 a = __builtin_elementwise_fma(nm2, cg2, ga), bq = __builtin_elementwise_fma(nm2, cu2, ua);
                const f32x2 arg = a * __builtin_elementwise_fma(a * a, k22, k12);
                const f32x2 ex = {__builtin_amdgcn_exp2f(arg[0]), __builtin_amdgcn_exp2f(arg[1])};
                const f32x2 den = __builtin_elementwise_fma(ex, k32, k32);
                const f32x2 rc = {__builtin_amdgcn_rcpf(den[0]), __builtin_amdgcn_rcpf(den[1])};
                const f32x2 y = (a * rc) * bq;
                v[4 * g + e] = y[0]; v[4 * g + e + 1] = y[1];
            }
        }
        if (blk < 43) {
#pragma unroll
            for (int g = 0; g < 2; ++g) {
                const u32x4 o = pack_swap(v[8 * g], v[8 * g + 1], v[8 * g + 2], v[8 * g + 3], v[8 * g + 4], v[8 * g + 5], v[8 * g + 6], v[8 * g + 7]);
                gst128(ws + WS_AF + ((size_t)tile * 86 + blk * 2 + g) * 1024, lane * 16, o);
            }
        }
    }
}

__global__ __launch_bounds__(640, 3) void k_block(const char* __restrict__ x, const char* __restrict__ wqkv, const char* __restrict__ wo, const char* __restrict__ wg,
                                                  const char* __restrict__ wu, const char* __restrict__ wd, char* __restrict__ ws, char* __restrict__ out) {
    extern __shared__ __attribute__((aligned(16))) char smem[];
    const Ptrs P = {x, wqkv, wo, wg, wu, wd, ws, out};
    const int b = blockIdx.x, xcd = b & 7, idx = b >> 3;
    const int lane = threadIdx.x & 63, w = rfl(threadIdx.x >> 6);
    const unsigned lds0 = (unsigned)(uintptr_t)smem;
    if (w >= 8 && blockIdx.x < 64) {
        const char* pc = (const char*)(__builtin_amdgcn_s_getpc() & ~127ull);
        asm volatile("s_mov_b32 m0, %0\n\ts_nop 0" ::"s"(lds0 + LDS_BYTES - 256) : "memory");
#pragma unroll
        for (int i = 0; i < (CODE_LINES + 63) / 64; ++i) { const int ln = i * 64 + lane; gtouch(pc, (ln < CODE_LINES ? ln : CODE_LINES - 1) * 128); }
    }
    const bool p1_gemm = idx >= 8;
    const int gi = idx - 8;
    const int tb1 = (xcd >> 1) * 4 + (gi & 3), cb1 = (xcd & 1) * 6 + (gi >> 2);
    const int head = xcd * 4 + (idx >> 3), qb = idx & 7;
    const int tb3 = xcd * 4 + (idx & 3), cb3 = idx >> 2;
    const bool p4_on = !(xcd & 1) || idx < 28;
    const int tb4 = (xcd >> 1) * 4 + (idx & 3), cg4 = (xcd & 1) * 8 + (idx >> 2);

    if (w < 8) {
        if (p1_gemm) {
            f32x16 accA, accB;
            if (w >= 4) p1_colsum(wqkv, ws, tb1, cb1, smem, w - 4, lane, accA, accB);
            else if (cb1 < 8) p1_compute<false>(ws, tb1, cb1, smem, w, lane, accA, accB); else p1_compute<true>(ws, tb1, cb1, smem, w, lane, accA, accB);
            const int tl = 2 * (w & 1) + (w >> 2), cp = (w >> 1) & 1;
            if (cb1 < 8) p1_epilogue<false>(ws, tb1, cb1, smem, tl, cp, accA, accB, lane); else p1_epilogue<true>(ws, tb1, cb1, smem, tl, cp, accA, accB, lane);
        } else p1_helper(x, wqkv, wg, wu, ws, idx * 8 + xcd, w, lane);
        pbar();
        p2_compute(ws, smem, head, qb, w, lane);
        pbar();
        u32x4 hkeep;
        p35_compute<0>(P, smem, tb3, cb3, w, lane, hkeep);
        pbar();
        if (p4_on) {
            if (w < 4) p4_compute<2>(ws, smem, tb4, cg4, w, lane); else p4_compute<1>(ws, smem, tb4, cg4, w, lane);
        } else bar();
        pbar();
        p35_compute<1>(P, smem, tb3, cb3, w, lane, hkeep);
        return;
    }

    const int L = w - 8;
    const char* flags = ws + WS_FLAG;
    const unsigned lin = lane * 16;
    if (!p1_gemm && L == 0) small_touch(ws, b, lane, lds0);
    const unsigned tag = p1_gemm ? p1_load(P, L, tb1, cb1, lds0, lane) : rfl(gld32_wait(ws + WS_EPOCH, 0)) + 1;
    bar();
    if (L == 0 && lane == 0) gst32(ws + WS_FLAG, b * 128, tag);
    {
        const int ptb = lane < 32 ? (lane & 15) : 2 * qb + (lane & 1), pcb = (lane < 16 ? 4 : lane < 32 ? 8 : 0) + (head >> 3);
        const int pb = ((pcb % 6) * 4 + (ptb & 3) + 8) * 8 + (ptb >> 2) * 2 + pcb / 6;
        poll(flags, pb * 128, lane < 34, tag);
        const char* src = ws + (L == 0 ? WS_KF : WS_VF) + (size_t)head * 65536;
        const unsigned dst = lds0 + L * 65536;
        {
            const char* q = ws + WS_QF + ((size_t)head * 64 + qb * 8) * 1024 + L * 4096;
            dma4lin(q, lin, lds0 + P2_X + L * 4096);
            if (L == 0) dma1dw(ws + WS_KM + head * 256, lane * 4, lds0 + P2_KM);
        }
        dma4lin(src, lin, dst); dma4lin(src + 32768, lin, dst + 32768);
        dma2lin(src + 4096, lin, dst + 4096); dma2lin(src + 32768 + 4096, lin, dst + 32768 + 4096);
        lbar<0>();
        dma2lin(src + 6144, lin, dst + 6144); dma2lin(src + 32768 + 6144, lin, dst + 32768 + 6144);
#pragma unroll 1
        for (int g = 2; g < 8; ++g) {
            dma4lin(src + g * 4096, lin, dst + g * 4096); dma4lin(src + 32768 + g * 4096, lin, dst + 32768 + g * 4096);
        }
        lbar<0>();
        if (L == 1) {
#pragma unroll 1
            for (int g = 0; g < 4; ++g) poll(flags, (g * 64 + lane) * 128, true, tag);
            if (b == 0 && lane == 0) gst32(ws + WS_EPOCH, 0, tag);
            asm volatile("s_mov_b32 m0, %0\n\ts_nop 0" ::"s"(lds0 + LDS_BYTES - 256) : "memory");
            gtouch(wo, (idx * 128 + lane) * 128); gtouch(wo, (idx * 128 + 64 + lane) * 128);
            {
                const int nl = (xcd & 1) ? 10 : 12, ln = lane & 15;
                const unsigned co = (xcd & 1) * 1536 + (ln < nl ? ln : 0) * 128;
#pragma unroll
                for (int i = 0; i < 4; ++i) {
                    unsigned o_ = (idx * 16 + i * 4 + (lane >> 4)) * (FF * 2) + co;
                    o_ = o_ > 512u * FF * 2 - 4 ? 512u * FF * 2 - 4 : o_;
                    gtouch(wg, o_); gtouch(wu, o_);
                }
            }
#pragma unroll
            for (int i = 0; i < 6; ++i) { int ln = i * 64 + lane; ln = ln > 343 ? 343 : ln; gtouch(wd, (idx * 344 + ln) * 128); }
        }
        bar();
    }
    unsigned wvo[4];
    {
        const unsigned wv = ((lane >> 5) * 4 + ((lane >> 2) & 3)) * 1024 + ((lane >> 4) & 1) * 64 + (lane & 3) * 16;
#pragma unroll
        for (int i = 0; i < 4; ++i) wvo[i] = wv + i * 8192 - i * 1024;
    }
    {
        bar();
        if (L == 1) {
            {
                const int rl = lane >> 3, cs = lane & 7;
                unsigned xv[4];
#pragma unroll
                for (int i = 0; i < 4; ++i) xv[i] = (8 * i + rl) * 1024 + ((cs ^ ((4 * (i & 1) + (rl >> 1)) & 7)) << 4) - i * 1024;
                const char* xs = x + (size_t)tb3 * 64 * 1024 + cb3 * 128;
                dma4(xs, xv[0], xv[1], xv[2], xv[3], lds0 + P3_X); dma4(xs + 32768, xv[0], xv[1], xv[2], xv[3], lds0 + P3_X + 4096);
            }
#pragma unroll 1
            for (int c = 0; c < 4; ++c) {
                const char* src = wo + cb3 * 128 + (size_t)c * 128 * 1024;
#pragma unroll
                for (int g = 0; g < 4; ++g) dma4(src + g * 32768, wvo[0], wvo[1], wvo[2], wvo[3], lds0 + c * 16384 + g * 4096);
            }
        }
        if (L == 0) {
            if (lane == 0) gst32(ws + WS_FLAG + 256 * 128, b * 128, tag);
            const int ph = lane & 31;
            poll(flags + 256 * 128, (((ph & 3) * 8 + (tb3 >> 2)) * 8 + (ph >> 2)) * 128, true, tag);
            const char* ab = ws + WS_OF + (size_t)tb3 * 2 * 32 * 1024;
#pragma unroll 1
            for (int c = 0; c < 4; ++c) {
                const char* src = ab + c * 8192;
                const unsigned dst = lds0 + 65536 + c * 16384;
                dma4lin(src, lin, dst); dma4lin(src + 4096, lin, dst + 4096); dma4lin(src + 32768, lin, dst + 8192); dma4lin(src + 32768 + 4096, lin, dst + 12288);
                if (c == 1) lbar<16>();
            }
            lbar<32>(); lbar<16>(); lbar<0>();
        } else {
            lbar<0>(); bar(); bar(); bar();
        }
        bar();
    }
    {
        unsigned gvo[12];
        if (L == 1 && p4_on) {
#pragma unroll
            for (int j = 0; j < 12; ++j) {
                const int s = j * 64 + lane, st = s >> 4, kb = st / 3, nb = st % 3, q = (s >> 2) & 3, c = s & 3;
                int blk = cg4 * 3 + nb; blk = blk > 42 ? 42 : blk;
                gvo[j] = (4 * kb + q) * (FF * 2) + blk * 64 + c * 16 - (j & 3) * 1024;
            }
        }
        auto issue_w = [&](int c, unsigned B) {
            const size_t so = (size_t)c * 64 * (FF * 2);
#pragma unroll
            for (int g = 0; g < 3; ++g) dma4(wg + so, gvo[4 * g], gvo[4 * g + 1], gvo[4 * g + 2], gvo[4 * g + 3], lds0 + B + g * 4096);
#pragma unroll
            for (int g = 0; g < 3; ++g) dma4(wu + so, gvo[4 * g], gvo[4 * g + 1], gvo[4 * g + 2], gvo[4 * g + 3], lds0 + B + 12288 + g * 4096);
        };
        bar();
        bar();
        if (L == 1 && p4_on) { issue_w(0, 0); issue_w(1, 24576); issue_w(2, 2 * 24576); issue_w(3, 3 * 24576); }
        if (L == 0) { if (lane == 0) gst32(ws + WS_FLAG + 2 * 256 * 128, b * 128, tag); }
        if (p4_on) {
            if (L == 0) {
                const int ptb = tb4 * 2 + (lane & 1), pcb = (lane >> 1) & 7;
                poll(flags + 2 * 256 * 128, ((pcb * 4 + (ptb & 3)) * 8 + (ptb >> 2)) * 128, true, tag);
                const char* hb = ws + WS_HF + (size_t)tb4 * 4 * 32 * 1024;
                auto issue_h = [&](int c, unsigned B) {
                    const char* src = hb + c * 4096;
#pragma unroll
                    for (int i = 0; i < 4; ++i) dma4lin(src + i * 32768, lin, lds0 + 98304 + B + i * 4096);
                };
                issue_h(0, 0); issue_h(1, 16384);
                lbar<16>();
                {
                    const char* sb = ws + WS_ST + (size_t)tb4 * 128 * 64;
                    dma4lin(sb, lin, lds0 + P4_ST); dma4lin(sb + 4096, lin, lds0 + P4_ST + 4096);
                    const char* cb_ = ws + WS_CS + cg4 * 384;
                    dma1dw(cb_, lane * 4, lds0 + P4_CS); dma1dw(cb_ + 256, lane * 4, lds0 + P4_CS + 256);
                    dma1dw(cb_ + FF * 4, lane * 4, lds0 + P4_CS + 512); dma1dw(cb_ + FF * 4 + 256, lane * 4, lds0 + P4_CS + 768);
                }
                issue_h(2, 2 * 16384);
                unsigned B = 0;
#pragma unroll 1
                for (int c = 1; c < 8; ++c) {
                    if (c < 7) lbar<16>(); else lbar<0>();
                    if (c + 2 < 8) { issue_h(c + 2, B); B = B == 2 * 16384 ? 0 : B + 16384; }
                }
            } else {
                unsigned B = 0;
#pragma unroll 1
                for (int c = 0; c < 8; ++c) {
                    if (c == 0) lbar<63>(); else if (c < 6) lbar<48>(); else if (c == 6) lbar<24>(); else lbar<0>();
                    if (c >= 1 && c + 3 < 8) { issue_w(c + 3, B); B = B == 3 * 24576 ? 0 : B + 24576; }
                }
            }
            bar();
        } else bar();
    }
    {
        auto issue_w = [&](int c, unsigned B) {
            const char* src = wd + cb3 * 128 + (size_t)c * 128 * 1024;
#pragma unroll
            for (int g = 0; g < 3; ++g) dma4(src + g * 32768, wvo[0], wvo[1], wvo[2], wvo[3], lds0 + B + g * 4096);
            dma4(src + (c < 10 ? 3 : 2) * 32768, wvo[0], wvo[1], wvo[2], wvo[3], lds0 + B + 3 * 4096);
        };
        if (L == 1) {
#pragma unroll 1
            for (int c = 6; c < 11; ++c) issue_w(c, (c - 6) * 16384);
        }
        bar();
        if (L == 0 && p4_on && lane == 0) gst32(ws + WS_FLAG + 3 * 256 * 128, b * 128, tag);
        if (L == 1) lbar<0>(); else bar();
        bar();
        if (L == 1) {
#pragma unroll 1
            for (int c = 0; c < 6; ++c) issue_w(c, c * 16384);
        }
        if (L == 0) {
            const int ptb = tb3 >> 1, pcg = lane < 15 ? lane : 0;
            poll(flags + 3 * 256 * 128, (((pcg & 7) * 4 + (ptb & 3)) * 8 + (ptb >> 2) * 2 + (pcg >> 3)) * 128, true, tag);
            const char* ab = ws + WS_AF + (size_t)tb3 * 2 * 86 * 1024;
            auto issue_a = [&](int c, unsigned B) {
                const char* src = ab + c * 8192;
                const unsigned dst = lds0 + 98304 + B;
                const int o2 = c < 10 ? 4096 : 2048;
                dma4lin(src, lin, dst); dma4lin(src + o2, lin, dst + o2);
                dma4lin(src + 86 * 1024, lin, dst + 8192); dma4lin(src + 86 * 1024 + o2, lin, dst + 8192 + o2);
            };
            issue_a(0, 0); issue_a(1, 16384);
            unsigned B = 2 * 16384;
#pragma unroll 1
            for (int c = 0; c < 11; ++c) {
                if (c < 10) lbar<16>(); else lbar<0>();
                if (c + 2 < 11) { issue_a(c + 2, B); B = B == 2 * 16384 ? 0 : B + 16384; }
            }
        } else {
            lbar<63>(); lbar<63>(); lbar<48>(); lbar<32>(); lbar<16>(); lbar<0>();
            bar(); bar(); bar(); bar(); bar();
        }
        bar();
    }
}

extern "C" void kernel_launch(void* const* d_in, const int* in_sizes, int n_in, void* d_out, int out_size, void* d_ws, size_t ws_size, hipStream_t stream) {
    (void)hipFuncSetAttribute((const void*)k_block, hipFuncAttributeMaxDynamicSharedMemorySize, LDS_BYTES);
    k_block<<<256, 640, LDS_BYTES, stream>>>((const char*)d_in[0], (const char*)d_in[1], (const char*)d_in[2], (const char*)d_in[4], (const char*)d_in[5], (const char*)d_in[6],
                                             (char*)d_ws, (char*)d_out);
}
```

```cpp
#include <hip/hip_runtime.h>
#include <hip/hip_bf16.h>
#include <stdint.h>

typedef __bf16 bf16x8 __attribute__((ext_vector_type(8)));
typedef __bf16 bf16x2 __attribute__((ext_vector_type(2)));
typedef short s16x4 __attribute__((ext_vector_type(4)));
typedef short s16x8 __attribute__((ext_vector_type(8)));
typedef float f32x16 __attribute__((ext_vector_type(16)));
typedef float f32x4 __attribute__((ext_vector_type(4)));
typedef float f32x2 __attribute__((ext_vector_type(2)));
typedef unsigned u32x4 __attribute__((ext_vector_type(4)));
typedef unsigned u32x2 __attribute__((ext_vector_type(2)));
#define LAS __attribute__((address_space(3)))
#define DI __device__ __forceinline__

constexpr int DM = 512, FF = 1376;
constexpr float LOG2E = 1.4426950408889634f;
constexpr float QSCALE = 0.25f * LOG2E;
constexpr float LN_EPS = 1e-5f;

constexpr size_t WS_QF = 0, WS_KF = 2u << 20, WS_VF = 4u << 20, WS_OF = 6u << 20, WS_HF = 8u << 20, WS_AF = 10u << 20,
                 WS_ST = 16u << 20, WS_CS = 17u << 20, WS_KM = (17u << 20) + 65536, WS_FLAG = 18u << 20, WS_EPOCH = WS_FLAG + 4 * 256 * 128,
                 WS_G1 = 19u << 20, WS_G2 = WS_G1 + 65536;
constexpr int LDS_BYTES = 163840;
constexpr unsigned SPIN_CAP = 1u << 17;
constexpr int CODE_LINES = 288;

#define MFMA(a, b, c) __builtin_amdgcn_mfma_f32_32x32x16_bf16(a, b, c, 0, 0, 0)
#define SB() __builtin_amdgcn_sched_barrier(0)
DI void cbar() { asm volatile("s_waitcnt lgkmcnt(0)\n\ts_barrier" ::: "memory"); }
DI void pbar() { asm volatile("s_waitcnt vmcnt(0) lgkmcnt(0)\n\ts_barrier" ::: "memory"); }
template <int N> DI void lbar() { asm volatile("s_waitcnt vmcnt(%0)\n\ts_barrier" ::"i"(N > 63 ? 63 : N) : "memory"); }
DI void bar() { asm volatile("s_barrier" ::: "memory"); }

DI void dma4(const void* sbase, unsigned v0, unsigned v1, unsigned v2, unsigned v3, unsigned dst) {
    asm volatile("s_mov_b32 m0, %5\n\ts_nop 0\n\tglobal_load_lds_dwordx4 %0, %4 sc1\n\tglobal_load_lds_dwordx4 %1, %4 offset:1024 sc1\n\t"
                 "global_load_lds_dwordx4 %2, %4 offset:2048 sc1\n\tglobal_load_lds_dwordx4 %3, %4 offset:3072 sc1"
                 :: "v"(v0), "v"(v1), "v"(v2), "v"(v3), "s"(sbase), "s"(dst) : "memory");
}
DI void dma4lin(const void* sbase, unsigned v, unsigned dst) { dma4(sbase, v, v, v, v, dst); }
DI void dma2lin(const void* sbase, unsigned v, unsigned dst) {
    asm volatile("s_mov_b32 m0, %2\n\ts_nop 0\n\tglobal_load_lds_dwordx4 %0, %1 sc1\n\tglobal_load_lds_dwordx4 %0, %1 offset:1024 sc1" :: "v"(v), "s"(sbase), "s"(dst) : "memory");
}
DI void dma1dw(const void* sbase, unsigned v, unsigned dst) {
    asm volatile("s_mov_b32 m0, %2\n\ts_nop 0\n\tglobal_load_lds_dword %0, %1 sc1" :: "v"(v), "s"(sbase), "s"(dst) : "memory");
}
DI void gtouch(const void* sbase, unsigned voff) {
    asm volatile("global_load_lds_dword %0, %1" :: "v"(voff), "s"(sbase) : "memory");
}
DI unsigned gld32_wait(const void* sbase, unsigned voff) {
    unsigned r; asm volatile("global_load_dword %0, %1, %2 sc1\n\ts_waitcnt vmcnt(0)" : "=&v"(r) : "v"(voff), "s"(sbase) : "memory"); return r;
}
DI void gst128(void* sbase, unsigned voff, u32x4 v) { asm volatile("global_store_dwordx4 %0, %1, %2 sc1\n\ts_nop 1" ::"v"(voff), "v"(v), "s"(sbase) : "memory"); }
DI void gst64(void* sbase, unsigned voff, u32x2 v) { asm volatile("global_store_dwordx2 %0, %1, %2 sc1\n\ts_nop 1" ::"v"(voff), "v"(v), "s"(sbase) : "memory"); }
DI void gst32(void* sbase, unsigned voff, unsigned v) { asm volatile("global_store_dword %0, %1, %2 sc1\n\ts_nop 1" ::"v"(voff), "v"(v), "s"(sbase) : "memory"); }
DI unsigned rfl(unsigned x) { return __builtin_amdgcn_readfirstlane(x); }
DI unsigned cvtpk(float lo, float hi) {
    f32x2 v = {lo, hi};
    bf16x2 b = __builtin_convertvector(v, bf16x2);
    return __builtin_bit_cast(unsigned, b);
}
DI float bflo(unsigned u) { return __uint_as_float(u << 16); }
DI float bfhi(unsigned u) { return __uint_as_float(u & 0xffff0000u); }
DI bf16x8 lds_b128(unsigned a) { return *(LAS const bf16x8*)(uintptr_t)a; }
typedef short v4i16_t __attribute__((ext_vector_type(4)));
DI s16x4 lds_tr(unsigned a) { return __builtin_bit_cast(s16x4, __builtin_amdgcn_ds_read_tr16_b64_v4i16((LAS v4i16_t*)(uintptr_t)a)); }
DI bf16x8 lds_trfrag(unsigned a, int off2) {
    s16x4 x = lds_tr(a), y = lds_tr(a + off2);
    s16x8 r = __builtin_shufflevector(x, y, 0, 1, 2, 3, 4, 5, 6, 7);
    return __builtin_bit_cast(bf16x8, r);
}
DI float dot2(unsigned a, unsigned b, float c) {
    return __builtin_amdgcn_fdot2_f32_bf16(__builtin_bit_cast(bf16x2, a), __builtin_bit_cast(bf16x2, b), c, false);
}
DI float other_half(float v) {
    auto r = __builtin_amdgcn_permlane32_swap(__float_as_uint(v), __float_as_uint(v), false, false);
    return __uint_as_float((threadIdx.x & 32) ? r[0] : r[1]);
}
DI float sum_halves(float v) {
    auto r = __builtin_amdgcn_permlane32_swap(__float_as_uint(v), __float_as_uint(v), false, false);
    return __uint_as_float(r[0]) + __uint_as_float(r[1]);
}
template <bool ALL> DI float dpp_max(float v) {
#define DPPMAX(ctrl, rm) v = fmaxf(v, __int_as_float(__builtin_amdgcn_update_dpp(0, __float_as_int(v), ctrl, rm, 0xf, true)))
    DPPMAX(0x111, 0xf); DPPMAX(0x112, 0xf); DPPMAX(0x114, 0xf); DPPMAX(0x118, 0xf); DPPMAX(0x142, 0xa);
    if (ALL) DPPMAX(0x143, 0xc);
    return v;
}
template <int CTRL> DI float dpp_add(float v) { return v + __int_as_float(__builtin_amdgcn_update_dpp(0, __float_as_int(v), CTRL, 0xf, 0xf, true)); }
DI u32x4 swap4(unsigned p0, unsigned p1, unsigned p2, unsigned p3) {
    auto s0 = __builtin_amdgcn_permlane32_swap(p0, p2, false, false);
    auto s1 = __builtin_amdgcn_permlane32_swap(p1, p3, false, false);
    u32x4 r = {s0[0], s1[0], s0[1], s1[1]};
    return r;
}
DI u32x4 pack_swap(float a0, float a1, float a2, float a3, float a4, float a5, float a6, float a7) {
    return swap4(cvtpk(a0, a1), cvtpk(a2, a3), cvtpk(a4, a5), cvtpk(a6, a7));
}
constexpr unsigned ONES = 0x3f803f80u;

DI void poll(const char* fl, unsigned voff, bool active, unsigned tag) {
    for (unsigned spins = 0; spins < SPIN_CAP; ++spins) {
        unsigned f;
        asm volatile("global_load_dword %0, %1, %2 sc1\n\ts_waitcnt vmcnt(0)" : "=&v"(f) : "v"(voff), "s"(fl) : "memory");
        if (__all(!active || f == tag)) break;
        __builtin_amdgcn_s_sleep(3);
    }
}

struct Ptrs { const char *x, *wqkv, *wo, *wg, *wu, *wd; char* ws; char* out; };

constexpr int P1NB = 4;
constexpr int P1_BUF = 32768, P1_MISC = P1NB * P1_BUF;

DI void gst_granule(char* sbase, unsigned voff, float v, unsigned tag) { u32x2 g = {__float_as_uint(v), tag}; gst64(sbase, voff, g); }
DI float sum_x16(float v) {
    auto r = __builtin_amdgcn_permlane16_swap(__float_as_uint(v), __float_as_uint(v), false, false);
    return __uint_as_float(r[0]) + __uint_as_float(r[1]);
}
template <bool ISV>
DI void p1_epilogue(char* ws, int tb, int cb, char* smem, int tl, int cp, const f32x16& accA, const f32x16& accB, int lane) {
    const int r = lane & 31, hh = lane >> 5;
    const LAS f32x2* st = (const LAS f32x2*)(smem + P1_MISC);
    const LAS float* csl = (const LAS float*)(smem + P1_MISC + 1024);
    const int sec = cb >> 2, tile = tb * 4 + tl;
#pragma unroll
    for (int b = 0; b < 2; ++b) {
        const f32x16& acc = b ? accB : accA;
        const int ct = 2 * cp + b, head0 = (cb & 3) * 8 + ct * 2;
        float v[16];
        if constexpr (!ISV) {
            const f32x2 ms = st[tl * 32 + r];
            const float sc = ms[1] * (sec == 0 ? QSCALE : 1.f), msc = -ms[0] * sc;
#pragma unroll
            for (int g = 0; g < 4; ++g) {
                const f32x4 c4 = *(LAS const f32x4*)(csl + ct * 32 + 8 * g + 4 * hh);
#pragma unroll
                for (int e = 0; e < 4; ++e) v[4 * g + e] = acc[4 * g + e] * sc + msc * c4[e];
            }
            char* dst = ws + (sec == 0 ? WS_QF : WS_KF) + ((size_t)head0 * 64 + tile) * 1024;
#pragma unroll
            for (int hs = 0; hs < 2; ++hs) {
                const u32x4 o = pack_swap(v[8 * hs], v[8 * hs + 1], v[8 * hs + 2], v[8 * hs + 3], v[8 * hs + 4], v[8 * hs + 5], v[8 * hs + 6], v[8 * hs + 7]);
                gst128(dst + hs * 65536, lane * 16, o);
                if (sec == 1) {
                    float n2 = 0.f;
#pragma unroll
                    for (int e = 0; e < 8; ++e) n2 = fmaf(v[8 * hs + e], v[8 * hs + e], n2);
                    n2 = dpp_max<false>(sum_halves(n2));
                    if (lane == 31) gst32(ws + WS_KM + (head0 + hs) * 256 + tile * 4, 0, __float_as_uint(n2));
                }
            }
        } else {
            const float csn = csl[ct * 32 + r];
#pragma unroll
            for (int g = 0; g < 4; ++g) {
                const LAS f32x4* sp = (const LAS f32x4*)(st + tl * 32 + 8 * g + 4 * hh);
                const f32x4 pa = sp[0], pb = sp[1];
                v[4 * g] = (acc[4 * g] - pa[0] * csn) * pa[1]; v[4 * g + 1] = (acc[4 * g + 1] - pa[2] * csn) * pa[3];
                v[4 * g + 2] = (acc[4 * g + 2] - pb[0] * csn) * pb[1]; v[4 * g + 3] = (acc[4 * g + 3] - pb[2] * csn) * pb[3];
            }
            char* dst = ws + WS_VF + ((size_t)head0 * 64 + tile) * 1024;
            const unsigned vo = (r >> 4) * 65536 + hh * 256 + (r & 15) * 16;
#pragma unroll
            for (int s = 0; s < 2; ++s) {
                u32x4 o = {cvtpk(v[8 * s], v[8 * s + 1]), cvtpk(v[8 * s + 2], v[8 * s + 3]), cvtpk(v[8 * s + 4], v[8 * s + 5]), cvtpk(v[8 * s + 6], v[8 * s + 7])};
                gst128(dst + s * 512, vo, o);
            }
        }
    }
}
constexpr int P1_XCH = (5 % P1NB) * P1_BUF;

DI void p1_colsum(const char* wqkv, char* ws, int tb, int cb, char* smem, int j, int lane, f32x16& accA, f32x16& accB) {
    const unsigned tag = __hip_atomic_load((const __attribute__((address_space(1))) unsigned*)(ws + WS_EPOCH), __ATOMIC_RELAXED, __HIP_MEMORY_SCOPE_AGENT) + 1;
    const char* src = wqkv + (size_t)((tb & 3) * 128 + j * 32 + (lane >> 4)) * 3072 + cb * 256 + (lane & 15) * 16;
    u32x4 v[8];
#pragma unroll
    for (int i = 0; i < 8; ++i) v[i] = *(const u32x4*)(src + (size_t)i * 4 * 3072);
    cbar(); cbar();
    float s[8] = {};
#pragma unroll
    for (int i = 0; i < 8; ++i)
#pragma unroll
        for (int e = 0; e < 4; ++e) { s[2 * e] += bflo(v[i][e]); s[2 * e + 1] += bfhi(v[i][e]); }
#pragma unroll
    for (int e = 0; e < 8; ++e) s[e] = sum_halves(sum_x16(s[e]));
    LAS float* part = (LAS float*)(smem + P1_MISC + 2048);
    if (lane < 16) {
        f32x4 a = {s[0], s[1], s[2], s[3]}, c = {s[4], s[5], s[6], s[7]};
        *(LAS f32x4*)(part + j * 128 + lane * 8) = a; *(LAS f32x4*)(part + j * 128 + lane * 8 + 4) = c;
    }
    cbar();
    if (j == 0) {
#pragma unroll
        for (int k = 0; k < 2; ++k) {
            const int c = lane + 64 * k;
            gst_granule(ws + WS_G2, ((cb * 128 + c) * 4 + (tb & 3)) * 8, part[c] + part[128 + c] + part[256 + c] + part[384 + c], tag);
        }
    }
    cbar(); cbar(); cbar(); cbar(); cbar();
    cbar();
    f32x16 acc[2];
    {
        const LAS f32x4* xr = (const LAS f32x4*)(smem + P1_XCH + j * 8192 + lane * 16);
#pragma unroll
        for (int b = 0; b < 2; ++b)
#pragma unroll
            for (int g = 0; g < 4; ++g) { const f32x4 t = xr[(b * 4 + g) * 64]; acc[b][4 * g] = t[0]; acc[b][4 * g + 1] = t[1]; acc[b][4 * g + 2] = t[2]; acc[b][4 * g + 3] = t[3]; }
    }
    accA = acc[0]; accB = acc[1];
}

template <bool ISV>
DI void p1_compute(char* ws, int tb, int cb, char* smem, int w, int lane, f32x16& accA, f32x16& accB) {
    const int r = lane & 31, hh = lane >> 5;
    const unsigned lds0 = (unsigned)(uintptr_t)smem;
    const int tp = w & 1, cp = w >> 1;
    unsigned x0[4];
    {
        const int f = (r >> 1) & 7;
#pragma unroll
        for (int ks = 0; ks < 4; ++ks) x0[ks] = lds0 + (64 * tp + r) * 128 + (((2 * ks + hh) ^ f) << 4);
    }
    const unsigned wro = lds0 + 16384 + hh * 2048 + ((lane & 15) >> 2) * 64 + ((lane >> 4) & 1) * 32 + (lane & 3) * 8 + cp * 512;
    f32x16 acc[2][2] = {};
    bf16x8 X[4][2], W[4][2];
#define P1_LOAD(S, B, ks) { X[S][0] = lds_b128(x0[ks] + (B)); X[S][1] = lds_b128(x0[ks] + (B) + 4096); W[S][0] = lds_trfrag(wro + (B) + (ks) * 4096, 1024); W[S][1] = lds_trfrag(wro + (B) + (ks) * 4096 + 256, 1024); }
#define P1_MMA(S)                                                                                                                 \
    {                                                                                                                             \
        _Pragma("unroll") for (int a = 0; a < 2; ++a) _Pragma("unroll") for (int b = 0; b < 2; ++b)                                \
            acc[a][b] = ISV ? MFMA(X[S][a], W[S][b], acc[a][b]) : MFMA(W[S][b], X[S][a], acc[a][b]);                               \
    }
#define P1_ILV() { __builtin_amdgcn_sched_group_barrier(0x8, 1, 0); __builtin_amdgcn_sched_group_barrier(0x100, 2, 0); __builtin_amdgcn_sched_group_barrier(0x8, 1, 0); __builtin_amdgcn_sched_group_barrier(0x100, 2, 0); \
                   __builtin_amdgcn_sched_group_barrier(0x8, 1, 0); __builtin_amdgcn_sched_group_barrier(0x100, 2, 0); __builtin_amdgcn_sched_group_barrier(0x8, 1, 0); SB(); }
    cbar();
    P1_LOAD(0, 0, 0); P1_LOAD(1, 0, 1);
    SB();
    unsigned B = 0;
#pragma unroll 1
    for (int ch = 0; ch < 7; ++ch) {
        P1_LOAD(2, B, 2); P1_MMA(0); P1_ILV();
        P1_LOAD(3, B, 3); P1_MMA(1); P1_ILV();
        cbar();
        B = B == (P1NB - 1) * P1_BUF ? 0 : B + P1_BUF;
        P1_LOAD(0, B, 0); P1_MMA(2); P1_ILV();
        P1_LOAD(1, B, 1); P1_MMA(3); P1_ILV();
    }
    P1_LOAD(2, B, 2); P1_MMA(0); P1_ILV();
    P1_LOAD(3, B, 3); P1_MMA(1); P1_ILV();
    P1_MMA(2); P1_MMA(3);
    {
        LAS f32x4* xw = (LAS f32x4*)(smem + P1_XCH + w * 8192 + lane * 16);
#pragma unroll
        for (int b = 0; b < 2; ++b)
#pragma unroll
            for (int g = 0; g < 4; ++g) { f32x4 t = {acc[1][b][4 * g], acc[1][b][4 * g + 1], acc[1][b][4 * g + 2], acc[1][b][4 * g + 3]}; xw[(b * 4 + g) * 64] = t; }
    }
    cbar();
    accA = acc[0][0]; accB = acc[0][1];
}

typedef const __attribute__((address_space(1))) char* gcptr;
template <int NROW16>
DI void colsum32(gcptr W, unsigned rs, int lane, float (&s)[8]) {
    const unsigned vo = (lane & 15) * rs + (lane >> 4) * 16;
    u32x4 v[NROW16];
#pragma unroll
    for (int i = 0; i < NROW16; ++i) v[i] = *(const __attribute__((address_space(1))) u32x4*)(W + (size_t)i * 16 * rs + vo);
    __builtin_amdgcn_sched_barrier(0);
#pragma unroll
    for (int e = 0; e < 8; ++e) s[e] = 0.f;
#pragma unroll
    for (int i = 0; i < NROW16; ++i)
#pragma unroll
        for (int e = 0; e < 4; ++e) { s[2 * e] += bflo(v[i][e]); s[2 * e + 1] += bfhi(v[i][e]); }
#pragma unroll
    for (int e = 0; e < 8; ++e) s[e] = dpp_add<0x118>(dpp_add<0x114>(dpp_add<0x112>(dpp_add<0x111>(s[e]))));
}
DI void p1_helper(const char* px, const char* pqkv, const char* pg, const char* pu, char* ws, int h, int w, int lane) {
    if (w < 4) {
        const unsigned tag = __hip_atomic_load((const __attribute__((address_space(1))) unsigned*)(ws + WS_EPOCH), __ATOMIC_RELAXED, __HIP_MEMORY_SCOPE_AGENT) + 1;
        const int u = w * 64 + h;
        if (u < 256) {
            const int rl = lane >> 3, seg = lane & 7;
            const char* src = px + (size_t)(u * 8 + rl) * 1024 + seg * 16;
            u32x4 v[8];
#pragma unroll
            for (int i = 0; i < 8; ++i) v[i] = *(const u32x4*)(src + i * 128);
            float s1 = 0.f, s2 = 0.f;
#pragma unroll
            for (int i = 0; i < 8; ++i)
#pragma unroll
                for (int e = 0; e < 4; ++e) { s1 = dot2(v[i][e], ONES, s1); s2 = dot2(v[i][e], v[i][e], s2); }
            s1 = dpp_add<0x114>(dpp_add<0x112>(dpp_add<0x111>(s1))); s2 = dpp_add<0x114>(dpp_add<0x112>(dpp_add<0x111>(s2)));
            const float mean = s1 * (1.f / DM);
            const float rstd = __builtin_amdgcn_rsqf(fmaxf(s2 * (1.f / DM) - mean * mean, 0.f) + LN_EPS);
            if (seg == 7) { gst_granule(ws + WS_G1, (u * 8 + rl) * 16, mean, tag); gst_granule(ws + WS_G1, (u * 8 + rl) * 16 + 8, rstd, tag); }
        }
    }
    const int job = w == 4 ? h : (w == 5 && h < 22) ? 64 + h : -1;
    if (job >= 0) {
        const int blk = job < 43 ? job : job - 43;
        const uintptr_t wa = (uintptr_t)pg + (job < 43 ? 0 : (uintptr_t)pu - (uintptr_t)pg);
        float s[8];
        colsum32<32>((gcptr)wa + blk * 64, FF * 2, lane, s);
        if ((lane & 15) == 15) {
            char* dst = ws + WS_CS + (job < 43 ? 0 : FF) * 4;
            const int c0 = blk * 32 + (lane >> 4) * 8;
            u32x4 a = {__float_as_uint(s[0]), __float_as_uint(s[1]), __float_as_uint(s[2]), __float_as_uint(s[3])};
            u32x4 c = {__float_as_uint(s[4]), __float_as_uint(s[5]), __float_as_uint(s[6]), __float_as_uint(s[7])};
            gst128(dst, c0 * 4, a); gst128(dst, c0 * 4 + 16, c);
        }
    }
}

DI void small_touch(char* ws, int b, int lane, unsigned lds0) {
    unsigned o_ = (unsigned)WS_FLAG + (lane & 3) * 256 * 128 + b * 128;
    if (lane == 4) o_ = (unsigned)WS_G1 + b * 128;
    if (lane >= 5 && lane < 9) o_ = (unsigned)WS_ST + (b * 4 + lane - 5) * 128;
    if (lane == 9) o_ = (unsigned)WS_KM + (b & 63) * 128;
    if (lane == 10) o_ = (unsigned)WS_CS + (b < 86 ? b : 0) * 128;
    if (lane >= 11 && lane < 17) o_ = (unsigned)WS_G2 + (b % 64) * 768 + (lane - 11) * 128;
    asm volatile("s_mov_b32 m0, %0\n\ts_nop 0\n\tglobal_load_lds_dword %1, %2 sc1" ::"s"(lds0 + LDS_BYTES - 256), "v"(o_), "s"(ws) : "memory");
}

DI unsigned p1_load(const Ptrs& P, int L, int tb, int cb, unsigned lds0, int lane) {
    unsigned vo[4];
    const char* base;
    unsigned cstep, gstep;
    if (L == 0) {
        const int rl = lane >> 3, cs = lane & 7;
#pragma unroll
        for (int i = 0; i < 4; ++i) vo[i] = (8 * i + rl) * 1024 + ((cs ^ ((4 * (i & 1) + (rl >> 1)) & 7)) << 4) - i * 1024;
        base = P.x + (size_t)tb * 128 * 1024; cstep = 128; gstep = 4 * 8192;
    } else {
        const int nb = lane >> 4, q = (lane >> 2) & 3, c = lane & 3;
#pragma unroll
        for (int i = 0; i < 4; ++i) vo[i] = (4 * i + q) * 3072 + nb * 64 + c * 16 - i * 1024;
        base = P.wqkv + cb * 256; cstep = 64 * 3072; gstep = 16 * 3072;
    }
    auto issue = [&](int c, unsigned B) {
        const char* src = base + (size_t)c * cstep;
        const unsigned dst = lds0 + B + L * 16384;
#pragma unroll
        for (int g = 0; g < 4; ++g) dma4(src + (size_t)g * gstep, vo[0], vo[1], vo[2], vo[3], dst + g * 4096);
    };
    issue(0, 0);
#pragma unroll
    for (int c = 1; c < P1NB - 1; ++c) issue(c, c * P1_BUF);
    if (L == 0) small_touch(P.ws, blockIdx.x, lane, lds0);
    unsigned B = (P1NB - 1) * P1_BUF;
    constexpr int LA = P1NB - 1;
#pragma unroll 1
    for (int c = 0; c < 8; ++c) {
        const int y = 7 - c < LA - 1 ? 7 - c : LA - 1;
        if (y == 3) lbar<48>(); else if (y == 2) lbar<32>(); else if (y == 1) lbar<16>(); else lbar<0>();
        if (c + LA < 8) { issue(c + LA, B); B = B == (P1NB - 1) * P1_BUF ? 0 : B + P1_BUF; }
    }
    const unsigned tag = rfl(gld32_wait(P.ws + WS_EPOCH, 0)) + 1;
    if (L == 1) {
        u32x4 a, b, c0, c1, c2, c3;
        for (unsigned spins = 0; spins < SPIN_CAP; ++spins) {
            asm volatile("global_load_dwordx4 %0, %6, %7 sc1\n\tglobal_load_dwordx4 %1, %6, %7 offset:16 sc1\n\t"
                         "global_load_dwordx4 %2, %8, %9 sc1\n\tglobal_load_dwordx4 %3, %8, %9 offset:16 sc1\n\t"
                         "global_load_dwordx4 %4, %8, %9 offset:32 sc1\n\tglobal_load_dwordx4 %5, %8, %9 offset:48 sc1\n\ts_waitcnt vmcnt(0)"
                         : "=&v"(a), "=&v"(b), "=&v"(c0), "=&v"(c1), "=&v"(c2), "=&v"(c3)
                         : "v"(lane * 32), "s"(P.ws + WS_G1 + tb * 128 * 16), "v"(lane * 64), "s"(P.ws + WS_G2 + cb * 128 * 32) : "memory");
            if (__all(a[1] == tag && a[3] == tag && b[1] == tag && b[3] == tag && c0[1] == tag && c0[3] == tag && c1[1] == tag && c1[3] == tag &&
                      c2[1] == tag && c2[3] == tag && c3[1] == tag && c3[3] == tag)) break;
            __builtin_amdgcn_s_sleep(1);
        }
        u32x4 ms = {a[0], a[2], b[0], b[2]};
        u32x2 cs = {__float_as_uint(__uint_as_float(c0[0]) + __uint_as_float(c0[2]) + __uint_as_float(c1[0]) + __uint_as_float(c1[2])),
                    __float_as_uint(__uint_as_float(c2[0]) + __uint_as_float(c2[2]) + __uint_as_float(c3[0]) + __uint_as_float(c3[2]))};
        *(LAS u32x4*)(uintptr_t)(lds0 + P1_MISC + lane * 16) = ms;
        *(LAS u32x2*)(uintptr_t)(lds0 + P1_MISC + 1024 + lane * 8) = cs;
        asm volatile("s_waitcnt lgkmcnt(0)" ::: "memory");
    }
    bar();
    return tag;
}

constexpr int P2_X = 131072 + 2048;
constexpr int P2_KM = P2_X + 8 * 2560;
constexpr float SAFE_RANGE = 80.f;

DI void p2_compute(char* ws, char* smem, int head, int qb, int w, int lane) {
    const int r = lane & 31, hh = lane >> 5;
    const unsigned lds0 = (unsigned)(uintptr_t)smem;
    const int qg = w & 3, kh = w >> 2;
    const int la = qg * 2 + kh, lb = qg * 2 + (1 - kh);
    cbar();
    const u32x4 qa = *(LAS const u32x4*)(uintptr_t)(lds0 + P2_X + la * 1024 + lane * 16), qbv = *(LAS const u32x4*)(uintptr_t)(lds0 + P2_X + lb * 1024 + lane * 16);
    const float kmax2 = __int_as_float(__builtin_amdgcn_readlane(__float_as_int(dpp_max<true>(*(LAS const float*)(uintptr_t)(lds0 + P2_KM + lane * 4))), 63));
    float qa2 = 0.f, qb2 = 0.f;
#pragma unroll
    for (int e = 0; e < 4; ++e) { qa2 = dot2(qa[e], qa[e], qa2); qb2 = dot2(qbv[e], qbv[e], qb2); }
    const float bnda = __builtin_amdgcn_sqrtf(sum_halves(qa2) * kmax2) * 1.01f, bndb = __builtin_amdgcn_sqrtf(sum_halves(qb2) * kmax2) * 1.01f;
    const bool fast = __all(2.f * bnda <= SAFE_RANGE && 2.f * bndb <= SAFE_RANGE);
    const unsigned ka = lds0 + kh * 32768 + lane * 16, va = lds0 + 65536 + kh * 32768 + hh * 256 + (r & 15) * 16;
    const unsigned xw = lds0 + P2_X + w * 2560 + lane * 16, xr = lds0 + P2_X + (w ^ 4) * 2560 + lane * 16;
    float o[9], ma;
    if (fast) {
        ma = bnda;
        unsigned kp = ka, vp = va, cnt;
#define EXP(r) "v_exp_f32 v" #r ", v" #r "\n\t"
#define CVT(d, a, b) "v_cvt_pk_bf16_f32 v" #d ", v" #a ", v" #b "\n\t"
#define MSQ(SN, KN, Q, C) "v_mfma_f32_32x32x16_bf16 " SN ", " KN ", " Q ", " C "\n\t"
#define MPO(O, V, P) "v_mfma_f32_32x32x16_bf16 " O ", " V ", " P ", " O "\n\t"
#define RDV(VN0, VN1, VOFF) "s_mov_b64 exec, %[msk]\n\t" "ds_read_b128 " VN0 ", %[vp] offset:" #VOFF "\n\t" "ds_read_b128 " VN1 ", %[vp] offset:" #VOFF "+512\n\t" "s_mov_b64 exec, -1\n\t"
#define RDK(KNN, KOFF) "ds_read_b128 " KNN ", %[kp] offset:" #KOFF "\n\t"
#define STEP(PRE, c0, c1, c2, c3, c4, c5, c6, c7, c8, c9, c10, c11, c12, c13, c14, c15, p14, p15, SN, KN, Q, CC, O, OP, VC0, VP1)  \
        EXP(c0) EXP(c1) CVT(127, p14, p15) EXP(c2) EXP(c3) MPO(OP, VP1, "v[124:127]") PRE                                    \
        CVT(120, c0, c1) EXP(c4) EXP(c5) CVT(121, c2, c3) EXP(c6)                                                            \
        "s_waitcnt lgkmcnt(3)\n\t" MSQ(SN, KN, Q, CC)                                                                        \
        EXP(c7) CVT(122, c4, c5) EXP(c8) EXP(c9) CVT(123, c6, c7) EXP(c10) EXP(c11)                                          \
        MPO(O, VC0, "v[120:123]")                                                                                            \
        CVT(124, c8, c9) EXP(c12) EXP(c13) CVT(125, c10, c11) EXP(c14) EXP(c15) CVT(126, c12, c13)
#define SA_ 32, 33, 34, 35, 36, 37, 38, 39, 40, 41, 42, 43, 44, 45, 46, 47
#define SB_ 48, 49, 50, 51, 52, 53, 54, 55, 56, 57, 58, 59, 60, 61, 62, 63
#define CALL(M, ...) M(__VA_ARGS__)
#define TE(KOFF, VOFF) CALL(STEP, RDV("v[112:115]", "v[116:119]", VOFF), SA_, 62, 63, "v[48:63]", "v[96:99]", "%[qb]", "v[144:159]", "v[64:79]", "v[80:95]", "v[104:107]", "v[116:119]") \
                       CALL(STEP, RDK("v[96:99]", KOFF), SB_, 46, 47, "v[32:47]", "v[100:103]", "%[qa]", "v[128:143]", "v[80:95]", "v[64:79]", "v[104:107]", "v[108:111]")
#define TO(KOFF, VOFF) CALL(STEP, RDV("v[104:107]", "v[108:111]", VOFF), SA_, 62, 63, "v[48:63]", "v[100:103]", "%[qb]", "v[144:159]", "v[64:79]", "v[80:95]", "v[112:115]", "v[108:111]") \
                       CALL(STEP, RDK("v[100:103]", KOFF), SB_, 46, 47, "v[32:47]", "v[96:99]", "%[qa]", "v[128:143]", "v[80:95]", "v[64:79]", "v[112:115]", "v[116:119]")
#define M64(a, b, c, d) "v_mov_b64 v[" #a ":" #b "], v[" #c ":" #d "]\n\t"
#define MOV4(a, b, c, d, src) "v_mov_b32 v" #a ", " src "\n\tv_mov_b32 v" #b ", " src "\n\tv_mov_b32 v" #c ", " src "\n\tv_mov_b32 v" #d ", " src "\n\t"
        asm volatile(
            "s_setprio 3\n\t"
            "ds_read_b128 v[96:99], %[kp]\n\t"
            "v_mov_b32 v128, %[na]\n\tv_mov_b32 v129, %[na]\n\tv_mov_b32 v144, %[nb]\n\tv_mov_b32 v145, %[nb]\n\t"
            "v_mov_b32 v104, %[one]\n\tv_mov_b32 v105, %[one]\n\tv_mov_b64 v[64:65], 0\n\t"
            M64(106, 107, 104, 105) M64(108, 109, 104, 105) M64(110, 111, 104, 105) M64(112, 113, 104, 105) M64(114, 115, 104, 105) M64(116, 117, 104, 105) M64(118, 119, 104, 105)
            "s_mov_b64 exec, %[msk]\n\t"
            "ds_read_b128 v[104:107], %[vp]\n\t"
            "ds_read_b128 v[108:111], %[vp] offset:512\n\t"
            "s_mov_b64 exec, -1\n\t"
            "ds_read_b128 v[100:103], %[kp] offset:1024\n\t"
            M64(130, 131, 128, 129) M64(132, 133, 128, 129) M64(134, 135, 128, 129) M64(136, 137, 128, 129) M64(138, 139, 128, 129) M64(140, 141, 128, 129) M64(142, 143, 128, 129)
            "s_waitcnt lgkmcnt(3)\n\t"
            "s_nop 1\n\t"
            "v_mfma_f32_32x32x16_bf16 v[32:47], v[96:99], %[qa], v[128:143]\n\t"
            M64(146, 147, 144, 145) M64(148, 149, 144, 145) M64(150, 151, 144, 145) M64(152, 153, 144, 145) M64(154, 155, 144, 145) M64(156, 157, 144, 145) M64(158, 159, 144, 145)
            M64(66, 67, 64, 65) M64(68, 69, 64, 65) M64(70, 71, 64, 65) M64(72, 73, 64, 65) M64(74, 75, 64, 65) M64(76, 77, 64, 65) M64(78, 79, 64, 65)
            M64(80, 81, 64, 65) M64(82, 83, 64, 65) M64(84, 85, 64, 65) M64(86, 87, 64, 65) M64(88, 89, 64, 65) M64(90, 91, 64, 65) M64(92, 93, 64, 65) M64(94, 95, 64, 65)
            M64(62, 63, 64, 65) M64(124, 125, 64, 65) M64(126, 127, 64, 65)
            "s_mov_b32 %[cnt], 0\n\t"
            "L_att_%=:\n\t"
            TE(2048, 1024) TO(3072, 2048) TE(4096, 3072) TO(5120, 4096)
            "v_add_u32 %[kp], 0x1000, %[kp]\n\t"
            "v_add_u32 %[vp], 0x1000, %[vp]\n\t"
            "s_cmp_lg_u32 %[cnt], 0\n\t"
            "s_cbranch_scc1 L_att_nb_%=\n\t"
            "s_barrier\n\t"
            "L_att_nb_%=:\n\t"
            "s_add_u32 %[cnt], %[cnt], 1\n\t"
            "s_cmp_eq_u32 %[cnt], 3\n\ts_cbranch_scc0 L_p1_%=\n\ts_setprio 2\n\tL_p1_%=:\n\t"
            "s_cmp_eq_u32 %[cnt], 6\n\ts_cbranch_scc0 L_p2_%=\n\ts_setprio 1\n\tL_p2_%=:\n\t"
            "s_cmp_eq_u32 %[cnt], 7\n\ts_cbranch_scc0 L_p3_%=\n\ts_setprio 0\n\tL_p3_%=:\n\t"
            "s_cmp_lt_u32 %[cnt], 8\n\t"
            "s_cbranch_scc1 L_att_%=\n\t"
            CVT(127, 62, 63)
            "s_nop 1\n\t"
            MPO("v[80:95]", "v[116:119]", "v[124:127]")
            "s_waitcnt lgkmcnt(0)\n\t"
            "s_nop 15\n\t"
            "v_mov_b32 v89, %[bb]\n\t"
            "v_mov_b32 %[o0], v64\n\tv_mov_b32 %[o1], v65\n\tv_mov_b32 %[o2], v66\n\tv_mov_b32 %[o3], v67\n\t"
            "v_mov_b32 %[o4], v68\n\tv_mov_b32 %[o5], v69\n\tv_mov_b32 %[o6], v70\n\tv_mov_b32 %[o7], v71\n\tv_mov_b32 %[o8], v72\n\t"
            "ds_write_b128 %[xw], v[80:83]\n\t"
            "ds_write_b128 %[xw], v[84:87] offset:1024\n\t"
            "ds_write_b64 %[xw2], v[88:89]\n\t"
            : [o0] "=&v"(o[0]), [o1] "=&v"(o[1]), [o2] "=&v"(o[2]), [o3] "=&v"(o[3]), [o4] "=&v"(o[4]), [o5] "=&v"(o[5]), [o6] "=&v"(o[6]), [o7] "=&v"(o[7]), [o8] "=&v"(o[8]),
              [kp] "+v"(kp), [vp] "+v"(vp), [cnt] "=&s"(cnt)
            : [qa] "v"(qa), [qb] "v"(qbv), [na] "v"(-bnda), [nb] "v"(-bndb), [bb] "v"(bndb), [msk] "s"(0x0000ffff0000ffffull), [xw] "v"(xw), [xw2] "v"(xw + 2048 - lane * 8), [one] "v"((r == 16 || r == 20) ? ONES : 0u)
            : "memory", "scc",
              "v32", "v33", "v34", "v35", "v36", "v37", "v38", "v39", "v40", "v41", "v42", "v43", "v44", "v45", "v46", "v47",
              "v48", "v49", "v50", "v51", "v52", "v53", "v54", "v55", "v56", "v57", "v58", "v59", "v60", "v61", "v62", "v63",
              "v64", "v65", "v66", "v67", "v68", "v69", "v70", "v71", "v72", "v73", "v74", "v75", "v76", "v77", "v78", "v79",
              "v80", "v81", "v82", "v83", "v84", "v85", "v86", "v87", "v88", "v89", "v90", "v91", "v92", "v93", "v94", "v95",
              "v96", "v97", "v98", "v99", "v100", "v101", "v102", "v103", "v104", "v105", "v106", "v107", "v108", "v109", "v110", "v111",
              "v112", "v113", "v114", "v115", "v116", "v117", "v118", "v119", "v120", "v121", "v122", "v123", "v124", "v125", "v126", "v127",
              "v128", "v129", "v130", "v131", "v132", "v133", "v134", "v135", "v136", "v137", "v138", "v139", "v140", "v141", "v142", "v143",
              "v144", "v145", "v146", "v147", "v148", "v149", "v150", "v151", "v152", "v153", "v154", "v155", "v156", "v157", "v158", "v159");
    } else {
        bar();
        const u32x4 ones = {ONES, ONES, ONES, ONES};
        const f32x16 zero = {};
#pragma unroll 1
        for (int sl = 1; sl >= 0; --sl) {
            const bf16x8 qf = __builtin_bit_cast(bf16x8, sl ? qbv : qa);
            bf16x8 v0 = __builtin_bit_cast(bf16x8, ones), v1 = v0, kf;
            f32x16 oo = {};
            float m = -INFINITY;
#pragma unroll 1
            for (int t = 0; t < 32; ++t) {
                asm volatile("ds_read_b128 %0, %3\n\ts_mov_b64 exec, %5\n\tds_read_b128 %1, %4\n\tds_read_b128 %2, %4 offset:512\n\ts_mov_b64 exec, -1\n\ts_waitcnt lgkmcnt(0)"
                             : "=&v"(kf), "+v"(v0), "+v"(v1) : "v"(ka + t * 1024), "v"(va + t * 1024), "s"(0x0000ffff0000ffffull) : "memory");
                const f32x16 s = MFMA(kf, qf, zero);
                float tm = s[0];
#pragma unroll
                for (int i = 1; i < 16; ++i) tm = fmaxf(tm, s[i]);
                tm = fmaxf(tm, other_half(tm));
                const float mn = fmaxf(m, tm);
                const float alpha = __builtin_amdgcn_exp2f(m - mn);
                m = mn;
#pragma unroll
                for (int i = 0; i < 16; ++i) oo[i] *= alpha;
                u32x4 p0, p1;
#pragma unroll
                for (int i = 0; i < 4; ++i) p0[i] = cvtpk(__builtin_amdgcn_exp2f(s[2 * i] - m), __builtin_amdgcn_exp2f(s[2 * i + 1] - m));
#pragma unroll
                for (int i = 0; i < 4; ++i) p1[i] = cvtpk(__builtin_amdgcn_exp2f(s[8 + 2 * i] - m), __builtin_amdgcn_exp2f(s[9 + 2 * i] - m));
                oo = MFMA(v0, __builtin_bit_cast(bf16x8, p0), oo);
                oo = MFMA(v1, __builtin_bit_cast(bf16x8, p1), oo);
            }
            if (sl == 1) {
                f32x4 t0 = {oo[0], oo[1], oo[2], oo[3]}, t1 = {oo[4], oo[5], oo[6], oo[7]};
                f32x2 t2 = {oo[8], m};
                *(LAS f32x4*)(uintptr_t)xw = t0; *(LAS f32x4*)(uintptr_t)(xw + 1024) = t1; *(LAS f32x2*)(uintptr_t)(xw + 2048 - lane * 8) = t2;
            } else {
#pragma unroll
                for (int i = 0; i < 9; ++i) o[i] = oo[i];
                ma = m;
            }
        }
    }
    cbar();
    {
        const f32x4 t0 = *(LAS const f32x4*)(uintptr_t)xr, t1 = *(LAS const f32x4*)(uintptr_t)(xr + 1024);
        const f32x2 t2 = *(LAS const f32x2*)(uintptr_t)(xr + 2048 - lane * 8);
        const float mo = t2[1], mn = fmaxf(ma, mo);
        const float fa = __builtin_amdgcn_exp2f(ma - mn), fo = __builtin_amdgcn_exp2f(mo - mn);
        const float inv = __builtin_amdgcn_rcpf(o[8] * fa + t2[0] * fo);
        const float ia = fa * inv, io = fo * inv;
        const u32x4 ov = pack_swap(o[0] * ia + t0[0] * io, o[1] * ia + t0[1] * io, o[2] * ia + t0[2] * io, o[3] * ia + t0[3] * io,
                                   o[4] * ia + t1[0] * io, o[5] * ia + t1[1] * io, o[6] * ia + t1[2] * io, o[7] * ia + t1[3] * io);
        gst128(ws + WS_OF + ((size_t)(qb * 8 + la) * 32 + head) * 1024, lane * 16, ov);
    }
}

constexpr int P3_X = 153600;
template <int MODE>
DI void p35_compute(const Ptrs& P, char* smem, int tb, int cb, int w, int lane, u32x4& hkeep) {
    constexpr int A0 = MODE == 0 ? 65536 : 98304, XA = MODE == 0 ? 131072 : 147456;
    char* ws = P.ws;
    const int r = lane & 31, hh = lane >> 5;
    const unsigned lds0 = (unsigned)(uintptr_t)smem;
    const int ta = w & 1, cj = (w >> 1) & 1, kh = w >> 2;
    const int tile = tb * 2 + ta, token = tile * 32 + r, n0 = cb * 64 + cj * 32 + kh * 16;
    u32x2 res[2];
    const unsigned aro = lds0 + A0 + ta * 8192 + lane * 16 + kh * 4096;
    const unsigned wro = lds0 + hh * 1024 + ((lane & 15) >> 2) * 64 + ((lane >> 4) & 1) * 32 + (lane & 3) * 8 + cj * 256 + kh * 8192;
    f32x16 acc = {};
    bf16x8 A[4], Wf[4];
#define P35_LD(S, BA, BW, ks) { A[S] = lds_b128(aro + (BA) + (ks) * 1024); Wf[S] = lds_trfrag(wro + (BW) + (ks) * 2048, 512); }
    if constexpr (MODE == 0) {
        cbar();
        P35_LD(0, 0, 0, 0); P35_LD(1, 0, 0, 1);
        unsigned BA = 0;
#pragma unroll 1
        for (int ch = 0; ch < 4; ++ch) {
            P35_LD(2, BA, BA, 2); SB(); acc = MFMA(Wf[0], A[0], acc); SB();
            P35_LD(3, BA, BA, 3); SB(); acc = MFMA(Wf[1], A[1], acc); SB();
            if (ch < 3) {
                cbar();
                BA += 16384;
                P35_LD(0, BA, BA, 0);
            }
            SB(); acc = MFMA(Wf[2], A[2], acc); SB();
            if (ch < 3) { P35_LD(1, BA, BA, 1); }
            SB(); acc = MFMA(Wf[3], A[3], acc); SB();
        }
    } else {
        bf16x8 WR[19];
        bar();
#pragma unroll
        for (int q = 0; q < 16; ++q) WR[q] = lds_trfrag(wro + (q >> 2) * 16384 + (q & 3) * 2048, 512);
#pragma unroll
        for (int j = 0; j < 3; ++j) WR[16 + j] = lds_trfrag(wro + 4 * 16384 - kh * 8192 + (kh * 3 + j) * 2048, 512);
        cbar();
        cbar();
        P35_LD(0, 0, 0, 0); P35_LD(1, 0, 0, 1);
        unsigned BA = 0, BW = 0;
#define P5_ADV() { BA = BA == 2 * 16384 ? 0 : BA + 16384; }
#define P5_LDA(S, ks) { A[S] = lds_b128(aro + BA + (ks) * 1024); }
#pragma unroll 1
        for (int ch = 0; ch < 5; ++ch) {
            P35_LD(2, BA, BW, 2); SB(); acc = MFMA(Wf[0], A[0], acc); SB();
            P35_LD(3, BA, BW, 3); SB(); acc = MFMA(Wf[1], A[1], acc); SB();
            cbar();
            P5_ADV(); BW += 16384;
            P35_LD(0, BA, BW, 0); SB(); acc = MFMA(Wf[2], A[2], acc); SB();
            P35_LD(1, BA, BW, 1); SB(); acc = MFMA(Wf[3], A[3], acc); SB();
        }
        P35_LD(2, BA, BW, 2); SB(); acc = MFMA(Wf[0], A[0], acc); SB();
        P35_LD(3, BA, BW, 3); SB(); acc = MFMA(Wf[1], A[1], acc); SB();
        cbar();
        P5_ADV();
        P5_LDA(0, 0); SB(); acc = MFMA(Wf[2], A[2], acc); SB();
        P5_LDA(1, 1); SB(); acc = MFMA(Wf[3], A[3], acc); SB();
#pragma unroll
        for (int c = 0; c < 4; ++c) {
            P5_LDA(2, 2); SB(); acc = MFMA(WR[4 * c], A[0], acc); SB();
            P5_LDA(3, 3); SB(); acc = MFMA(WR[4 * c + 1], A[1], acc); SB();
            cbar();
            P5_ADV();
            if (c < 3) { P5_LDA(0, 0); }
            SB(); acc = MFMA(WR[4 * c + 2], A[2], acc); SB();
            if (c < 3) { P5_LDA(1, 1); }
            SB(); acc = MFMA(WR[4 * c + 3], A[3], acc); SB();
        }
#pragma unroll
        for (int j = 0; j < 3; ++j) acc = MFMA(WR[16 + j], lds_b128(aro + BA - kh * 4096 + (kh * 3 + j) * 1024), acc);
    }
    {
        LAS f32x4* xw = (LAS f32x4*)(smem + XA + w * 2048 + lane * 16);
        f32x4 s0, s1;
#pragma unroll
        for (int e = 0; e < 4; ++e) { s0[e] = kh ? acc[e] : acc[8 + e]; s1[e] = kh ? acc[4 + e] : acc[12 + e]; }
        xw[0] = s0; xw[64] = s1;
    }
    cbar();
    float v[8];
    {
        LAS const f32x4* xr = (LAS const f32x4*)(smem + XA + (w ^ 4) * 2048 + lane * 16);
        const f32x4 s0 = xr[0], s1 = xr[64];
        if constexpr (MODE == 0) {
            const int row = ta * 32 + r;
#pragma unroll
            for (int g = 0; g < 2; ++g)
                res[g] = *(LAS const u32x2*)(smem + P3_X + row * 128 + (((cj * 4 + kh * 2 + g) ^ ((row >> 1) & 7)) << 4) + hh * 8);
        } else { res[0][0] = hkeep[0]; res[0][1] = hkeep[1]; res[1][0] = hkeep[2]; res[1][1] = hkeep[3]; }
#pragma unroll
        for (int e = 0; e < 4; ++e) { v[e] = (kh ? acc[8 + e] : acc[e]) + s0[e]; v[4 + e] = (kh ? acc[12 + e] : acc[4 + e]) + s1[e]; }
#pragma unroll
        for (int g = 0; g < 2; ++g) {
            v[4 * g] += bflo(res[g][0]); v[4 * g + 1] += bfhi(res[g][0]); v[4 * g + 2] += bflo(res[g][1]); v[4 * g + 3] += bfhi(res[g][1]);
        }
    }
    const unsigned p0 = cvtpk(v[0], v[1]), p1 = cvtpk(v[2], v[3]), p2 = cvtpk(v[4], v[5]), p3 = cvtpk(v[6], v[7]);
    const u32x4 o = swap4(p0, p1, p2, p3);
    if constexpr (MODE == 0) {
        hkeep[0] = p0; hkeep[1] = p1; hkeep[2] = p2; hkeep[3] = p3;
        gst128(ws + WS_HF + ((size_t)tile * 32 + (n0 >> 4)) * 1024, lane * 16, o);
        float s1 = 0.f, s2 = 0.f;
#pragma unroll
        for (int e = 0; e < 4; ++e) { s1 = dot2(o[e], ONES, s1); s2 = dot2(o[e], o[e], s2); }
        s1 = sum_halves(s1); s2 = sum_halves(s2);
        LAS f32x2* sl = (LAS f32x2*)(smem + XA + 8 * 2048);
        if (lane < 32) { f32x2 sv = {s1, s2}; sl[(ta * 32 + r) * 4 + cj * 2 + kh] = sv; }
        cbar();
        if (w == 0) {
            const f32x4 p = *(LAS const f32x4*)(sl + lane * 4), q = *(LAS const f32x4*)(sl + lane * 4 + 2);
            u32x2 sv = {__float_as_uint(p[0] + p[2] + q[0] + q[2]), __float_as_uint(p[1] + p[3] + q[1] + q[3])};
            gst64(ws + WS_ST + (size_t)tb * 64 * 64 + cb * 8, lane * 64, sv);
        }
    } else {
        *(u32x4*)(P.out + (size_t)token * 1024 + (n0 + 8 * hh) * 2) = o;
    }
}

constexpr int P4_ST = 147456, P4_CS = P4_ST + 8192;
template <int NB>
DI void p4_compute(char* ws, char* smem, int tb, int cg, int w, int lane) {
    const int r = lane & 31, hh = lane >> 5, i = w & 3, j0 = NB == 2 ? 0 : 2;
    const unsigned lds0 = (unsigned)(uintptr_t)smem;
    const int tile = tb * 4 + i;
    const unsigned hro = lds0 + 98304 + i * 4096 + lane * 16;
    const unsigned wro = lds0 + hh * 1536 + ((lane & 15) >> 2) * 64 + ((lane >> 4) & 1) * 32 + (lane & 3) * 8 + j0 * 256;
    f32x16 ag[NB], au[NB];
#pragma unroll
    for (int j = 0; j < NB; ++j) { ag[j] = f32x16{}; au[j] = f32x16{}; }
    bf16x8 H[4], G[4][NB], U[4][NB];
#define P4_LOAD(S, BH, BW, ks)                                                                                                    \
    {                                                                                                                             \
        H[S] = lds_b128(hro + (BH) + (ks) * 1024);                                                                                 \
        _Pragma("unroll") for (int j = 0; j < NB; ++j) { G[S][j] = lds_trfrag(wro + (BW) + (ks) * 3072 + j * 256, 768); U[S][j] = lds_trfrag(wro + (BW) + 12288 + (ks) * 3072 + j * 256, 768); } \
    }
#define P4_MMA(S) { _Pragma("unroll") for (int j = 0; j < NB; ++j) { ag[j] = MFMA(G[S][j], H[S], ag[j]); au[j] = MFMA(U[S][j], H[S], au[j]); } }
    cbar();
    P4_LOAD(0, 0, 0, 0); P4_LOAD(1, 0, 0, 1);
    unsigned BH = 0, BW = 0;
#pragma unroll 1
    for (int ch = 0; ch < 8; ++ch) {
        P4_LOAD(2, BH, BW, 2); SB(); P4_MMA(0); SB();
        P4_LOAD(3, BH, BW, 3); SB(); P4_MMA(1); SB();
        cbar();
        if (ch < 7) {
            BH = BH == 2 * 16384 ? 0 : BH + 16384; BW = BW == 3 * 24576 ? 0 : BW + 24576;
            P4_LOAD(0, BH, BW, 0);
        }
        SB(); P4_MMA(2); SB();
        if (ch < 7) { P4_LOAD(1, BH, BW, 1); }
        SB(); P4_MMA(3); SB();
    }
    float mean, rstd;
    {
        float s1 = 0.f, s2 = 0.f;
        const LAS f32x4* sp = (const LAS f32x4*)(smem + P4_ST + (i * 32 + r) * 64);
#pragma unroll
        for (int k = 0; k < 4; ++k) { const f32x4 v = sp[k]; s1 += v[0] + v[2]; s2 += v[1] + v[3]; }
        mean = s1 * (1.f / DM);
        rstd = __builtin_amdgcn_rsqf(fmaxf(s2 * (1.f / DM) - mean * mean, 0.f) + LN_EPS);
    }
    constexpr float C1 = -2.f * 0.7978845608028654f * LOG2E, C2 = C1 * 0.044715f;
    const float k1 = C1 * rstd, k2 = C2 * rstd * rstd * rstd, k3 = __builtin_amdgcn_rcpf(rstd * rstd);
    const f32x2 nm2 = {-mean, -mean}, k12 = {k1, k1}, k22 = {k2, k2}, k32 = {k3, k3};
#pragma unroll
    for (int j = 0; j < NB; ++j) {
        const int blk = cg * 3 + j0 + j;
        float v[16];
#pragma unroll
        for (int g = 0; g < 4; ++g) {
            const LAS f32x4* cp = (const LAS f32x4*)(smem + P4_CS + ((j0 + j) * 32 + 8 * g + 4 * hh) * 4);
            const f32x4 cgf = cp[0], cuf = cp[32];
#pragma unroll
            for (int e = 0; e < 4; e += 2) {
                const f32x2 ga = {ag[j][4 * g + e], ag[j][4 * g + e + 1]}, ua = {au[j][4 * g + e], au[j][4 * g + e + 1]};
                const f32x2 cg2 = {cgf[e], cgf[e + 1]}, cu2 = {cuf[e], cuf[e + 1]};
                const f32x2 a = __builtin_elementwise_fma(nm2, cg2, ga), bq = __builtin_elementwise_fma(nm2, cu2, ua);
                const f32x2 arg = a * __builtin_elementwise_fma(a * a, k22, k12);
                const f32x2 ex = {__builtin_amdgcn_exp2f(arg[0]), __builtin_amdgcn_exp2f(arg[1])};
                const f32x2 den = __builtin_elementwise_fma(ex, k32, k32);
                const f32x2 rc = {__builtin_amdgcn_rcpf(den[0]), __builtin_amdgcn_rcpf(den[1])};
                const f32x2 y = (a * rc) * bq;
                v[4 * g + e] = y[0]; v[4 * g + e + 1] = y[1];
            }
        }
        if (blk < 43) {
#pragma unroll
            for (int g = 0; g < 2; ++g) {
                const u32x4 o = pack_swap(v[8 * g], v[8 * g + 1], v[8 * g + 2], v[8 * g + 3], v[8 * g + 4], v[8 * g + 5], v[8 * g + 6], v[8 * g + 7]);
                gst128(ws + WS_AF + ((size_t)tile * 86 + blk * 2 + g) * 1024, lane * 16, o);
            }
        }
    }
}

__global__ __launch_bounds__(640, 3) void k_block(const char* __restrict__ x, const char* __restrict__ wqkv, const char* __restrict__ wo, const char* __restrict__ wg,
                                                  const char* __restrict__ wu, const char* __restrict__ wd, char* __restrict__ ws, char* __restrict__ out) {
    extern __shared__ __attribute__((aligned(16))) char smem[];
    const Ptrs P = {x, wqkv, wo, wg, wu, wd, ws, out};
    const int b = blockIdx.x, xcd = b & 7, idx = b >> 3;
    const int lane = threadIdx.x & 63, w = rfl(threadIdx.x >> 6);
    const unsigned lds0 = (unsigned)(uintptr_t)smem;
    if (w >= 8 && blockIdx.x >= 192) {
        const char* pc = (const char*)(__builtin_amdgcn_s_getpc() & ~127ull);
        asm volatile("s_mov_b32 m0, %0\n\ts_nop 0" ::"s"(lds0 + LDS_BYTES - 256) : "memory");
#pragma unroll
        for (int i = 0; i < (CODE_LINES + 63) / 64; ++i) { const int ln = i * 64 + lane; gtouch(pc, (ln < CODE_LINES ? ln : CODE_LINES - 1) * 128); }
    }
    const bool p1_gemm = idx < 24;
    const int gi = idx;
    const int tb1 = (xcd >> 1) * 4 + (gi & 3), cb1 = (xcd & 1) * 6 + (gi >> 2);
    const int head = xcd * 4 + (idx >> 3), qb = idx & 7;
    const int tb3 = xcd * 4 + (idx & 3), cb3 = idx >> 2;
    const bool p4_on = !(xcd & 1) || idx < 28;
    const int tb4 = (xcd >> 1) * 4 + (idx & 3), cg4 = (xcd & 1) * 8 + (idx >> 2);

    if (w < 8) {
        if (p1_gemm) {
            f32x16 accA, accB;
            if (w >= 4) p1_colsum(wqkv, ws, tb1, cb1, smem, w - 4, lane, accA, accB);
            else if (cb1 < 8) p1_compute<false>(ws, tb1, cb1, smem, w, lane, accA, accB); else p1_compute<true>(ws, tb1, cb1, smem, w, lane, accA, accB);
            const int tl = 2 * (w & 1) + (w >> 2), cp = (w >> 1) & 1;
            if (cb1 < 8) p1_epilogue<false>(ws, tb1, cb1, smem, tl, cp, accA, accB, lane); else p1_epilogue<true>(ws, tb1, cb1, smem, tl, cp, accA, accB, lane);
        } else p1_helper(x, wqkv, wg, wu, ws, (idx - 24) * 8 + xcd, w, lane);
        pbar();
        p2_compute(ws, smem, head, qb, w, lane);
        pbar();
        u32x4 hkeep;
        p35_compute<0>(P, smem, tb3, cb3, w, lane, hkeep);
        pbar();
        if (p4_on) {
            if (w < 4) p4_compute<2>(ws, smem, tb4, cg4, w, lane); else p4_compute<1>(ws, smem, tb4, cg4, w, lane);
        } else bar();
        pbar();
        p35_compute<1>(P, smem, tb3, cb3, w, lane, hkeep);
        return;
    }

    const int L = w - 8;
    const char* flags = ws + WS_FLAG;
    const unsigned lin = lane * 16;
    if (!p1_gemm && L == 0) small_touch(ws, b, lane, lds0);
    const unsigned tag = p1_gemm ? p1_load(P, L, tb1, cb1, lds0, lane) : rfl(gld32_wait(ws + WS_EPOCH, 0)) + 1;
    bar();
    if (L == 0 && lane == 0) gst32(ws + WS_FLAG, b * 128, tag);
    {
        const int ptb = lane < 32 ? (lane & 15) : 2 * qb + (lane & 1), pcb = (lane < 16 ? 4 : lane < 32 ? 8 : 0) + (head >> 3);
        const int pb = ((pcb % 6) * 4 + (ptb & 3)) * 8 + (ptb >> 2) * 2 + pcb / 6;
        poll(flags, pb * 128, lane < 34, tag);
        const char* src = ws + (L == 0 ? WS_KF : WS_VF) + (size_t)head * 65536;
        const unsigned dst = lds0 + L * 65536;
        {
            const char* q = ws + WS_QF + ((size_t)head * 64 + qb * 8) * 1024 + L * 4096;
            dma4lin(q, lin, lds0 + P2_X + L * 4096);
            if (L == 0) dma1dw(ws + WS_KM + head * 256, lane * 4, lds0 + P2_KM);
        }
        dma4lin(src, lin, dst); dma4lin(src + 32768, lin, dst + 32768);
        dma2lin(src + 4096, lin, dst + 4096); dma2lin(src + 32768 + 4096, lin, dst + 32768 + 4096);
        lbar<0>();
        dma2lin(src + 6144, lin, dst + 6144); dma2lin(src + 32768 + 6144, lin, dst + 32768 + 6144);
#pragma unroll 1
        for (int g = 2; g < 8; ++g) {
            dma4lin(src + g * 4096, lin, dst + g * 4096); dma4lin(src + 32768 + g * 4096, lin, dst + 32768 + g * 4096);
        }
        lbar<0>();
        if (L == 1) {
#pragma unroll 1
            for (int g = 0; g < 4; ++g) poll(flags, (g * 64 + lane) * 128, true, tag);
            if (b == 0 && lane == 0) gst32(ws + WS_EPOCH, 0, tag);
            asm volatile("s_mov_b32 m0, %0\n\ts_nop 0" ::"s"(lds0 + LDS_BYTES - 256) : "memory");
            gtouch(wo, (idx * 128 + lane) * 128); gtouch(wo, (idx * 128 + 64 + lane) * 128);
            {
                const int nl = (xcd & 1) ? 10 : 12, ln = lane & 15;
                const unsigned co = (xcd & 1) * 1536 + (ln < nl ? ln : 0) * 128;
#pragma unroll
                for (int i = 0; i < 4; ++i) {
                    unsigned o_ = (idx * 16 + i * 4 + (lane >> 4)) * (FF * 2) + co;
                    o_ = o_ > 512u * FF * 2 - 4 ? 512u * FF * 2 - 4 : o_;
                    gtouch(wg, o_); gtouch(wu, o_);
                }
            }
#pragma unroll
            for (int i = 0; i < 6; ++i) { int ln = i * 64 + lane; ln = ln > 343 ? 343 : ln; gtouch(wd, (idx * 344 + ln) * 128); }
        }
        bar();
    }
    unsigned wvo[4];
    {
        const unsigned wv = ((lane >> 5) * 4 + ((lane >> 2) & 3)) * 1024 + ((lane >> 4) & 1) * 64 + (lane & 3) * 16;
#pragma unroll
        for (int i = 0; i < 4; ++i) wvo[i] = wv + i * 8192 - i * 1024;
    }
    {
        bar();
        if (L == 1) {
            {
                const int rl = lane >> 3, cs = lane & 7;
                unsigned xv[4];
#pragma unroll
                for (int i = 0; i < 4; ++i) xv[i] = (8 * i + rl) * 1024 + ((cs ^ ((4 * (i & 1) + (rl >> 1)) & 7)) << 4) - i * 1024;
                const char* xs = x + (size_t)tb3 * 64 * 1024 + cb3 * 128;
                dma4(xs, xv[0], xv[1], xv[2], xv[3], lds0 + P3_X); dma4(xs + 32768, xv[0], xv[1], xv[2], xv[3], lds0 + P3_X + 4096);
            }
#pragma unroll 1
            for (int c = 0; c < 4; ++c) {
                const char* src = wo + cb3 * 128 + (size_t)c * 128 * 1024;
#pragma unroll
                for (int g = 0; g < 4; ++g) dma4(src + g * 32768, wvo[0], wvo[1], wvo[2], wvo[3], lds0 + c * 16384 + g * 4096);
            }
        }
        if (L == 0) {
            if (lane == 0) gst32(ws + WS_FLAG + 256 * 128, b * 128, tag);
            const int ph = lane & 31;
            poll(flags + 256 * 128, (((ph & 3) * 8 + (tb3 >> 2)) * 8 + (ph >> 2)) * 128, true, tag);
            const char* ab = ws + WS_OF + (size_t)tb3 * 2 * 32 * 1024;
#pragma unroll 1
            for (int c = 0; c < 4; ++c) {
                const char* src = ab + c * 8192;
                const unsigned dst = lds0 + 65536 + c * 16384;
                dma4lin(src, lin, dst); dma4lin(src + 4096, lin, dst + 4096); dma4lin(src + 32768, lin, dst + 8192); dma4lin(src + 32768 + 4096, lin, dst + 12288);
                if (c == 1) lbar<16>();
            }
            lbar<32>(); lbar<16>(); lbar<0>();
        } else {
            lbar<0>(); bar(); bar(); bar();
        }
        bar();
    }
    {
        unsigned gvo[12];
        if (L == 1 && p4_on) {
#pragma unroll
            for (int j = 0; j < 12; ++j) {
                const int s = j * 64 + lane, st = s >> 4, kb = st / 3, nb = st % 3, q = (s >> 2) & 3, c = s & 3;
                int blk = cg4 * 3 + nb; blk = blk > 42 ? 42 : blk;
                gvo[j] = (4 * kb + q) * (FF * 2) + blk * 64 + c * 16 - (j & 3) * 1024;
            }
        }
        auto issue_w = [&](int c, unsigned B) {
            const size_t so = (size_t)c * 64 * (FF * 2);
#pragma unroll
            for (int g = 0; g < 3; ++g) dma4(wg + so, gvo[4 * g], gvo[4 * g + 1], gvo[4 * g + 2], gvo[4 * g + 3], lds0 + B + g * 4096);
#pragma unroll
            for (int g = 0; g < 3; ++g) dma4(wu + so, gvo[4 * g], gvo[4 * g + 1], gvo[4 * g + 2], gvo[4 * g + 3], lds0 + B + 12288 + g * 4096);
        };
        bar();
        bar();
        if (L == 1 && p4_on) { issue_w(0, 0); issue_w(1, 24576); issue_w(2, 2 * 24576); issue_w(3, 3 * 24576); }
        if (L == 0) { if (lane == 0) gst32(ws + WS_FLAG + 2 * 256 * 128, b * 128, tag); }
        if (p4_on) {
            if (L == 0) {
                const int ptb = tb4 * 2 + (lane & 1), pcb = (lane >> 1) & 7;
                poll(flags + 2 * 256 * 128, ((pcb * 4 + (ptb & 3)) * 8 + (ptb >> 2)) * 128, true, tag);
                const char* hb = ws + WS_HF + (size_t)tb4 * 4 * 32 * 1024;
                auto issue_h = [&](int c, unsigned B) {
                    const char* src = hb + c * 4096;
#pragma unroll
                    for (int i = 0; i < 4; ++i) dma4lin(src + i * 32768, lin, lds0 + 98304 + B + i * 4096);
                };
                issue_h(0, 0); issue_h(1, 16384);
                lbar<16>();
                {
                    const char* sb = ws + WS_ST + (size_t)tb4 * 128 * 64;
                    dma4lin(sb, lin, lds0 + P4_ST); dma4lin(sb + 4096, lin, lds0 + P4_ST + 4096);
                    const char* cb_ = ws + WS_CS + cg4 * 384;
                    dma1dw(cb_, lane * 4, lds0 + P4_CS); dma1dw(cb_ + 256, lane * 4, lds0 + P4_CS + 256);
                    dma1dw(cb_ + FF * 4, lane * 4, lds0 + P4_CS + 512); dma1dw(cb_ + FF * 4 + 256, lane * 4, lds0 + P4_CS + 768);
                }
                issue_h(2, 2 * 16384);
                unsigned B = 0;
#pragma unroll 1
                for (int c = 1; c < 8; ++c) {
                    if (c < 7) lbar<16>(); else lbar<0>();
                    if (c + 2 < 8) { issue_h(c + 2, B); B = B == 2 * 16384 ? 0 : B + 16384; }
                }
            } else {
                unsigned B = 0;
#pragma unroll 1
                for (int c = 0; c < 8; ++c) {
                    if (c == 0) lbar<63>(); else if (c < 6) lbar<48>(); else if (c == 6) lbar<24>(); else lbar<0>();
                    if (c >= 1 && c + 3 < 8) { issue_w(c + 3, B); B = B == 3 * 24576 ? 0 : B + 24576; }
                }
            }
            bar();
        } else bar();
    }
    {
        auto issue_w = [&](int c, unsigned B) {
            const char* src = wd + cb3 * 128 + (size_t)c * 128 * 1024;
#pragma unroll
            for (int g = 0; g < 3; ++g) dma4(src + g * 32768, wvo[0], wvo[1], wvo[2], wvo[3], lds0 + B + g * 4096);
            dma4(src + (c < 10 ? 3 : 2) * 32768, wvo[0], wvo[1], wvo[2], wvo[3], lds0 + B + 3 * 4096);
        };
        if (L == 1) {
#pragma unroll 1
            for (int c = 6; c < 11; ++c) issue_w(c, (c - 6) * 16384);
        }
        bar();
        if (L == 0 && p4_on && lane == 0) gst32(ws + WS_FLAG + 3 * 256 * 128, b * 128, tag);
        if (L == 1) lbar<0>(); else bar();
        bar();
        if (L == 1) {
#pragma unroll 1
            for (int c = 0; c < 6; ++c) issue_w(c, c * 16384);
        }
        if (L == 0) {
            const int ptb = tb3 >> 1, pcg = lane < 15 ? lane : 0;
            poll(flags + 3 * 256 * 128, (((pcg & 7) * 4 + (ptb & 3)) * 8 + (ptb >> 2) * 2 + (pcg >> 3)) * 128, true, tag);
            const char* ab = ws + WS_AF + (size_t)tb3 * 2 * 86 * 1024;
            auto issue_a = [&](int c, unsigned B) {
                const char* src = ab + c * 8192;
                const unsigned dst = lds0 + 98304 + B;
                const int o2 = c < 10 ? 4096 : 2048;
                dma4lin(src, lin, dst); dma4lin(src + o2, lin, dst + o2);
                dma4lin(src + 86 * 1024, lin, dst + 8192); dma4lin(src + 86 * 1024 + o2, lin, dst + 8192 + o2);
            };
            issue_a(0, 0); issue_a(1, 16384);
            unsigned B = 2 * 16384;
#pragma unroll 1
            for (int c = 0; c < 11; ++c) {
                if (c < 10) lbar<16>(); else lbar<0>();
                if (c + 2 < 11) { issue_a(c + 2, B); B = B == 2 * 16384 ? 0 : B + 16384; }
            }
        } else {
            lbar<63>(); lbar<63>(); lbar<48>(); lbar<32>(); lbar<16>(); lbar<0>();
            bar(); bar(); bar(); bar(); bar();
        }
        bar();
    }
}

extern "C" void kernel_launch(void* const* d_in, const int* in_sizes, int n_in, void* d_out, int out_size, void* d_ws, size_t ws_size, hipStream_t stream) {
    (void)hipFuncSetAttribute((const void*)k_block, hipFuncAttributeMaxDynamicSharedMemorySize, LDS_BYTES);
    k_block<<<256, 640, LDS_BYTES, stream>>>((const char*)d_in[0], (const char*)d_in[1], (const char*)d_in[2], (const char*)d_in[4], (const char*)d_in[5], (const char*)d_in[6],
                                             (char*)d_ws, (char*)d_out);
}
```

```cpp
#include <hip/hip_runtime.h>
#include <hip/hip_bf16.h>
#include <stdint.h>

typedef __bf16 bf16x8 __attribute__((ext_vector_type(8)));
typedef __bf16 bf16x2 __attribute__((ext_vector_type(2)));
typedef short s16x4 __attribute__((ext_vector_type(4)));
typedef short s16x8 __attribute__((ext_vector_type(8)));
typedef float f32x16 __attribute__((ext_vector_type(16)));
typedef float f32x4 __attribute__((ext_vector_type(4)));
typedef float f32x2 __attribute__((ext_vector_type(2)));
typedef unsigned u32x4 __attribute__((ext_vector_type(4)));
typedef unsigned u32x2 __attribute__((ext_vector_type(2)));
#define LAS __attribute__((address_space(3)))
#define DI __device__ __forceinline__

constexpr int DM = 512, FF = 1376;
constexpr float LOG2E = 1.4426950408889634f;
constexpr float QSCALE = 0.25f * LOG2E;
constexpr float LN_EPS = 1e-5f;

constexpr size_t WS_QF = 0, WS_KF = 2u << 20, WS_VF = 4u << 20, WS_OF = 6u << 20, WS_HF = 8u << 20, WS_AF = 10u << 20,
                 WS_ST = 16u << 20, WS_CS = 17u << 20, WS_KM = (17u << 20) + 65536, WS_FLAG = 18u << 20, WS_EPOCH = WS_FLAG + 4 * 256 * 128,
                 WS_G1 = 19u << 20, WS_G2 = WS_G1 + 65536;
constexpr int LDS_BYTES = 163840;
constexpr unsigned SPIN_CAP = 1u << 17;
constexpr int CODE_LINES = 288;

#define MFMA(a, b, c) __builtin_amdgcn_mfma_f32_32x32x16_bf16(a, b, c, 0, 0, 0)
#define SB() __builtin_amdgcn_sched_barrier(0)
DI void cbar() { asm volatile("s_waitcnt lgkmcnt(0)\n\ts_barrier" ::: "memory"); }
DI void pbar() { asm volatile("s_waitcnt vmcnt(0) lgkmcnt(0)\n\ts_barrier" ::: "memory"); }
template <int N> DI void lbar() { asm volatile("s_waitcnt vmcnt(%0)\n\ts_barrier" ::"i"(N > 63 ? 63 : N) : "memory"); }
DI void bar() { asm volatile("s_barrier" ::: "memory"); }

DI void dma4(const void* sbase, unsigned v0, unsigned v1, unsigned v2, unsigned v3, unsigned dst) {
    asm volatile("s_mov_b32 m0, %5\n\ts_nop 0\n\tglobal_load_lds_dwordx4 %0, %4 sc1\n\tglobal_load_lds_dwordx4 %1, %4 offset:1024 sc1\n\t"
                 "global_load_lds_dwordx4 %2, %4 offset:2048 sc1\n\tglobal_load_lds_dwordx4 %3, %4 offset:3072 sc1"
                 :: "v"(v0), "v"(v1), "v"(v2), "v"(v3), "s"(sbase), "s"(dst) : "memory");
}
DI void dma4lin(const void* sbase, unsigned v, unsigned dst) { dma4(sbase, v, v, v, v, dst); }
DI void dma2lin(const void* sbase, unsigned v, unsigned dst) {
    asm volatile("s_mov_b32 m0, %2\n\ts_nop 0\n\tglobal_load_lds_dwordx4 %0, %1 sc1\n\tglobal_load_lds_dwordx4 %0, %1 offset:1024 sc1" :: "v"(v), "s"(sbase), "s"(dst) : "memory");
}
DI void dma1dw(const void* sbase, unsigned v, unsigned dst) {
    asm volatile("s_mov_b32 m0, %2\n\ts_nop 0\n\tglobal_load_lds_dword %0, %1 sc1" :: "v"(v), "s"(sbase), "s"(dst) : "memory");
}
DI void gtouch(const void* sbase, unsigned voff) {
    asm volatile("global_load_lds_dword %0, %1" :: "v"(voff), "s"(sbase) : "memory");
}
DI unsigned gld32_wait(const void* sbase, unsigned voff) {
    unsigned r; asm volatile("global_load_dword %0, %1, %2 sc1\n\ts_waitcnt vmcnt(0)" : "=&v"(r) : "v"(voff), "s"(sbase) : "memory"); return r;
}
DI void gst128(void* sbase, unsigned voff, u32x4 v) { asm volatile("global_store_dwordx4 %0, %1, %2 sc1\n\ts_nop 1" ::"v"(voff), "v"(v), "s"(sbase) : "memory"); }
DI void gst64(void* sbase, unsigned voff, u32x2 v) { asm volatile("global_store_dwordx2 %0, %1, %2 sc1\n\ts_nop 1" ::"v"(voff), "v"(v), "s"(sbase) : "memory"); }
DI void gst32(void* sbase, unsigned voff, unsigned v) { asm volatile("global_store_dword %0, %1, %2 sc1\n\ts_nop 1" ::"v"(voff), "v"(v), "s"(sbase) : "memory"); }
DI unsigned rfl(unsigned x) { return __builtin_amdgcn_readfirstlane(x); }
DI unsigned cvtpk(float lo, float hi) {
    f32x2 v = {lo, hi};
    bf16x2 b = __builtin_convertvector(v, bf16x2);
    return __builtin_bit_cast(unsigned, b);
}
DI float bflo(unsigned u) { return __uint_as_float(u << 16); }
DI float bfhi(unsigned u) { return __uint_as_float(u & 0xffff0000u); }
DI bf16x8 lds_b128(unsigned a) { return *(LAS const bf16x8*)(uintptr_t)a; }
typedef short v4i16_t __attribute__((ext_vector_type(4)));
DI s16x4 lds_tr(unsigned a) { return __builtin_bit_cast(s16x4, __builtin_amdgcn_ds_read_tr16_b64_v4i16((LAS v4i16_t*)(uintptr_t)a)); }
DI bf16x8 lds_trfrag(unsigned a, int off2) {
    s16x4 x = lds_tr(a), y = lds_tr(a + off2);
    s16x8 r = __builtin_shufflevector(x, y, 0, 1, 2, 3, 4, 5, 6, 7);
    return __builtin_bit_cast(bf16x8, r);
}
DI float dot2(unsigned a, unsigned b, float c) {
    return __builtin_amdgcn_fdot2_f32_bf16(__builtin_bit_cast(bf16x2, a), __builtin_bit_cast(bf16x2, b), c, false);
}
DI float other_half(float v) {
    auto r = __builtin_amdgcn_permlane32_swap(__float_as_uint(v), __float_as_uint(v), false, false);
    return __uint_as_float((threadIdx.x & 32) ? r[0] : r[1]);
}
DI float sum_halves(float v) {
    auto r = __builtin_amdgcn_permlane32_swap(__float_as_uint(v), __float_as_uint(v), false, false);
    return __uint_as_float(r[0]) + __uint_as_float(r[1]);
}
template <bool ALL> DI float dpp_max(float v) {
#define DPPMAX(ctrl, rm) v = fmaxf(v, __int_as_float(__builtin_amdgcn_update_dpp(0, __float_as_int(v), ctrl, rm, 0xf, true)))
    DPPMAX(0x111, 0xf); DPPMAX(0x112, 0xf); DPPMAX(0x114, 0xf); DPPMAX(0x118, 0xf); DPPMAX(0x142, 0xa);
    if (ALL) DPPMAX(0x143, 0xc);
    return v;
}
template <int CTRL> DI float dpp_add(float v) { return v + __int_as_float(__builtin_amdgcn_update_dpp(0, __float_as_int(v), CTRL, 0xf, 0xf, true)); }
DI u32x4 swap4(unsigned p0, unsigned p1, unsigned p2, unsigned p3) {
    auto s0 = __builtin_amdgcn_permlane32_swap(p0, p2, false, false);
    auto s1 = __builtin_amdgcn_permlane32_swap(p1, p3, false, false);
    u32x4 r = {s0[0], s1[0], s0[1], s1[1]};
    return r;
}
DI u32x4 pack_swap(float a0, float a1, float a2, float a3, float a4, float a5, float a6, float a7) {
    return swap4(cvtpk(a0, a1), cvtpk(a2, a3), cvtpk(a4, a5), cvtpk(a6, a7));
}
constexpr unsigned ONES = 0x3f803f80u;

DI void poll(const char* fl, unsigned voff, bool active, unsigned tag) {
    for (unsigned spins = 0; spins < SPIN_CAP; ++spins) {
        unsigned f;
        asm volatile("global_load_dword %0, %1, %2 sc1\n\ts_waitcnt vmcnt(0)" : "=&v"(f) : "v"(voff), "s"(fl) : "memory");
        if (__all(!active || f == tag)) break;
        __builtin_amdgcn_s_sleep(3);
    }
}

struct Ptrs { const char *x, *wqkv, *wo, *wg, *wu, *wd; char* ws; char* out; };

constexpr int P1NB = 4;
constexpr int P1_BUF = 32768, P1_MISC = P1NB * P1_BUF;

DI void gst_granule(char* sbase, unsigned voff, float v, unsigned tag) { u32x2 g = {__float_as_uint(v), tag}; gst64(sbase, voff, g); }
DI float sum_x16(float v) {
    auto r = __builtin_amdgcn_permlane16_swap(__float_as_uint(v), __float_as_uint(v), false, false);
    return __uint_as_float(r[0]) + __uint_as_float(r[1]);
}
template <bool ISV>
DI void p1_epilogue(char* ws, int tb, int cb, char* smem, int tl, int cp, const f32x16& accA, const f32x16& accB, int lane) {
    const int r = lane & 31, hh = lane >> 5;
    const LAS f32x2* st = (const LAS f32x2*)(smem + P1_MISC);
    const LAS float* csl = (const LAS float*)(smem + P1_MISC + 1024);
    const int sec = cb >> 2, tile = tb * 4 + tl;
#pragma unroll
    for (int b = 0; b < 2; ++b) {
        const f32x16& acc = b ? accB : accA;
        const int ct = 2 * cp + b, head0 = (cb & 3) * 8 + ct * 2;
        float v[16];
        if constexpr (!ISV) {
            const f32x2 ms = st[tl * 32 + r];
            const float sc = ms[1] * (sec == 0 ? QSCALE : 1.f), msc = -ms[0] * sc;
#pragma unroll
            for (int g = 0; g < 4; ++g) {
                const f32x4 c4 = *(LAS const f32x4*)(csl + ct * 32 + 8 * g + 4 * hh);
#pragma unroll
                for (int e = 0; e < 4; ++e) v[4 * g + e] = acc[4 * g + e] * sc + msc * c4[e];
            }
            char* dst = ws + (sec == 0 ? WS_QF : WS_KF) + ((size_t)head0 * 64 + tile) * 1024;
#pragma unroll
            for (int hs = 0; hs < 2; ++hs) {
                const u32x4 o = pack_swap(v[8 * hs], v[8 * hs + 1], v[8 * hs + 2], v[8 * hs + 3], v[8 * hs + 4], v[8 * hs + 5], v[8 * hs + 6], v[8 * hs + 7]);
                gst128(dst + hs * 65536, lane * 16, o);
                if (sec == 1) {
                    float n2 = 0.f;
#pragma unroll
                    for (int e = 0; e < 8; ++e) n2 = fmaf(v[8 * hs + e], v[8 * hs + e], n2);
                    n2 = dpp_max<false>(sum_halves(n2));
                    if (lane == 31) gst32(ws + WS_KM + (head0 + hs) * 256 + tile * 4, 0, __float_as_uint(n2));
                }
            }
        } else {
            const float csn = csl[ct * 32 + r];
#pragma unroll
            for (int g = 0; g < 4; ++g) {
                const LAS f32x4* sp = (const LAS f32x4*)(st + tl * 32 + 8 * g + 4 * hh);
                const f32x4 pa = sp[0], pb = sp[1];
                v[4 * g] = (acc[4 * g] - pa[0] * csn) * pa[1]; v[4 * g + 1] = (acc[4 * g + 1] - pa[2] * csn) * pa[3];
                v[4 * g + 2] = (acc[4 * g + 2] - pb[0] * csn) * pb[1]; v[4 * g + 3] = (acc[4 * g + 3] - pb[2] * csn) * pb[3];
            }
            char* dst = ws + WS_VF + ((size_t)head0 * 64 + tile) * 1024;
            const unsigned vo = (r >> 4) * 65536 + hh * 256 + (r & 15) * 16;
#pragma unroll
            for (int s = 0; s < 2; ++s) {
                u32x4 o = {cvtpk(v[8 * s], v[8 * s + 1]), cvtpk(v[8 * s + 2], v[8 * s + 3]), cvtpk(v[8 * s + 4], v[8 * s + 5]), cvtpk(v[8 * s + 6], v[8 * s + 7])};
                gst128(dst + s * 512, vo, o);
            }
        }
    }
}
constexpr int P1_XCH = (5 % P1NB) * P1_BUF;

DI void p1_colsum(const char* wqkv, char* ws, int tb, int cb, char* smem, int j, int lane, f32x16& accA, f32x16& accB) {
    const unsigned tag = __hip_atomic_load((const __attribute__((address_space(1))) unsigned*)(ws + WS_EPOCH), __ATOMIC_RELAXED, __HIP_MEMORY_SCOPE_AGENT) + 1;
    const char* src = wqkv + (size_t)((tb & 3) * 128 + j * 32 + (lane >> 4)) * 3072 + cb * 256 + (lane & 15) * 16;
    u32x4 v[8];
#pragma unroll
    for (int i = 0; i < 8; ++i) v[i] = *(const u32x4*)(src + (size_t)i * 4 * 3072);
    cbar(); cbar();
    float s[8] = {};
#pragma unroll
    for (int i = 0; i < 8; ++i)
#pragma unroll
        for (int e = 0; e < 4; ++e) { s[2 * e] += bflo(v[i][e]); s[2 * e + 1] += bfhi(v[i][e]); }
#pragma unroll
    for (int e = 0; e < 8; ++e) s[e] = sum_halves(sum_x16(s[e]));
    LAS float* part = (LAS float*)(smem + P1_MISC + 2048);
    if (lane < 16) {
        f32x4 a = {s[0], s[1], s[2], s[3]}, c = {s[4], s[5], s[6], s[7]};
        *(LAS f32x4*)(part + j * 128 + lane * 8) = a; *(LAS f32x4*)(part + j * 128 + lane * 8 + 4) = c;
    }
    cbar();
    if (j == 0) {
#pragma unroll
        for (int k = 0; k < 2; ++k) {
            const int c = lane + 64 * k;
            gst_granule(ws + WS_G2, ((cb * 128 + c) * 4 + (tb & 3)) * 8, part[c] + part[128 + c] + part[256 + c] + part[384 + c], tag);
        }
    }
    cbar(); cbar(); cbar(); cbar(); cbar();
    cbar();
    f32x16 acc[2];
    {
        const LAS f32x4* xr = (const LAS f32x4*)(smem + P1_XCH + j * 8192 + lane * 16);
#pragma unroll
        for (int b = 0; b < 2; ++b)
#pragma unroll
            for (int g = 0; g < 4; ++g) { const f32x4 t = xr[(b * 4 + g) * 64]; acc[b][4 * g] = t[0]; acc[b][4 * g + 1] = t[1]; acc[b][4 * g + 2] = t[2]; acc[b][4 * g + 3] = t[3]; }
    }
    accA = acc[0]; accB = acc[1];
}

template <bool ISV>
DI void p1_compute(char* ws, int tb, int cb, char* smem, int w, int lane, f32x16& accA, f32x16& accB) {
    const int r = lane & 31, hh = lane >> 5;
    const unsigned lds0 = (unsigned)(uintptr_t)smem;
    const int tp = w & 1, cp = w >> 1;
    unsigned x0[4];
    {
        const int f = (r >> 1) & 7;
#pragma unroll
        for (int ks = 0; ks < 4; ++ks) x0[ks] = lds0 + (64 * tp + r) * 128 + (((2 * ks + hh) ^ f) << 4);
    }
    const unsigned wro = lds0 + 16384 + hh * 2048 + ((lane & 15) >> 2) * 64 + ((lane >> 4) & 1) * 32 + (lane & 3) * 8 + cp * 512;
    f32x16 acc[2][2] = {};
    bf16x8 X[4][2], W[4][2];
#define P1_LOAD(S, B, ks) { X[S][0] = lds_b128(x0[ks] + (B)); X[S][1] = lds_b128(x0[ks] + (B) + 4096); W[S][0] = lds_trfrag(wro + (B) + (ks) * 4096, 1024); W[S][1] = lds_trfrag(wro + (B) + (ks) * 4096 + 256, 1024); }
#define P1_MMA(S)                                                                                                                 \
    {                                                                                                                             \
        _Pragma("unroll") for (int a = 0; a < 2; ++a) _Pragma("unroll") for (int b = 0; b < 2; ++b)                                \
            acc[a][b] = ISV ? MFMA(X[S][a], W[S][b], acc[a][b]) : MFMA(W[S][b], X[S][a], acc[a][b]);                               \
    }
#define P1_ILV() { __builtin_amdgcn_sched_group_barrier(0x8, 1, 0); __builtin_amdgcn_sched_group_barrier(0x100, 2, 0); __builtin_amdgcn_sched_group_barrier(0x8, 1, 0); __builtin_amdgcn_sched_group_barrier(0x100, 2, 0); \
                   __builtin_amdgcn_sched_group_barrier(0x8, 1, 0); __builtin_amdgcn_sched_group_barrier(0x100, 2, 0); __builtin_amdgcn_sched_group_barrier(0x8, 1, 0); SB(); }
    cbar();
    P1_LOAD(0, 0, 0); P1_LOAD(1, 0, 1);
    SB();
    unsigned B = 0;
#pragma unroll 1
    for (int ch = 0; ch < 7; ++ch) {
        P1_LOAD(2, B, 2); P1_MMA(0); P1_ILV();
        P1_LOAD(3, B, 3); P1_MMA(1); P1_ILV();
        cbar();
        B = B == (P1NB - 1) * P1_BUF ? 0 : B + P1_BUF;
        P1_LOAD(0, B, 0); P1_MMA(2); P1_ILV();
        P1_LOAD(1, B, 1); P1_MMA(3); P1_ILV();
    }
    P1_LOAD(2, B, 2); P1_MMA(0); P1_ILV();
    P1_LOAD(3, B, 3); P1_MMA(1); P1_ILV();
    P1_MMA(2); P1_MMA(3);
    {
        LAS f32x4* xw = (LAS f32x4*)(smem + P1_XCH + w * 8192 + lane * 16);
#pragma unroll
        for (int b = 0; b < 2; ++b)
#pragma unroll
            for (int g = 0; g < 4; ++g) { f32x4 t = {acc[1][b][4 * g], acc[1][b][4 * g + 1], acc[1][b][4 * g + 2], acc[1][b][4 * g + 3]}; xw[(b * 4 + g) * 64] = t; }
    }
    cbar();
    accA = acc[0][0]; accB = acc[0][1];
}

typedef const __attribute__((address_space(1))) char* gcptr;
template <int NROW16>
DI void colsum32(gcptr W, unsigned rs, int lane, float (&s)[8]) {
    const unsigned vo = (lane & 15) * rs + (lane >> 4) * 16;
    u32x4 v[NROW16];
#pragma unroll
    for (int i = 0; i < NROW16; ++i) v[i] = *(const __attribute__((address_space(1))) u32x4*)(W + (size_t)i * 16 * rs + vo);
    __builtin_amdgcn_sched_barrier(0);
#pragma unroll
    for (int e = 0; e < 8; ++e) s[e] = 0.f;
#pragma unroll
    for (int i = 0; i < NROW16; ++i)
#pragma unroll
        for (int e = 0; e < 4; ++e) { s[2 * e] += bflo(v[i][e]); s[2 * e + 1] += bfhi(v[i][e]); }
#pragma unroll
    for (int e = 0; e < 8; ++e) s[e] = dpp_add<0x118>(dpp_add<0x114>(dpp_add<0x112>(dpp_add<0x111>(s[e]))));
}
DI void p1_helper(const char* px, const char* pqkv, const char* pg, const char* pu, const char* pwo, const char* pwd, unsigned dummy, char* ws, int h, int w, int lane) {
    if (w < 4) {
        const unsigned tag = __hip_atomic_load((const __attribute__((address_space(1))) unsigned*)(ws + WS_EPOCH), __ATOMIC_RELAXED, __HIP_MEMORY_SCOPE_AGENT) + 1;
        const int u = w * 64 + h;
        if (u < 256) {
            const int rl = lane >> 3, seg = lane & 7;
            const char* src = px + (size_t)(u * 8 + rl) * 1024 + seg * 16;
            u32x4 v[8];
#pragma unroll
            for (int i = 0; i < 8; ++i) v[i] = *(const u32x4*)(src + i * 128);
            float s1 = 0.f, s2 = 0.f;
#pragma unroll
            for (int i = 0; i < 8; ++i)
#pragma unroll
                for (int e = 0; e < 4; ++e) { s1 = dot2(v[i][e], ONES, s1); s2 = dot2(v[i][e], v[i][e], s2); }
            s1 = dpp_add<0x114>(dpp_add<0x112>(dpp_add<0x111>(s1))); s2 = dpp_add<0x114>(dpp_add<0x112>(dpp_add<0x111>(s2)));
            const float mean = s1 * (1.f / DM);
            const float rstd = __builtin_amdgcn_rsqf(fmaxf(s2 * (1.f / DM) - mean * mean, 0.f) + LN_EPS);
            if (seg == 7) { gst_granule(ws + WS_G1, (u * 8 + rl) * 16, mean, tag); gst_granule(ws + WS_G1, (u * 8 + rl) * 16 + 8, rstd, tag); }
        }
    }
    if (w >= 6) {
        asm volatile("s_mov_b32 m0, %0\n\ts_nop 0" ::"s"(dummy) : "memory");
        const int part = h * 2 + (w - 6);
        gtouch(pwo, (part * 32 + (lane & 31)) * 128);
#pragma unroll
        for (int i = 0; i < 2; ++i) { int ln = part * 86 + i * 64 + lane; ln = (i * 64 + lane) < 86 ? ln : part * 86; gtouch(pwd, ln * 128); }
#pragma unroll
        for (int i = 0; i < 2; ++i) { int ln = part * 86 + i * 64 + lane; ln = (i * 64 + lane) < 86 ? ln : part * 86; gtouch(pg, ln * 128); gtouch(pu, ln * 128); }
        asm volatile("s_waitcnt vmcnt(0)" ::: "memory");
    }
    const int job = w == 4 ? h : (w == 5 && h < 22) ? 64 + h : -1;
    if (job >= 0) {
        const int blk = job < 43 ? job : job - 43;
        const uintptr_t wa = (uintptr_t)pg + (job < 43 ? 0 : (uintptr_t)pu - (uintptr_t)pg);
        float s[8];
        colsum32<32>((gcptr)wa + blk * 64, FF * 2, lane, s);
        if ((lane & 15) == 15) {
            char* dst = ws + WS_CS + (job < 43 ? 0 : FF) * 4;
            const int c0 = blk * 32 + (lane >> 4) * 8;
            u32x4 a = {__float_as_uint(s[0]), __float_as_uint(s[1]), __float_as_uint(s[2]), __float_as_uint(s[3])};
            u32x4 c = {__float_as_uint(s[4]), __float_as_uint(s[5]), __float_as_uint(s[6]), __float_as_uint(s[7])};
            gst128(dst, c0 * 4, a); gst128(dst, c0 * 4 + 16, c);
        }
    }
}

DI void small_touch(char* ws, int b, int lane, unsigned lds0) {
    unsigned o_ = (unsigned)WS_FLAG + (lane & 3) * 256 * 128 + b * 128;
    if (lane == 4) o_ = (unsigned)WS_G1 + b * 128;
    if (lane >= 5 && lane < 9) o_ = (unsigned)WS_ST + (b * 4 + lane - 5) * 128;
    if (lane == 9) o_ = (unsigned)WS_KM + (b & 63) * 128;
    if (lane == 10) o_ = (unsigned)WS_CS + (b < 86 ? b : 0) * 128;
    if (lane >= 11 && lane < 17) o_ = (unsigned)WS_G2 + (b % 64) * 768 + (lane - 11) * 128;
    asm volatile("s_mov_b32 m0, %0\n\ts_nop 0\n\tglobal_load_lds_dword %1, %2 sc1" ::"s"(lds0 + LDS_BYTES - 256), "v"(o_), "s"(ws) : "memory");
}

DI unsigned p1_load(const Ptrs& P, int L, int tb, int cb, unsigned lds0, int lane) {
    unsigned vo[4];
    const char* base;
    unsigned cstep, gstep;
    if (L == 0) {
        const int rl = lane >> 3, cs = lane & 7;
#pragma unroll
        for (int i = 0; i < 4; ++i) vo[i] = (8 * i + rl) * 1024 + ((cs ^ ((4 * (i & 1) + (rl >> 1)) & 7)) << 4) - i * 1024;
        base = P.x + (size_t)tb * 128 * 1024; cstep = 128; gstep = 4 * 8192;
    } else {
        const int nb = lane >> 4, q = (lane >> 2) & 3, c = lane & 3;
#pragma unroll
        for (int i = 0; i < 4; ++i) vo[i] = (4 * i + q) * 3072 + nb * 64 + c * 16 - i * 1024;
        base = P.wqkv + cb * 256; cstep = 64 * 3072; gstep = 16 * 3072;
    }
    auto issue = [&](int c, unsigned B) {
        const char* src = base + (size_t)c * cstep;
        const unsigned dst = lds0 + B + L * 16384;
#pragma unroll
        for (int g = 0; g < 4; ++g) dma4(src + (size_t)g * gstep, vo[0], vo[1], vo[2], vo[3], dst + g * 4096);
    };
    issue(0, 0);
#pragma unroll
    for (int c = 1; c < P1NB - 1; ++c) issue(c, c * P1_BUF);
    if (L == 0) small_touch(P.ws, blockIdx.x, lane, lds0);
    unsigned B = (P1NB - 1) * P1_BUF;
    constexpr int LA = P1NB - 1;
#pragma unroll 1
    for (int c = 0; c < 8; ++c) {
        const int y = 7 - c < LA - 1 ? 7 - c : LA - 1;
        if (y == 3) lbar<48>(); else if (y == 2) lbar<32>(); else if (y == 1) lbar<16>(); else lbar<0>();
        if (c + LA < 8) { issue(c + LA, B); B = B == (P1NB - 1) * P1_BUF ? 0 : B + P1_BUF; }
    }
    const unsigned tag = rfl(gld32_wait(P.ws + WS_EPOCH, 0)) + 1;
    if (L == 1) {
        u32x4 a, b, c0, c1, c2, c3;
        for (unsigned spins = 0; spins < SPIN_CAP; ++spins) {
            asm volatile("global_load_dwordx4 %0, %6, %7 sc1\n\tglobal_load_dwordx4 %1, %6, %7 offset:16 sc1\n\t"
                         "global_load_dwordx4 %2, %8, %9 sc1\n\tglobal_load_dwordx4 %3, %8, %9 offset:16 sc1\n\t"
                         "global_load_dwordx4 %4, %8, %9 offset:32 sc1\n\tglobal_load_dwordx4 %5, %8, %9 offset:48 sc1\n\ts_waitcnt vmcnt(0)"
                         : "=&v"(a), "=&v"(b), "=&v"(c0), "=&v"(c1), "=&v"(c2), "=&v"(c3)
                         : "v"(lane * 32), "s"(P.ws + WS_G1 + tb * 128 * 16), "v"(lane * 64), "s"(P.ws + WS_G2 + cb * 128 * 32) : "memory");
            if (__all(a[1] == tag && a[3] == tag && b[1] == tag && b[3] == tag && c0[1] == tag && c0[3] == tag && c1[1] == tag && c1[3] == tag &&
                      c2[1] == tag && c2[3] == tag && c3[1] == tag && c3[3] == tag)) break;
            __builtin_amdgcn_s_sleep(1);
        }
        u32x4 ms = {a[0], a[2], b[0], b[2]};
        u32x2 cs = {__float_as_uint(__uint_as_float(c0[0]) + __uint_as_float(c0[2]) + __uint_as_float(c1[0]) + __uint_as_float(c1[2])),
                    __float_as_uint(__uint_as_float(c2[0]) + __uint_as_float(c2[2]) + __uint_as_float(c3[0]) + __uint_as_float(c3[2]))};
        *(LAS u32x4*)(uintptr_t)(lds0 + P1_MISC + lane * 16) = ms;
        *(LAS u32x2*)(uintptr_t)(lds0 + P1_MISC + 1024 + lane * 8) = cs;
        asm volatile("s_waitcnt lgkmcnt(0)" ::: "memory");
    }
    bar();
    return tag;
}

constexpr int P2_X = 131072 + 2048;
constexpr int P2_KM = P2_X + 8 * 2560;
constexpr float SAFE_RANGE = 80.f;

DI void p2_compute(char* ws, char* smem, int head, int qb, int w, int lane) {
    const int r = lane & 31, hh = lane >> 5;
    const unsigned lds0 = (unsigned)(uintptr_t)smem;
    const int qg = w & 3, kh = w >> 2;
    const int la = qg * 2 + kh, lb = qg * 2 + (1 - kh);
    cbar();
    const u32x4 qa = *(LAS const u32x4*)(uintptr_t)(lds0 + P2_X + la * 1024 + lane * 16), qbv = *(LAS const u32x4*)(uintptr_t)(lds0 + P2_X + lb * 1024 + lane * 16);
    const float kmax2 = __int_as_float(__builtin_amdgcn_readlane(__float_as_int(dpp_max<true>(*(LAS const float*)(uintptr_t)(lds0 + P2_KM + lane * 4))), 63));
    float qa2 = 0.f, qb2 = 0.f;
#pragma unroll
    for (int e = 0; e < 4; ++e) { qa2 = dot2(qa[e], qa[e], qa2); qb2 = dot2(qbv[e], qbv[e], qb2); }
    const float bnda = __builtin_amdgcn_sqrtf(sum_halves(qa2) * kmax2) * 1.01f, bndb = __builtin_amdgcn_sqrtf(sum_halves(qb2) * kmax2) * 1.01f;
    const bool fast = __all(2.f * bnda <= SAFE_RANGE && 2.f * bndb <= SAFE_RANGE);
    const unsigned ka = lds0 + kh * 32768 + lane * 16, va = lds0 + 65536 + kh * 32768 + hh * 256 + (r & 15) * 16;
    const unsigned xw = lds0 + P2_X + w * 2560 + lane * 16, xr = lds0 + P2_X + (w ^ 4) * 2560 + lane * 16;
    float o[9], ma;
    if (fast) {
        ma = bnda;
        unsigned kp = ka, vp = va, cnt;
#define EXP(r) "v_exp_f32 v" #r ", v" #r "\n\t"
#define CVT(d, a, b) "v_cvt_pk_bf16_f32 v" #d ", v" #a ", v" #b "\n\t"
#define MSQ(SN, KN, Q, C) "v_mfma_f32_32x32x16_bf16 " SN ", " KN ", " Q ", " C "\n\t"
#define MPO(O, V, P) "v_mfma_f32_32x32x16_bf16 " O ", " V ", " P ", " O "\n\t"
#define RDV(VN0, VN1, VOFF) "s_mov_b64 exec, %[msk]\n\t" "ds_read_b128 " VN0 ", %[vp] offset:" #VOFF "\n\t" "ds_read_b128 " VN1 ", %[vp] offset:" #VOFF "+512\n\t" "s_mov_b64 exec, -1\n\t"
#define RDK(KNN, KOFF) "ds_read_b128 " KNN ", %[kp] offset:" #KOFF "\n\t"
#define STEP(PRE, c0, c1, c2, c3, c4, c5, c6, c7, c8, c9, c10, c11, c12, c13, c14, c15, p14, p15, SN, KN, Q, CC, O, OP, VC0, VP1)  \
        EXP(c0) EXP(c1) CVT(127, p14, p15) EXP(c2) EXP(c3) MPO(OP, VP1, "v[124:127]") PRE                                    \
        CVT(120, c0, c1) EXP(c4) EXP(c5) CVT(121, c2, c3) EXP(c6)                                                            \
        "s_waitcnt lgkmcnt(3)\n\t" MSQ(SN, KN, Q, CC)                                                                        \
        EXP(c7) CVT(122, c4, c5) EXP(c8) EXP(c9) CVT(123, c6, c7) EXP(c10) EXP(c11)                                          \
        MPO(O, VC0, "v[120:123]")                                                                                            \
        CVT(124, c8, c9) EXP(c12) EXP(c13) CVT(125, c10, c11) EXP(c14) EXP(c15) CVT(126, c12, c13)
#define SA_ 32, 33, 34, 35, 36, 37, 38, 39, 40, 41, 42, 43, 44, 45, 46, 47
#define SB_ 48, 49, 50, 51, 52, 53, 54, 55, 56, 57, 58, 59, 60, 61, 62, 63
#define CALL(M, ...) M(__VA_ARGS__)
#define TE(KOFF, VOFF) CALL(STEP, RDV("v[112:115]", "v[116:119]", VOFF), SA_, 62, 63, "v[48:63]", "v[96:99]", "%[qb]", "v[144:159]", "v[64:79]", "v[80:95]", "v[104:107]", "v[116:119]") \
                       CALL(STEP, RDK("v[96:99]", KOFF), SB_, 46, 47, "v[32:47]", "v[100:103]", "%[qa]", "v[128:143]", "v[80:95]", "v[64:79]", "v[104:107]", "v[108:111]")
#define TO(KOFF, VOFF) CALL(STEP, RDV("v[104:107]", "v[108:111]", VOFF), SA_, 62, 63, "v[48:63]", "v[100:103]", "%[qb]", "v[144:159]", "v[64:79]", "v[80:95]", "v[112:115]", "v[108:111]") \
                       CALL(STEP, RDK("v[100:103]", KOFF), SB_, 46, 47, "v[32:47]", "v[96:99]", "%[qa]", "v[128:143]", "v[80:95]", "v[64:79]", "v[112:115]", "v[116:119]")
#define M64(a, b, c, d) "v_mov_b64 v[" #a ":" #b "], v[" #c ":" #d "]\n\t"
#define MOV4(a, b, c, d, src) "v_mov_b32 v" #a ", " src "\n\tv_mov_b32 v" #b ", " src "\n\tv_mov_b32 v" #c ", " src "\n\tv_mov_b32 v" #d ", " src "\n\t"
        asm volatile(
            "s_setprio 3\n\t"
            "ds_read_b128 v[96:99], %[kp]\n\t"
            "v_mov_b32 v128, %[na]\n\tv_mov_b32 v129, %[na]\n\tv_mov_b32 v144, %[nb]\n\tv_mov_b32 v145, %[nb]\n\t"
            "v_mov_b32 v104, %[one]\n\tv_mov_b32 v105, %[one]\n\tv_mov_b64 v[64:65], 0\n\t"
            M64(106, 107, 104, 105) M64(108, 109, 104, 105) M64(110, 111, 104, 105) M64(112, 113, 104, 105) M64(114, 115, 104, 105) M64(116, 117, 104, 105) M64(118, 119, 104, 105)
            "s_mov_b64 exec, %[msk]\n\t"
            "ds_read_b128 v[104:107], %[vp]\n\t"
            "ds_read_b128 v[108:111], %[vp] offset:512\n\t"
            "s_mov_b64 exec, -1\n\t"
            "ds_read_b128 v[100:103], %[kp] offset:1024\n\t"
            M64(130, 131, 128, 129) M64(132, 133, 128, 129) M64(134, 135, 128, 129) M64(136, 137, 128, 129) M64(138, 139, 128, 129) M64(140, 141, 128, 129) M64(142, 143, 128, 129)
            "s_waitcnt lgkmcnt(3)\n\t"
            "s_nop 1\n\t"
            "v_mfma_f32_32x32x16_bf16 v[32:47], v[96:99], %[qa], v[128:143]\n\t"
            M64(146, 147, 144, 145) M64(148, 149, 144, 145) M64(150, 151, 144, 145) M64(152, 153, 144, 145) M64(154, 155, 144, 145) M64(156, 157, 144, 145) M64(158, 159, 144, 145)
            M64(66, 67, 64, 65) M64(68, 69, 64, 65) M64(70, 71, 64, 65) M64(72, 73, 64, 65) M64(74, 75, 64, 65) M64(76, 77, 64, 65) M64(78, 79, 64, 65)
            M64(80, 81, 64, 65) M64(82, 83, 64, 65) M64(84, 85, 64, 65) M64(86, 87, 64, 65) M64(88, 89, 64, 65) M64(90, 91, 64, 65) M64(92, 93, 64, 65) M64(94, 95, 64, 65)
            M64(62, 63, 64, 65) M64(124, 125, 64, 65) M64(126, 127, 64, 65)
            "s_mov_b32 %[cnt], 0\n\t"
            "L_att_%=:\n\t"
            TE(2048, 1024) TO(3072, 2048) TE(4096, 3072) TO(5120, 4096)
            "v_add_u32 %[kp], 0x1000, %[kp]\n\t"
            "v_add_u32 %[vp], 0x1000, %[vp]\n\t"
            "s_cmp_lg_u32 %[cnt], 0\n\t"
            "s_cbranch_scc1 L_att_nb_%=\n\t"
            "s_barrier\n\t"
            "L_att_nb_%=:\n\t"
            "s_add_u32 %[cnt], %[cnt], 1\n\t"
            "s_cmp_eq_u32 %[cnt], 3\n\ts_cbranch_scc0 L_p1_%=\n\ts_setprio 2\n\tL_p1_%=:\n\t"
            "s_cmp_eq_u32 %[cnt], 6\n\ts_cbranch_scc0 L_p2_%=\n\ts_setprio 1\n\tL_p2_%=:\n\t"
            "s_cmp_eq_u32 %[cnt], 7\n\ts_cbranch_scc0 L_p3_%=\n\ts_setprio 0\n\tL_p3_%=:\n\t"
            "s_cmp_lt_u32 %[cnt], 8\n\t"
            "s_cbranch_scc1 L_att_%=\n\t"
            CVT(127, 62, 63)
            "s_nop 1\n\t"
            MPO("v[80:95]", "v[116:119]", "v[124:127]")
            "s_waitcnt lgkmcnt(0)\n\t"
            "s_nop 15\n\t"
            "v_mov_b32 v89, %[bb]\n\t"
            "v_mov_b32 %[o0], v64\n\tv_mov_b32 %[o1], v65\n\tv_mov_b32 %[o2], v66\n\tv_mov_b32 %[o3], v67\n\t"
            "v_mov_b32 %[o4], v68\n\tv_mov_b32 %[o5], v69\n\tv_mov_b32 %[o6], v70\n\tv_mov_b32 %[o7], v71\n\tv_mov_b32 %[o8], v72\n\t"
            "ds_write_b128 %[xw], v[80:83]\n\t"
            "ds_write_b128 %[xw], v[84:87] offset:1024\n\t"
            "ds_write_b64 %[xw2], v[88:89]\n\t"
            : [o0] "=&v"(o[0]), [o1] "=&v"(o[1]), [o2] "=&v"(o[2]), [o3] "=&v"(o[3]), [o4] "=&v"(o[4]), [o5] "=&v"(o[5]), [o6] "=&v"(o[6]), [o7] "=&v"(o[7]), [o8] "=&v"(o[8]),
              [kp] "+v"(kp), [vp] "+v"(vp), [cnt] "=&s"(cnt)
            : [qa] "v"(qa), [qb] "v"(qbv), [na] "v"(-bnda), [nb] "v"(-bndb), [bb] "v"(bndb), [msk] "s"(0x0000ffff0000ffffull), [xw] "v"(xw), [xw2] "v"(xw + 2048 - lane * 8), [one] "v"((r == 16 || r == 20) ? ONES : 0u)
            : "memory", "scc",
              "v32", "v33", "v34", "v35", "v36", "v37", "v38", "v39", "v40", "v41", "v42", "v43", "v44", "v45", "v46", "v47",
              "v48", "v49", "v50", "v51", "v52", "v53", "v54", "v55", "v56", "v57", "v58", "v59", "v60", "v61", "v62", "v63",
              "v64", "v65", "v66", "v67", "v68", "v69", "v70", "v71", "v72", "v73", "v74", "v75", "v76", "v77", "v78", "v79",
              "v80", "v81", "v82", "v83", "v84", "v85", "v86", "v87", "v88", "v89", "v90", "v91", "v92", "v93", "v94", "v95",
              "v96", "v97", "v98", "v99", "v100", "v101", "v102", "v103", "v104", "v105", "v106", "v107", "v108", "v109", "v110", "v111",
              "v112", "v113", "v114", "v115", "v116", "v117", "v118", "v119", "v120", "v121", "v122", "v123", "v124", "v125", "v126", "v127",
              "v128", "v129", "v130", "v131", "v132", "v133", "v134", "v135", "v136", "v137", "v138", "v139", "v140", "v141", "v142", "v143",
              "v144", "v145", "v146", "v147", "v148", "v149", "v150", "v151", "v152", "v153", "v154", "v155", "v156", "v157", "v158", "v159");
    } else {
        bar();
        const u32x4 ones = {ONES, ONES, ONES, ONES};
        const f32x16 zero = {};
#pragma unroll 1
        for (int sl = 1; sl >= 0; --sl) {
            const bf16x8 qf = __builtin_bit_cast(bf16x8, sl ? qbv : qa);
            bf16x8 v0 = __builtin_bit_cast(bf16x8, ones), v1 = v0, kf;
            f32x16 oo = {};
            float m = -INFINITY;
#pragma unroll 1
            for (int t = 0; t < 32; ++t) {
                asm volatile("ds_read_b128 %0, %3\n\ts_mov_b64 exec, %5\n\tds_read_b128 %1, %4\n\tds_read_b128 %2, %4 offset:512\n\ts_mov_b64 exec, -1\n\ts_waitcnt lgkmcnt(0)"
                             : "=&v"(kf), "+v"(v0), "+v"(v1) : "v"(ka + t * 1024), "v"(va + t * 1024), "s"(0x0000ffff0000ffffull) : "memory");
                const f32x16 s = MFMA(kf, qf, zero);
                float tm = s[0];
#pragma unroll
                for (int i = 1; i < 16; ++i) tm = fmaxf(tm, s[i]);
                tm = fmaxf(tm, other_half(tm));
                const float mn = fmaxf(m, tm);
                const float alpha = __builtin_amdgcn_exp2f(m - mn);
                m = mn;
#pragma unroll
                for (int i = 0; i < 16; ++i) oo[i] *= alpha;
                u32x4 p0, p1;
#pragma unroll
                for (int i = 0; i < 4; ++i) p0[i] = cvtpk(__builtin_amdgcn_exp2f(s[2 * i] - m), __builtin_amdgcn_exp2f(s[2 * i + 1] - m));
#pragma unroll
                for (int i = 0; i < 4; ++i) p1[i] = cvtpk(__builtin_amdgcn_exp2f(s[8 + 2 * i] - m), __builtin_amdgcn_exp2f(s[9 + 2 * i] - m));
                oo = MFMA(v0, __builtin_bit_cast(bf16x8, p0), oo);
                oo = MFMA(v1, __builtin_bit_cast(bf16x8, p1), oo);
            }
            if (sl == 1) {
                f32x4 t0 = {oo[0], oo[1], oo[2], oo[3]}, t1 = {oo[4], oo[5], oo[6], oo[7]};
                f32x2 t2 = {oo[8], m};
                *(LAS f32x4*)(uintptr_t)xw = t0; *(LAS f32x4*)(uintptr_t)(xw + 1024) = t1; *(LAS f32x2*)(uintptr_t)(xw + 2048 - lane * 8) = t2;
            } else {
#pragma unroll
                for (int i = 0; i < 9; ++i) o[i] = oo[i];
                ma = m;
            }
        }
    }
    cbar();
    {
        const f32x4 t0 = *(LAS const f32x4*)(uintptr_t)xr, t1 = *(LAS const f32x4*)(uintptr_t)(xr + 1024);
        const f32x2 t2 = *(LAS const f32x2*)(uintptr_t)(xr + 2048 - lane * 8);
        const float mo = t2[1], mn = fmaxf(ma, mo);
        const float fa = __builtin_amdgcn_exp2f(ma - mn), fo = __builtin_amdgcn_exp2f(mo - mn);
        const float inv = __builtin_amdgcn_rcpf(o[8] * fa + t2[0] * fo);
        const float ia = fa * inv, io = fo * inv;
        const u32x4 ov = pack_swap(o[0] * ia + t0[0] * io, o[1] * ia + t0[1] * io, o[2] * ia + t0[2] * io, o[3] * ia + t0[3] * io,
                                   o[4] * ia + t1[0] * io, o[5] * ia + t1[1] * io, o[6] * ia + t1[2] * io, o[7] * ia + t1[3] * io);
        gst128(ws + WS_OF + ((size_t)(qb * 8 + la) * 32 + head) * 1024, lane * 16, ov);
    }
}

constexpr int P3_X = 153600;
template <int MODE>
DI void p35_compute(const Ptrs& P, char* smem, int tb, int cb, int w, int lane, u32x4& hkeep) {
    constexpr int A0 = MODE == 0 ? 65536 : 98304, XA = MODE == 0 ? 131072 : 147456;
    char* ws = P.ws;
    const int r = lane & 31, hh = lane >> 5;
    const unsigned lds0 = (unsigned)(uintptr_t)smem;
    const int ta = w & 1, cj = (w >> 1) & 1, kh = w >> 2;
    const int tile = tb * 2 + ta, token = tile * 32 + r, n0 = cb * 64 + cj * 32 + kh * 16;
    u32x2 res[2];
    const unsigned aro = lds0 + A0 + ta * 8192 + lane * 16 + kh * 4096;
    const unsigned wro = lds0 + hh * 1024 + ((lane & 15) >> 2) * 64 + ((lane >> 4) & 1) * 32 + (lane & 3) * 8 + cj * 256 + kh * 8192;
    f32x16 acc = {};
    bf16x8 A[4], Wf[4];
#define P35_LD(S, BA, BW, ks) { A[S] = lds_b128(aro + (BA) + (ks) * 1024); Wf[S] = lds_trfrag(wro + (BW) + (ks) * 2048, 512); }
    if constexpr (MODE == 0) {
        cbar();
        P35_LD(0, 0, 0, 0); P35_LD(1, 0, 0, 1);
        unsigned BA = 0;
#pragma unroll 1
        for (int ch = 0; ch < 4; ++ch) {
            P35_LD(2, BA, BA, 2); SB(); acc = MFMA(Wf[0], A[0], acc); SB();
            P35_LD(3, BA, BA, 3); SB(); acc = MFMA(Wf[1], A[1], acc); SB();
            if (ch < 3) {
                cbar();
                BA += 16384;
                P35_LD(0, BA, BA, 0);
            }
            SB(); acc = MFMA(Wf[2], A[2], acc); SB();
            if (ch < 3) { P35_LD(1, BA, BA, 1); }
            SB(); acc = MFMA(Wf[3], A[3], acc); SB();
        }
    } else {
        bf16x8 WR[19];
        bar();
#pragma unroll
        for (int q = 0; q < 16; ++q) WR[q] = lds_trfrag(wro + (q >> 2) * 16384 + (q & 3) * 2048, 512);
#pragma unroll
        for (int j = 0; j < 3; ++j) WR[16 + j] = lds_trfrag(wro + 4 * 16384 - kh * 8192 + (kh * 3 + j) * 2048, 512);
        cbar();
        cbar();
        P35_LD(0, 0, 0, 0); P35_LD(1, 0, 0, 1);
        unsigned BA = 0, BW = 0;
#define P5_ADV() { BA = BA == 2 * 16384 ? 0 : BA + 16384; }
#define P5_LDA(S, ks) { A[S] = lds_b128(aro + BA + (ks) * 1024); }
#pragma unroll 1
        for (int ch = 0; ch < 5; ++ch) {
            P35_LD(2, BA, BW, 2); SB(); acc = MFMA(Wf[0], A[0], acc); SB();
            P35_LD(3, BA, BW, 3); SB(); acc = MFMA(Wf[1], A[1], acc); SB();
            cbar();
            P5_ADV(); BW += 16384;
            P35_LD(0, BA, BW, 0); SB(); acc = MFMA(Wf[2], A[2], acc); SB();
            P35_LD(1, BA, BW, 1); SB(); acc = MFMA(Wf[3], A[3], acc); SB();
        }
        P35_LD(2, BA, BW, 2); SB(); acc = MFMA(Wf[0], A[0], acc); SB();
        P35_LD(3, BA, BW, 3); SB(); acc = MFMA(Wf[1], A[1], acc); SB();
        cbar();
        P5_ADV();
        P5_LDA(0, 0); SB(); acc = MFMA(Wf[2], A[2], acc); SB();
        P5_LDA(1, 1); SB(); acc = MFMA(Wf[3], A[3], acc); SB();
#pragma unroll
        for (int c = 0; c < 4; ++c) {
            P5_LDA(2, 2); SB(); acc = MFMA(WR[4 * c], A[0], acc); SB();
            P5_LDA(3, 3); SB(); acc = MFMA(WR[4 * c + 1], A[1], acc); SB();
            cbar();
            P5_ADV();
            if (c < 3) { P5_LDA(0, 0); }
            SB(); acc = MFMA(WR[4 * c + 2], A[2], acc); SB();
            if (c < 3) { P5_LDA(1, 1); }
            SB(); acc = MFMA(WR[4 * c + 3], A[3], acc); SB();
        }
#pragma unroll
        for (int j = 0; j < 3; ++j) acc = MFMA(WR[16 + j], lds_b128(aro + BA - kh * 4096 + (kh * 3 + j) * 1024), acc);
    }
    {
        LAS f32x4* xw = (LAS f32x4*)(smem + XA + w * 2048 + lane * 16);
        f32x4 s0, s1;
#pragma unroll
        for (int e = 0; e < 4; ++e) { s0[e] = kh ? acc[e] : acc[8 + e]; s1[e] = kh ? acc[4 + e] : acc[12 + e]; }
        xw[0] = s0; xw[64] = s1;
    }
    cbar();
    float v[8];
    {
        LAS const f32x4* xr = (LAS const f32x4*)(smem + XA + (w ^ 4) * 2048 + lane * 16);
        const f32x4 s0 = xr[0], s1 = xr[64];
        if constexpr (MODE == 0) {
            const int row = ta * 32 + r;
#pragma unroll
            for (int g = 0; g < 2; ++g)
                res[g] = *(LAS const u32x2*)(smem + P3_X + row * 128 + (((cj * 4 + kh * 2 + g) ^ ((row >> 1) & 7)) << 4) + hh * 8);
        } else { res[0][0] = hkeep[0]; res[0][1] = hkeep[1]; res[1][0] = hkeep[2]; res[1][1] = hkeep[3]; }
#pragma unroll
        for (int e = 0; e < 4; ++e) { v[e] = (kh ? acc[8 + e] : acc[e]) + s0[e]; v[4 + e] = (kh ? acc[12 + e] : acc[4 + e]) + s1[e]; }
#pragma unroll
        for (int g = 0; g < 2; ++g) {
            v[4 * g] += bflo(res[g][0]); v[4 * g + 1] += bfhi(res[g][0]); v[4 * g + 2] += bflo(res[g][1]); v[4 * g + 3] += bfhi(res[g][1]);
        }
    }
    const unsigned p0 = cvtpk(v[0], v[1]), p1 = cvtpk(v[2], v[3]), p2 = cvtpk(v[4], v[5]), p3 = cvtpk(v[6], v[7]);
    const u32x4 o = swap4(p0, p1, p2, p3);
    if constexpr (MODE == 0) {
        hkeep[0] = p0; hkeep[1] = p1; hkeep[2] = p2; hkeep[3] = p3;
        gst128(ws + WS_HF + ((size_t)tile * 32 + (n0 >> 4)) * 1024, lane * 16, o);
        float s1 = 0.f, s2 = 0.f;
#pragma unroll
        for (int e = 0; e < 4; ++e) { s1 = dot2(o[e], ONES, s1); s2 = dot2(o[e], o[e], s2); }
        s1 = sum_halves(s1); s2 = sum_halves(s2);
        LAS f32x2* sl = (LAS f32x2*)(smem + XA + 8 * 2048);
        if (lane < 32) { f32x2 sv = {s1, s2}; sl[(ta * 32 + r) * 4 + cj * 2 + kh] = sv; }
        cbar();
        if (w == 0) {
            const f32x4 p = *(LAS const f32x4*)(sl + lane * 4), q = *(LAS const f32x4*)(sl + lane * 4 + 2);
            u32x2 sv = {__float_as_uint(p[0] + p[2] + q[0] + q[2]), __float_as_uint(p[1] + p[3] + q[1] + q[3])};
            gst64(ws + WS_ST + (size_t)tb * 64 * 64 + cb * 8, lane * 64, sv);
        }
    } else {
        *(u32x4*)(P.out + (size_t)token * 1024 + (n0 + 8 * hh) * 2) = o;
    }
}

constexpr int P4_ST = 147456, P4_CS = P4_ST + 8192;
template <int NB>
DI void p4_compute(char* ws, char* smem, int tb, int cg, int w, int lane) {
    const int r = lane & 31, hh = lane >> 5, i = w & 3, j0 = NB == 2 ? 0 : 2;
    const unsigned lds0 = (unsigned)(uintptr_t)smem;
    const int tile = tb * 4 + i;
    const unsigned hro = lds0 + 98304 + i * 4096 + lane * 16;
    const unsigned wro = lds0 + hh * 1536 + ((lane & 15) >> 2) * 64 + ((lane >> 4) & 1) * 32 + (lane & 3) * 8 + j0 * 256;
    f32x16 ag[NB], au[NB];
#pragma unroll
    for (int j = 0; j < NB; ++j) { ag[j] = f32x16{}; au[j] = f32x16{}; }
    bf16x8 H[4], G[4][NB], U[4][NB];
#define P4_LOAD(S, BH, BW, ks)                                                                                                    \
    {                                                                                                                             \
        H[S] = lds_b128(hro + (BH) + (ks) * 1024);                                                                                 \
        _Pragma("unroll") for (int j = 0; j < NB; ++j) { G[S][j] = lds_trfrag(wro + (BW) + (ks) * 3072 + j * 256, 768); U[S][j] = lds_trfrag(wro + (BW) + 12288 + (ks) * 3072 + j * 256, 768); } \
    }
#define P4_MMA(S) { _Pragma("unroll") for (int j = 0; j < NB; ++j) { ag[j] = MFMA(G[S][j], H[S], ag[j]); au[j] = MFMA(U[S][j], H[S], au[j]); } }
    cbar();
    P4_LOAD(0, 0, 0, 0); P4_LOAD(1, 0, 0, 1);
    unsigned BH = 0, BW = 0;
#pragma unroll 1
    for (int ch = 0; ch < 8; ++ch) {
        P4_LOAD(2, BH, BW, 2); SB(); P4_MMA(0); SB();
        P4_LOAD(3, BH, BW, 3); SB(); P4_MMA(1); SB();
        cbar();
        if (ch < 7) {
            BH = BH == 2 * 16384 ? 0 : BH + 16384; BW = BW == 3 * 24576 ? 0 : BW + 24576;
            P4_LOAD(0, BH, BW, 0);
        }
        SB(); P4_MMA(2); SB();
        if (ch < 7) { P4_LOAD(1, BH, BW, 1); }
        SB(); P4_MMA(3); SB();
    }
    float mean, rstd;
    {
        float s1 = 0.f, s2 = 0.f;
        const LAS f32x4* sp = (const LAS f32x4*)(smem + P4_ST + (i * 32 + r) * 64);
#pragma unroll
        for (int k = 0; k < 4; ++k) { const f32x4 v = sp[k]; s1 += v[0] + v[2]; s2 += v[1] + v[3]; }
        mean = s1 * (1.f / DM);
        rstd = __builtin_amdgcn_rsqf(fmaxf(s2 * (1.f / DM) - mean * mean, 0.f) + LN_EPS);
    }
    constexpr float C1 = -2.f * 0.7978845608028654f * LOG2E, C2 = C1 * 0.044715f;
    const float k1 = C1 * rstd, k2 = C2 * rstd * rstd * rstd, k3 = __builtin_amdgcn_rcpf(rstd * rstd);
    const f32x2 nm2 = {-mean, -mean}, k12 = {k1, k1}, k22 = {k2, k2}, k32 = {k3, k3};
#pragma unroll
    for (int j = 0; j < NB; ++j) {
        const int blk = cg * 3 + j0 + j;
        float v[16];
#pragma unroll
        for (int g = 0; g < 4; ++g) {
            const LAS f32x4* cp = (const LAS f32x4*)(smem + P4_CS + ((j0 + j) * 32 + 8 * g + 4 * hh) * 4);
            const f32x4 cgf = cp[0], cuf = cp[32];
#pragma unroll
            for (int e = 0; e < 4; e += 2) {
                const f32x2 ga = {ag[j][4 * g + e], ag[j][4 * g + e + 1]}, ua = {au[j][4 * g + e], au[j][4 * g + e + 1]};
                const f32x2 cg2 = {cgf[e], cgf[e + 1]}, cu2 = {cuf[e], cuf[e + 1]};
                const f32x2 a = __builtin_elementwise_fma(nm2, cg2, ga), bq = __builtin_elementwise_fma(nm2, cu2, ua);
                const f32x2 arg = a * __builtin_elementwise_fma(a * a, k22, k12);
                const f32x2 ex = {__builtin_amdgcn_exp2f(arg[0]), __builtin_amdgcn_exp2f(arg[1])};
                const f32x2 den = __builtin_elementwise_fma(ex, k32, k32);
                const f32x2 rc = {__builtin_amdgcn_rcpf(den[0]), __builtin_amdgcn_rcpf(den[1])};
                const f32x2 y = (a * rc) * bq;
                v[4 * g + e] = y[0]; v[4 * g + e + 1] = y[1];
            }
        }
        if (blk < 43) {
#pragma unroll
            for (int g = 0; g < 2; ++g) {
                const u32x4 o = pack_swap(v[8 * g], v[8 * g + 1], v[8 * g + 2], v[8 * g + 3], v[8 * g + 4], v[8 * g + 5], v[8 * g + 6], v[8 * g + 7]);
                gst128(ws + WS_AF + ((size_t)tile * 86 + blk * 2 + g) * 1024, lane * 16, o);
            }
        }
    }
}

__global__ __launch_bounds__(640, 3) void k_block(const char* __restrict__ x, const char* __restrict__ wqkv, const char* __restrict__ wo, const char* __restrict__ wg,
                                                  const char* __restrict__ wu, const char* __restrict__ wd, char* __restrict__ ws, char* __restrict__ out) {
    extern __shared__ __attribute__((aligned(16))) char smem[];
    const Ptrs P = {x, wqkv, wo, wg, wu, wd, ws, out};
    const int b = blockIdx.x, xcd = b & 7, idx = b >> 3;
    const int lane = threadIdx.x & 63, w = rfl(threadIdx.x >> 6);
    const unsigned lds0 = (unsigned)(uintptr_t)smem;
    if (w >= 8 && blockIdx.x < 64) {
        const char* pc = (const char*)(__builtin_amdgcn_s_getpc() & ~127ull);
        asm volatile("s_mov_b32 m0, %0\n\ts_nop 0" ::"s"(lds0 + LDS_BYTES - 256) : "memory");
#pragma unroll
        for (int i = 0; i < (CODE_LINES + 63) / 64; ++i) { const int ln = i * 64 + lane; gtouch(pc, (ln < CODE_LINES ? ln : CODE_LINES - 1) * 128); }
    }
    const bool p1_gemm = idx >= 8;
    const int gi = idx - 8;
    const int tb1 = (xcd >> 1) * 4 + (gi & 3), cb1 = (xcd & 1) * 6 + (gi >> 2);
    const int head = xcd * 4 + (idx >> 3), qb = idx & 7;
    const int tb3 = xcd * 4 + (idx & 3), cb3 = idx >> 2;
    const bool p4_on = !(xcd & 1) || idx < 28;
    const int tb4 = (xcd >> 1) * 4 + (idx & 3), cg4 = (xcd & 1) * 8 + (idx >> 2);

    if (w < 8) {
        if (p1_gemm) {
            f32x16 accA, accB;
            if (w >= 4) p1_colsum(wqkv, ws, tb1, cb1, smem, w - 4, lane, accA, accB);
            else if (cb1 < 8) p1_compute<false>(ws, tb1, cb1, smem, w, lane, accA, accB); else p1_compute<true>(ws, tb1, cb1, smem, w, lane, accA, accB);
            const int tl = 2 * (w & 1) + (w >> 2), cp = (w >> 1) & 1;
            if (cb1 < 8) p1_epilogue<false>(ws, tb1, cb1, smem, tl, cp, accA, accB, lane); else p1_epilogue<true>(ws, tb1, cb1, smem, tl, cp, accA, accB, lane);
        } else p1_helper(x, wqkv, wg, wu, wo, wd, lds0 + LDS_BYTES - 512, ws, idx * 8 + xcd, w, lane);
        pbar();
        p2_compute(ws, smem, head, qb, w, lane);
        pbar();
        u32x4 hkeep;
        p35_compute<0>(P, smem, tb3, cb3, w, lane, hkeep);
        pbar();
        if (p4_on) {
            if (w < 4) p4_compute<2>(ws, smem, tb4, cg4, w, lane); else p4_compute<1>(ws, smem, tb4, cg4, w, lane);
        } else bar();
        pbar();
        p35_compute<1>(P, smem, tb3, cb3, w, lane, hkeep);
        return;
    }

    const int L = w - 8;
    const char* flags = ws + WS_FLAG;
    const unsigned lin = lane * 16;
    if (!p1_gemm && L == 0) small_touch(ws, b, lane, lds0);
    const unsigned tag = p1_gemm ? p1_load(P, L, tb1, cb1, lds0, lane) : rfl(gld32_wait(ws + WS_EPOCH, 0)) + 1;
    bar();
    if (L == 0 && lane == 0) gst32(ws + WS_FLAG, b * 128, tag);
    {
        const int ptb = lane < 32 ? (lane & 15) : 2 * qb + (lane & 1), pcb = (lane < 16 ? 4 : lane < 32 ? 8 : 0) + (head >> 3);
        const int pb = ((pcb % 6) * 4 + (ptb & 3) + 8) * 8 + (ptb >> 2) * 2 + pcb / 6;
        poll(flags, pb * 128, lane < 34, tag);
        const char* src = ws + (L == 0 ? WS_KF : WS_VF) + (size_t)head * 65536;
        const unsigned dst = lds0 + L * 65536;
        {
            const char* q = ws + WS_QF + ((size_t)head * 64 + qb * 8) * 1024 + L * 4096;
            dma4lin(q, lin, lds0 + P2_X + L * 4096);
            if (L == 0) dma1dw(ws + WS_KM + head * 256, lane * 4, lds0 + P2_KM);
        }
        dma4lin(src, lin, dst); dma4lin(src + 32768, lin, dst + 32768);
        dma2lin(src + 4096, lin, dst + 4096); dma2lin(src + 32768 + 4096, lin, dst + 32768 + 4096);
        lbar<0>();
        dma2lin(src + 6144, lin, dst + 6144); dma2lin(src + 32768 + 6144, lin, dst + 32768 + 6144);
#pragma unroll 1
        for (int g = 2; g < 8; ++g) {
            dma4lin(src + g * 4096, lin, dst + g * 4096); dma4lin(src + 32768 + g * 4096, lin, dst + 32768 + g * 4096);
        }
        lbar<0>();
        if (L == 1) {
#pragma unroll 1
            for (int g = 0; g < 4; ++g) poll(flags, (g * 64 + lane) * 128, true, tag);
            if (b == 0 && lane == 0) gst32(ws + WS_EPOCH, 0, tag);
            asm volatile("s_mov_b32 m0, %0\n\ts_nop 0" ::"s"(lds0 + LDS_BYTES - 256) : "memory");
            gtouch(wo, (idx * 128 + lane) * 128); gtouch(wo, (idx * 128 + 64 + lane) * 128);
            {
                const int nl = (xcd & 1) ? 10 : 12, ln = lane & 15;
                const unsigned co = (xcd & 1) * 1536 + (ln < nl ? ln : 0) * 128;
#pragma unroll
                for (int i = 0; i < 4; ++i) {
                    unsigned o_ = (idx * 16 + i * 4 + (lane >> 4)) * (FF * 2) + co;
                    o_ = o_ > 512u * FF * 2 - 4 ? 512u * FF * 2 - 4 : o_;
                    gtouch(wg, o_); gtouch(wu, o_);
                }
            }
#pragma unroll
            for (int i = 0; i < 6; ++i) { int ln = i * 64 + lane; ln = ln > 343 ? 343 : ln; gtouch(wd, (idx * 344 + ln) * 128); }
        }
        bar();
    }
    unsigned wvo[4];
    {
        const unsigned wv = ((lane >> 5) * 4 + ((lane >> 2) & 3)) * 1024 + ((lane >> 4) & 1) * 64 + (lane & 3) * 16;
#pragma unroll
        for (int i = 0; i < 4; ++i) wvo[i] = wv + i * 8192 - i * 1024;
    }
    {
        bar();
        if (L == 1) {
            {
                const int rl = lane >> 3, cs = lane & 7;
                unsigned xv[4];
#pragma unroll
                for (int i = 0; i < 4; ++i) xv[i] = (8 * i + rl) * 1024 + ((cs ^ ((4 * (i & 1) + (rl >> 1)) & 7)) << 4) - i * 1024;
                const char* xs = x + (size_t)tb3 * 64 * 1024 + cb3 * 128;
                dma4(xs, xv[0], xv[1], xv[2], xv[3], lds0 + P3_X); dma4(xs + 32768, xv[0], xv[1], xv[2], xv[3], lds0 + P3_X + 4096);
            }
#pragma unroll 1
            for (int c = 0; c < 4; ++c) {
                const char* src = wo + cb3 * 128 + (size_t)c * 128 * 1024;
#pragma unroll
                for (int g = 0; g < 4; ++g) dma4(src + g * 32768, wvo[0], wvo[1], wvo[2], wvo[3], lds0 + c * 16384 + g * 4096);
            }
        }
        if (L == 0) {
            if (lane == 0) gst32(ws + WS_FLAG + 256 * 128, b * 128, tag);
            const int ph = lane & 31;
            poll(flags + 256 * 128, (((ph & 3) * 8 + (tb3 >> 2)) * 8 + (ph >> 2)) * 128, true, tag);
            const char* ab = ws + WS_OF + (size_t)tb3 * 2 * 32 * 1024;
#pragma unroll 1
            for (int c = 0; c < 4; ++c) {
                const char* src = ab + c * 8192;
                const unsigned dst = lds0 + 65536 + c * 16384;
                dma4lin(src, lin, dst); dma4lin(src + 4096, lin, dst + 4096); dma4lin(src + 32768, lin, dst + 8192); dma4lin(src + 32768 + 4096, lin, dst + 12288);
                if (c == 1) lbar<16>();
            }
            lbar<32>(); lbar<16>(); lbar<0>();
        } else {
            lbar<0>(); bar(); bar(); bar();
        }
        bar();
    }
    {
        unsigned gvo[12];
        if (L == 1 && p4_on) {
#pragma unroll
            for (int j = 0; j < 12; ++j) {
                const int s = j * 64 + lane, st = s >> 4, kb = st / 3, nb = st % 3, q = (s >> 2) & 3, c = s & 3;
                int blk = cg4 * 3 + nb; blk = blk > 42 ? 42 : blk;
                gvo[j] = (4 * kb + q) * (FF * 2) + blk * 64 + c * 16 - (j & 3) * 1024;
            }
        }
        auto issue_w = [&](int c, unsigned B) {
            const size_t so = (size_t)c * 64 * (FF * 2);
#pragma unroll
            for (int g = 0; g < 3; ++g) dma4(wg + so, gvo[4 * g], gvo[4 * g + 1], gvo[4 * g + 2], gvo[4 * g + 3], lds0 + B + g * 4096);
#pragma unroll
            for (int g = 0; g < 3; ++g) dma4(wu + so, gvo[4 * g], gvo[4 * g + 1], gvo[4 * g + 2], gvo[4 * g + 3], lds0 + B + 12288 + g * 4096);
        };
        bar();
        bar();
        if (L == 1 && p4_on) { issue_w(0, 0); issue_w(1, 24576); issue_w(2, 2 * 24576); issue_w(3, 3 * 24576); }
        if (L == 0) { if (lane == 0) gst32(ws + WS_FLAG + 2 * 256 * 128, b * 128, tag); }
        if (p4_on) {
            if (L == 0) {
                const int ptb = tb4 * 2 + (lane & 1), pcb = (lane >> 1) & 7;
                poll(flags + 2 * 256 * 128, ((pcb * 4 + (ptb & 3)) * 8 + (ptb >> 2)) * 128, true, tag);
                const char* hb = ws + WS_HF + (size_t)tb4 * 4 * 32 * 1024;
                auto issue_h = [&](int c, unsigned B) {
                    const char* src = hb + c * 4096;
#pragma unroll
                    for (int i = 0; i < 4; ++i) dma4lin(src + i * 32768, lin, lds0 + 98304 + B + i * 4096);
                };
                issue_h(0, 0); issue_h(1, 16384);
                lbar<16>();
                {
                    const char* sb = ws + WS_ST + (size_t)tb4 * 128 * 64;
                    dma4lin(sb, lin, lds0 + P4_ST); dma4lin(sb + 4096, lin, lds0 + P4_ST + 4096);
                    const char* cb_ = ws + WS_CS + cg4 * 384;
                    dma1dw(cb_, lane * 4, lds0 + P4_CS); dma1dw(cb_ + 256, lane * 4, lds0 + P4_CS + 256);
                    dma1dw(cb_ + FF * 4, lane * 4, lds0 + P4_CS + 512); dma1dw(cb_ + FF * 4 + 256, lane * 4, lds0 + P4_CS + 768);
                }
                issue_h(2, 2 * 16384);
                unsigned B = 0;
#pragma unroll 1
                for (int c = 1; c < 8; ++c) {
                    if (c < 7) lbar<16>(); else lbar<0>();
                    if (c + 2 < 8) { issue_h(c + 2, B); B = B == 2 * 16384 ? 0 : B + 16384; }
                }
            } else {
                unsigned B = 0;
#pragma unroll 1
                for (int c = 0; c < 8; ++c) {
                    if (c == 0) lbar<63>(); else if (c < 6) lbar<48>(); else if (c == 6) lbar<24>(); else lbar<0>();
                    if (c >= 1 && c + 3 < 8) { issue_w(c + 3, B); B = B == 3 * 24576 ? 0 : B + 24576; }
                }
            }
            bar();
        } else bar();
    }
    {
        auto issue_w = [&](int c, unsigned B) {
            const char* src = wd + cb3 * 128 + (size_t)c * 128 * 1024;
#pragma unroll
            for (int g = 0; g < 3; ++g) dma4(src + g * 32768, wvo[0], wvo[1], wvo[2], wvo[3], lds0 + B + g * 4096);
            dma4(src + (c < 10 ? 3 : 2) * 32768, wvo[0], wvo[1], wvo[2], wvo[3], lds0 + B + 3 * 4096);
        };
        if (L == 1) {
#pragma unroll 1
            for (int c = 6; c < 11; ++c) issue_w(c, (c - 6) * 16384);
        }
        bar();
        if (L == 0 && p4_on && lane == 0) gst32(ws + WS_FLAG + 3 * 256 * 128, b * 128, tag);
        if (L == 1) lbar<0>(); else bar();
        bar();
        if (L == 1) {
#pragma unroll 1
            for (int c = 0; c < 6; ++c) issue_w(c, c * 16384);
        }
        if (L == 0) {
            const int ptb = tb3 >> 1, pcg = lane < 15 ? lane : 0;
            poll(flags + 3 * 256 * 128, (((pcg & 7) * 4 + (ptb & 3)) * 8 + (ptb >> 2) * 2 + (pcg >> 3)) * 128, true, tag);
            const char* ab = ws + WS_AF + (size_t)tb3 * 2 * 86 * 1024;
            auto issue_a = [&](int c, unsigned B) {
                const char* src = ab + c * 8192;
                const unsigned dst = lds0 + 98304 + B;
                const int o2 = c < 10 ? 4096 : 2048;
                dma4lin(src, lin, dst); dma4lin(src + o2, lin, dst + o2);
                dma4lin(src + 86 * 1024, lin, dst + 8192); dma4lin(src + 86 * 1024 + o2, lin, dst + 8192 + o2);
            };
            issue_a(0, 0); issue_a(1, 16384);
            unsigned B = 2 * 16384;
#pragma unroll 1
            for (int c = 0; c < 11; ++c) {
                if (c < 10) lbar<16>(); else lbar<0>();
                if (c + 2 < 11) { issue_a(c + 2, B); B = B == 2 * 16384 ? 0 : B + 16384; }
            }
        } else {
            lbar<63>(); lbar<63>(); lbar<48>(); lbar<32>(); lbar<16>(); lbar<0>();
            bar(); bar(); bar(); bar(); bar();
        }
        bar();
    }
}

extern "C" void kernel_launch(void* const* d_in, const int* in_sizes, int n_in, void* d_out, int out_size, void* d_ws, size_t ws_size, hipStream_t stream) {
    (void)hipFuncSetAttribute((const void*)k_block, hipFuncAttributeMaxDynamicSharedMemorySize, LDS_BYTES);
    k_block<<<256, 640, LDS_BYTES, stream>>>((const char*)d_in[0], (const char*)d_in[1], (const char*)d_in[2], (const char*)d_in[4], (const char*)d_in[5], (const char*)d_in[6],
                                             (char*)d_ws, (char*)d_out);
}
```
